```python
import torch

def _softmax(x, axis=-1):
    return torch.softmax(x, dim=axis)

def _gelu(x):
    return torch.nn.functional.gelu(x, approximate='tanh')

def _silu(x):
    return torch.nn.functional.silu(x)

def _sigmoid(x):
    return torch.sigmoid(x)

def _layernorm(x, axis=-1, eps=1e-05):
    m = x.mean(dim=axis, keepdim=True)
    v = x.var(dim=axis, keepdim=True, unbiased=False)
    return (x - m) / torch.sqrt(v + eps)

def _scatter_add(tgt, idx, src, axis=0):
    return tgt.clone().index_add_(axis, idx, src)

def _segment_sum(src, seg, n):
    out = torch.zeros((n,) + src.shape[1:], dtype=src.dtype)
    return out.index_add_(0, seg, src)

def _pad(x, pw):
    flat = tuple((v for pair in reversed(pw) for v in pair))
    return torch.nn.functional.pad(x, flat)
kernel_name = 'adaLN_cond__mha_block_cached__glu_gated_geglu__prompt'

def setup_inputs():

    _w = lambda a: (lambda t: t.to(torch.bfloat16) if t.is_floating_point() else t)(torch.as_tensor(a))

    def _A_setup():
        B, Sq, D = (1, 2048, 448)
        H, Sk, Dh = (16, 2048, 256)
        return {'x': _w((lambda *s: torch.randn(*s, dtype=torch.bfloat16) * 0.05)(B, Sq, D)), 'k_cache': _w((lambda *s: torch.randn(*s, dtype=torch.bfloat16) * 0.8)(B, H, Sk, Dh)), 'v_cache': _w((lambda *s: torch.randn(*s, dtype=torch.bfloat16) * 3.2)(B, H, Sk, Dh)), 'wq': _w((lambda *s: torch.randn(*s, dtype=torch.bfloat16) * 0.8)(D, H * Dh) * D ** (-0.5)), 'wo': _w((lambda *s: torch.randn(*s, dtype=torch.bfloat16) * 3.2)(H * Dh, D) * (H * Dh) ** (-0.5))}
    _A_in = _A_setup()

    def _B_setup():
        B, S, Dm, Df = (1, 2048, 448, 1792)
        return {'x': _w((lambda *s: torch.randn(*s, dtype=torch.bfloat16) * 0.05)(B, S, Dm)), 'w_gate': _w((lambda *s: torch.randn(*s, dtype=torch.bfloat16) * 3.2)(Dm, Df) * Dm ** (-0.5)), 'w_up': _w((lambda *s: torch.randn(*s, dtype=torch.bfloat16) * 3.2)(Dm, Df) * Dm ** (-0.5)), 'w_down': _w((lambda *s: torch.randn(*s, dtype=torch.bfloat16) * 3.2)(Df, Dm) * Df ** (-0.5))}
    _B_in = _B_setup()
    return {'A_x': _A_in['x'], 'A_k_cache': _A_in['k_cache'], 'A_v_cache': _A_in['v_cache'], 'A_wq': _A_in['wq'], 'A_wo': _A_in['wo'], 'B_x': _B_in['x'], 'B_w_gate': _B_in['w_gate'], 'B_w_up': _B_in['w_up'], 'B_w_down': _B_in['w_down'], 'c': _w((lambda *s: torch.randn(*s, dtype=torch.bfloat16) * 0.4)(1, 448) * 8.0), 'w_mod': _w((lambda *s: torch.randn(*s, dtype=torch.bfloat16) * 0.4)(448, 6 * 448) * 448 ** (-0.5) * 8.0)}

def reference(A_x, A_k_cache, A_v_cache, A_wq, A_wo, B_x, B_w_gate, B_w_up, B_w_down, c, w_mod):

    def _A_ref(x, k_cache, v_cache, wq, wo):
        B, Sq, D = x.shape
        H, Dh = (16, 256)
        q = (lambda x, *d: x.permute(*d))((x @ wq).reshape(B, Sq, H, Dh), 0, 2, 1, 3)
        scores = torch.einsum('bhqd,bhkd->bhqk', q, k_cache) * Dh ** (-0.5)
        p = _softmax(scores, axis=-1)
        o = torch.einsum('bhqk,bhkd->bhqd', p, v_cache)
        return (lambda x, *d: x.permute(*d))(o, 0, 2, 1, 3).reshape(B, Sq, H * Dh) @ wo

    def _B_ref(x, w_gate, w_up, w_down):
        g = torch.einsum('bsd,df->bsf', x, w_gate)
        u = torch.einsum('bsd,df->bsf', x, w_up)
        h = _gelu(g) * u
        return torch.einsum('bsf,fd->bsd', h, w_down)
    x = A_x
    mod = (_silu(c) @ w_mod).reshape(c.shape[0], 6, 1, -1)
    sa, ca, ga, sb, cb, gb = (mod[:, 0].clone(), mod[:, 1].clone(), mod[:, 2].clone(), mod[:, 3].clone(), mod[:, 4].clone(), mod[:, 5].clone())
    h = _layernorm(x, axis=-1) * (1 + ca) + sa
    x = x + ga * _A_ref(h, A_k_cache, A_v_cache, A_wq, A_wo)
    h = _layernorm(x, axis=-1) * (1 + cb) + sb
    x = x + gb * _B_ref(h, B_w_gate, B_w_up, B_w_down)
    return x
```

```cpp
#include <hip/hip_runtime.h>
#include <hip/hip_bf16.h>
typedef unsigned short u16;
__device__ __forceinline__ float b2f(u16 x){ return __uint_as_float(((unsigned)x)<<16); }
__device__ __forceinline__ u16 f2b(float f){ unsigned u=__float_as_uint(f); u += 0x7fffu + ((u>>16)&1u); return (u16)(u>>16); }
constexpr int S=2048, D=448, H=16, DH=256, F=1792;

__global__ void k_mod(const u16* c, const u16* w, float* mod){
  int n = blockIdx.x*64+threadIdx.x; float a=0;
  for(int k=0;k<D;k++){ float v=b2f(c[k]); v = v/(1.f+__expf(-v)); a += v*b2f(w[k*6*D+n]); }
  mod[n]=a;
}
__global__ void k_ln(const float* xf, const u16* xb, const float* sh, const float* sc, float* h){
  int r=blockIdx.x; __shared__ float red[64]; float v[7]; float s=0;
  for(int i=0;i<7;i++){ int c=threadIdx.x+64*i; v[i]= xf? xf[r*D+c] : b2f(xb[r*D+c]); s+=v[i]; }
  for(int o=32;o;o>>=1) s+=__shfl_xor(s,o); float m=s/D; float q=0;
  for(int i=0;i<7;i++){ q+=(v[i]-m)*(v[i]-m);} for(int o=32;o;o>>=1) q+=__shfl_xor(q,o);
  float rs=rsqrtf(q/D+1e-5f);
  for(int i=0;i<7;i++){ int c=threadIdx.x+64*i; h[r*D+c]=(v[i]-m)*rs*(1.f+sc[c])+sh[c]; }
}
template<int MODE> __global__ void k_gemm(const float* A, const u16* B, float* C, int M,int N,int K){
  __shared__ float As[16][65], Bs[16][65];
  int tx=threadIdx.x&15, ty=threadIdx.x>>4; int m0=blockIdx.y*64, n0=blockIdx.x*64; float acc[4][4]={};
  for(int k0=0;k0<K;k0+=16){
    for(int i=threadIdx.x;i<1024;i+=256){ int r=i>>4,c=i&15; As[c][r]=A[(size_t)(m0+r)*K+k0+c]; }
    for(int i=threadIdx.x;i<1024;i+=256){ int r=i>>6,c=i&63; Bs[r][c]=b2f(B[(size_t)(k0+r)*N+n0+c]); }
    __syncthreads();
    for(int k=0;k<16;k++){ for(int i=0;i<4;i++)for(int j=0;j<4;j++) acc[i][j]+=As[k][ty*4+i]*Bs[k][tx*4+j]; }
    __syncthreads();
  }
  for(int i=0;i<4;i++)for(int j=0;j<4;j++) C[(size_t)(m0+ty*4+i)*N+n0+tx*4+j]=acc[i][j];
}
__global__ void k_attn(const float* q, const u16* Kc, const u16* Vc, float* o){
  int h=blockIdx.y, r0=blockIdx.x*8; __shared__ float qs[8][DH]; __shared__ float sc[8][S]; __shared__ float red[8][4];
  int t=threadIdx.x;
  for(int i=t;i<8*DH;i+=256) qs[i/DH][i%DH]=q[(size_t)(r0+i/DH)*H*DH+h*DH+i%DH];
  __syncthreads();
  for(int k=t;k<S;k+=256){ float a[8]={}; const u16* kp=Kc+((size_t)h*S+k)*DH;
    for(int d=0;d<DH;d++){ float kv=b2f(kp[d]); for(int i=0;i<8;i++) a[i]+=qs[i][d]*kv; }
    for(int i=0;i<8;i++) sc[i][k]=a[i]*0.0625f; }
  __syncthreads();
  int w=t>>6,l=t&63;
  for(int i=w*2;i<w*2+2;i++){ float m=-1e30f; for(int k=l;k<S;k+=64) m=fmaxf(m,sc[i][k]); for(int o2=32;o2;o2>>=1) m=fmaxf(m,__shfl_xor(m,o2));
    float s=0; for(int k=l;k<S;k+=64){ float e=__expf(sc[i][k]-m); sc[i][k]=e; s+=e;} for(int o2=32;o2;o2>>=1) s+=__shfl_xor(s,o2);
    if(l==0) red[i][0]=1.f/s; }
  __syncthreads();
  float a[8]={}; for(int k=0;k<S;k++){ float v=b2f(Vc[((size_t)h*S+k)*DH+t]); for(int i=0;i<8;i++) a[i]+=sc[i][k]*v; }
  for(int i=0;i<8;i++) o[(size_t)(r0+i)*H*DH+h*DH+t]=a[i]*red[i][0];
}
__global__ void k_res(const u16* xb, const float* a, const float* g, float* x2){ int i=blockIdx.x*256+threadIdx.x; x2[i]=b2f(xb[i])+g[i%D]*a[i]; }
__global__ void k_act(const float* g, const float* u, float* a){ int i=blockIdx.x*256+threadIdx.x; float x=g[i]; float t=tanhf(0.7978845608f*(x+0.044715f*x*x*x)); a[i]=0.5f*x*(1.f+t)*u[i]; }
__global__ void k_out(const float* x2, const float* a, const float* g, u16* o){ int i=blockIdx.x*256+threadIdx.x; o[i]=f2b(x2[i]+g[i%D]*a[i]); }

extern "C" void kernel_launch(void* const* d_in, const int* in_sizes, int n_in, void* d_out, int out_size, void* d_ws, size_t ws_size, hipStream_t stream){
  const u16 *x=(const u16*)d_in[0],*kc=(const u16*)d_in[1],*vc=(const u16*)d_in[2],*wq=(const u16*)d_in[3],*wo=(const u16*)d_in[4];
  const u16 *wg=(const u16*)d_in[6],*wu=(const u16*)d_in[7],*wd=(const u16*)d_in[8],*c=(const u16*)d_in[9],*wm=(const u16*)d_in[10];
  float* ws=(float*)d_ws; float* mod=ws; float* h=mod+4096; float* q=h+S*D; float* o=q+S*H*DH; float* at=o+S*H*DH; float* x2=at+S*D; float* g=x2+S*D; float* u=g+S*F; float* ac=u+S*F;
  k_mod<<<6*D/64,64,0,stream>>>(c,wm,mod);
  k_ln<<<S,64,0,stream>>>(nullptr,x,mod,mod+D,h);
  k_gemm<0><<<dim3(H*DH/64,S/64),256,0,stream>>>(h,wq,q,S,H*DH,D);
  k_attn<<<dim3(S/8,H),256,0,stream>>>(q,kc,vc,o);
  k_gemm<0><<<dim3(D/64,S/64),256,0,stream>>>(o,wo,at,S,D,H*DH);
  k_res<<<S*D/256,256,0,stream>>>(x,at,mod+2*D,x2);
  k_ln<<<S,64,0,stream>>>(x2,nullptr,mod+3*D,mod+4*D,h);
  k_gemm<0><<<dim3(F/64,S/64),256,0,stream>>>(h,wg,g,S,F,D);
  k_gemm<0><<<dim3(F/64,S/64),256,0,stream>>>(h,wu,u,S,F,D);
  k_act<<<S*F/256,256,0,stream>>>(g,u,ac);
  k_gemm<0><<<dim3(D/64,S/64),256,0,stream>>>(ac,wd,at,S,D,F);
  k_out<<<S*D/256,256,0,stream>>>(x2,at,mod+5*D,(u16*)d_out);
}
```

```cpp
#include <hip/hip_runtime.h>

__global__ __launch_bounds__(256, 1) void k_block(const char* x, const char* kc, const char* vc, const char* wq, const char* wo, const char* wg, const char* wu,
                                                  const char* wd, const char* c, const char* wm, char* out, char* ws) {
  const unsigned w = __builtin_amdgcn_readfirstlane(threadIdx.x >> 6);
  asm volatile(
    "s_load_dwordx16 s[4:19], %0, 0x0\n"
    "s_load_dwordx8 s[20:27], %0, 0x40\n"
    "v_mbcnt_lo_u32_b32 v0, -1, 0\n"
    "v_mbcnt_hi_u32_b32 v0, -1, v0\n"
    "v_lshlrev_b32 v254, 4, v0\n"
    "s_mov_b32 s72, %2\n"
    "s_getpc_b64 s[84:85]\n"
    "CBEG_%=:\n"
    "s_mov_b32 s86, CEND_%=-CBEG_%=\n"
    "s_mov_b32 s87, 0x20000\n"
    "s_lshr_b32 s70, %1, 3\n"
    "s_lshl_b32 s70, s70, 2\n"
    "s_add_u32 s70, s70, s72\n"
    "s_mov_b32 m0, 98304\n"
    "s_lshl_b32 s70, s70, 10\n"
    "buffer_load_dword v254, s[84:87], s70 offen lds\n"
    "s_lshr_b32 s30, %1, 4\n"
    "s_mov_b32 s82, 1835008\n"
    "s_mov_b32 s83, 0x20000\n"
    "s_mov_b32 s90, 896\n"
    "s_mov_b32 s91, 0x20000\n"
    "s_mov_b32 s78, 2408448\n"
    "s_mov_b32 s79, 0x20000\n"
    "s_mul_i32 s44, %1, 7168\n"
    "s_mul_i32 s69, s72, 1792\n"
    "s_add_u32 s44, s44, s69\n"
    "s_lshl_b32 s45, s72, 8\n"
    "v_lshlrev_b32 v4, 2, v0\n"
    "s_and_b32 s49, %1, 15\n"
    "s_lshr_b32 s50, 0x9000, s49\n"
    "s_and_b32 s50, s50, 1\n"
    "s_mul_i32 s50, s50, 3\n"
    "s_xor_b32 s49, s49, s50\n"
    "s_lshl_b32 s50, s30, 1\n"
    "s_add_u32 s50, s50, s49\n"
    "s_cmp_lt_u32 s49, 14\n"
    "s_cselect_b32 s51, s49, s50\n"
    "s_min_u32 s70, s51, 41\n"
    "s_lshl_b32 s70, s70, 7\n"
    "s_mul_i32 s71, s72, 602112\n"
    "s_add_u32 s68, s70, s71\n"
    "s_mul_i32 s48, s72, 14336\n"
    "v_lshrrev_b32 v1, 3, v0\n"
    "v_mul_u32_u24 v2, 5376, v1\n"
    "v_mul_u32_u24 v6, 896, v1\n"
    "v_bfe_u32 v3, v0, 1, 2\n"
    "v_lshrrev_b32 v1, 1, v1\n"
    "v_xor_b32 v3, v3, v1\n"
    "v_lshl_add_u32 v2, v3, 5, v2\n"
    "v_and_b32 v3, 1, v0\n"
    "v_lshl_add_u32 v2, v3, 4, v2\n"
    "s_and_b32 s40, %1, 7\n"
    "s_lshl_b32 s40, s40, 1\n"
    "s_bfe_u32 s41, %1, 0x10003\n"
    "s_add_u32 s40, s40, s41\n"
    "s_and_b32 s41, s72, 1\n"
    "s_lshl_b32 s41, s41, 1\n"
    "s_lshr_b32 s42, s72, 1\n"
    "s_add_u32 s41, s41, s42\n"
    "s_lshl_b32 s42, s30, 2\n"
    "s_add_u32 s41, s41, s42\n"
    "s_mul_i32 s42, s41, 458752\n"
    "s_mul_i32 s43, s40, 7168\n"
    "s_add_u32 s42, s42, s43\n"
    "s_add_u32 s42, s42, 11743232\n"
    "s_waitcnt lgkmcnt(0)\n"
    "s_load_dwordx2 s[74:75], s[26:27], 0\n"
    "s_mov_b64 s[80:81], s[4:5]\n"
    "s_mov_b64 s[88:89], s[20:21]\n"
    "s_mov_b64 s[76:77], s[22:23]\n"
    "buffer_load_dword v254, s[80:83], s44 offen lds\n"
    "buffer_load_dword v254, s[80:83], s44 offen offset:768 lds\n"
    "buffer_load_dword v5, v4, s[88:91], s45 offen\n"
    "v_add_u32 v4, s45, v4\n"
    "s_lshl_b32 s71, s30, 2\n"
    "s_add_u32 s71, s71, s72\n"
    "s_mul_i32 s71, s71, 37632\n"
    "s_add_u32 s71, s71, s70\n"
    "buffer_load_dword v2, s[76:79], s71 offen lds\n"
    "s_mov_b32 s64, s48\n"
    "s_mov_b32 s65, s68\n"
    "s_add_u32 m0, s64, 106496\n"
    "s_nop 0\n"
    "buffer_load_dwordx4 v2, s[76:79], s65 offen lds\n"
    "s_add_u32 m0, s64, 107520\n"
    "s_add_u32 s65, s65, 43008\n"
    "buffer_load_dwordx4 v2, s[76:79], s65 offen lds\n"
    "s_add_u32 m0, s64, 108544\n"
    "s_add_u32 s65, s65, 43008\n"
    "buffer_load_dwordx4 v2, s[76:79], s65 offen lds\n"
    "s_add_u32 m0, s64, 109568\n"
    "s_add_u32 s65, s65, 43008\n"
    "buffer_load_dwordx4 v2, s[76:79], s65 offen lds\n"
    "s_add_u32 m0, s64, 110592\n"
    "s_add_u32 s65, s65, 43008\n"
    "buffer_load_dwordx4 v2, s[76:79], s65 offen lds\n"
    "s_add_u32 m0, s64, 111616\n"
    "s_add_u32 s65, s65, 43008\n"
    "buffer_load_dwordx4 v2, s[76:79], s65 offen lds\n"
    "s_add_u32 m0, s64, 112640\n"
    "s_add_u32 s65, s65, 43008\n"
    "buffer_load_dwordx4 v2, s[76:79], s65 offen lds\n"
    "s_add_u32 m0, s64, 113664\n"
    "s_add_u32 s65, s65, 43008\n"
    "buffer_load_dwordx4 v2, s[76:79], s65 offen lds\n"
    "s_add_u32 m0, s64, 114688\n"
    "s_add_u32 s65, s65, 43008\n"
    "buffer_load_dwordx4 v2, s[76:79], s65 offen lds\n"
    "s_add_u32 m0, s64, 115712\n"
    "s_add_u32 s65, s65, 43008\n"
    "buffer_load_dwordx4 v2, s[76:79], s65 offen lds\n"
    "s_add_u32 m0, s64, 116736\n"
    "s_add_u32 s65, s65, 43008\n"
    "buffer_load_dwordx4 v2, s[76:79], s65 offen lds\n"
    "s_add_u32 m0, s64, 117760\n"
    "s_add_u32 s65, s65, 43008\n"
    "buffer_load_dwordx4 v2, s[76:79], s65 offen lds\n"
    "s_add_u32 m0, s64, 118784\n"
    "s_add_u32 s65, s65, 43008\n"
    "buffer_load_dwordx4 v2, s[76:79], s65 offen lds\n"
    "s_add_u32 m0, s64, 119808\n"
    "s_add_u32 s65, s65, 43008\n"
    "buffer_load_dwordx4 v2, s[76:79], s65 offen lds\n"
    "v_and_b32 v3, 7, v0\n"
    "v_xor_b32 v3, v3, v1\n"
    "v_lshl_add_u32 v7, v3, 4, v6\n"
    "v_xor_b32 v3, 4, v3\n"
    "v_lshl_add_u32 v6, v3, 4, v6\n"
    "v_add_u32 v6, 7168, v6\n"
    "s_mul_i32 s69, s41, 28672\n"
    "s_add_u32 s68, s69, 14336\n"
    "s_mul_i32 s48, s72, 24576\n"
    "s_add_u32 m0, s48, 0\n"
    "s_nop 0\n"
    "buffer_load_dwordx4 v7, s[80:83], s69 offen offset:0 lds\n"
    "s_add_u32 m0, s48, 1024\n"
    "s_nop 0\n"
    "buffer_load_dwordx4 v6, s[80:83], s69 offen offset:0 lds\n"
    "s_add_u32 m0, s48, 2048\n"
    "s_nop 0\n"
    "buffer_load_dwordx4 v7, s[80:83], s68 offen offset:0 lds\n"
    "s_add_u32 m0, s48, 3072\n"
    "s_nop 0\n"
    "buffer_load_dwordx4 v6, s[80:83], s68 offen offset:0 lds\n"
    "v_lshrrev_b32 v3, 4, v0\n"
    "v_lshrrev_b32 v8, 2, v0\n"
    "v_lshlrev_b32 v8, 7, v8\n"
    "v_and_b32 v9, 3, v0\n"
    "v_lshl_add_u32 v8, v9, 3, v8\n"
    "v_bfe_u32 v9, v0, 3, 2\n"
    "v_xor_b32 v9, s72, v9\n"
    "v_lshl_add_u32 v8, v9, 5, v8\n"
    "v_add_u32 v8, 106496, v8\n"
    "v_lshlrev_b32 v9, 3, v3\n"
    "v_add_u32 v9, 101376, v9\n"
    "v_and_b32 v1, 15, v0\n"
    "v_lshlrev_b32 v10, 7, v1\n"
    "v_add_u32 v10, s48, v10\n"
    "v_lshrrev_b32 v1, 1, v1\n"
    "v_xor_b32 v1, v1, v3\n"
    "v_lshl_add_u32 v11, v1, 4, v10\n"
    "v_xor_b32 v1, 4, v1\n"
    "v_lshl_add_u32 v10, v1, 4, v10\n"
    "v_add_u32 v4, 101376, v4\n"
    "v_lshrrev_b32 v1, 5, v0\n"
    "v_and_b32 v2, 31, v0\n"
    "v_lshlrev_b32 v2, 4, v2\n"
    "v_lshl_add_u32 v255, v1, 15, v2\n"
    "v_lshl_add_u32 v253, v1, 13, v2\n"
    "v_lshlrev_b32 v243, 5, v3\n"
    "v_add_u32 v243, 102400, v243\n"
    "s_add_u32 s28, s26, s42\n"
    "s_addc_u32 s29, s27, 0\n"
    "s_lshl_b32 s42, s40, 20\n"
    "s_add_u32 s6, s6, s42\n"
    "s_addc_u32 s7, s7, 0\n"
    "s_add_u32 s8, s8, s42\n"
    "s_addc_u32 s9, s9, 0\n"
    "s_lshl_b32 s42, s40, 9\n"
    "s_add_u32 s10, s10, s42\n"
    "s_addc_u32 s11, s11, 0\n"
    "s_mul_i32 s42, s40, 229376\n"
    "s_add_u32 s12, s12, s42\n"
    "s_addc_u32 s13, s13, 0\n"
    "s_mov_b64 s[56:57], s[10:11]\n"
    "s_mov_b32 s58, 3662336\n"
    "s_mov_b32 s59, 0x20000\n"
    "s_mov_b64 s[84:85], s[26:27]\n"
    "s_mov_b32 s86, 41103360\n"
    "s_mov_b32 s87, 0x20000\n"
    "s_mov_b64 s[40:41], s[6:7]\n"
    "s_mov_b32 s42, 1048576\n"
    "s_mov_b32 s43, 0x20000\n"
    "s_mov_b64 s[44:45], s[8:9]\n"
    "s_mov_b32 s46, 1048576\n"
    "s_mov_b32 s47, 0x20000\n"
    "s_lshl_b32 s70, s30, 12\n"
    "s_add_u32 s70, s70, 12288\n"
    "s_lshl_b32 s71, s51, 8\n"
    "s_cmp_lt_u32 s49, 14\n"
    "s_cselect_b32 s88, s70, 512\n"
    "s_add_u32 s88, s88, s71\n"
    "s_lshl_b32 s71, s72, 6\n"
    "s_add_u32 s88, s88, s71\n"
    "s_cmp_gt_u32 s51, 41\n"
    "s_cselect_b32 s88, 2.0, s88\n"
    "s_mul_i32 s71, s72, 896\n"
    "s_add_u32 s73, s70, s71\n"
    "s_lshl_b32 s49, s72, 13\n"
    "s_add_u32 s50, s49, 4096\n"
    "s_mov_b32 s51, s49\n"
    "s_mov_b32 s52, s50\n"
    "s_mov_b32 s54, 0x3e800000\n"
    "s_lshl_b32 s53, s30, 2\n"
    "s_add_u32 s53, s53, s72\n"
    "s_lshl_b32 s53, s53, 10\n"
    "s_add_u32 s53, s53, 131072\n"
    "v_and_b32 v1, 15, v0\n"
    "v_mul_u32_u24 v1, 1056, v1\n"
    "v_lshrrev_b32 v2, 4, v0\n"
    "v_lshl_add_u32 v252, v2, 4, v1\n"
    "v_lshrrev_b32 v1, 2, v0\n"
    "v_mul_u32_u24 v1, 1056, v1\n"
    "v_and_b32 v2, 3, v0\n"
    "v_lshl_add_u32 v240, v2, 3, v1\n"
    "v_add_u32 v241, 67584, v240\n"
    "v_add_u32 v242, 130048, v240\n"
    "s_lshr_b32 s70, s72, 1\n"
    "s_mul_i32 s70, s70, 16896\n"
    "v_add_u32 v252, s70, v252\n"
    "s_lshl_b32 s70, s72, 10\n"
    "s_add_u32 s70, s70, 67584\n"
    "s_xor_b32 s71, s72, 2\n"
    "s_lshl_b32 s71, s71, 10\n"
    "s_add_u32 s71, s71, 67584\n"
    "v_add_u32 v248, s70, v254\n"
    "v_add_u32 v249, s71, v254\n"
    "v_and_b32 v1, 15, v0\n"
    "v_lshlrev_b32 v1, 4, v1\n"
    "v_lshrrev_b32 v2, 5, v0\n"
    "v_lshl_add_u32 v1, v2, 8, v1\n"
    "v_bfe_u32 v2, v0, 4, 1\n"
    "v_lshl_add_u32 v1, v2, 3, v1\n"
    "v_add_u32 v250, s70, v1\n"
    "s_mov_b32 s55, s53\n"
    "s_mov_b32 s53, 0xffff0000\n"
    "v_and_b32 v1, 1, v0\n"
    "v_cmp_eq_u32 s[64:65], 1, v1\n"
    "v_and_b32 v1, 2, v0\n"
    "v_cmp_eq_u32 s[66:67], 2, v1\n"
    "v_bfe_u32 v1, v0, 2, 2\n"
    "v_cmp_eq_u32 s[90:91], v1, v3\n"
    "s_waitcnt vmcnt(19)\n"
    "v_lshlrev_b32 v12, 16, v5\n"
    "v_and_b32 v13, 0xffff0000, v5\n"
    "v_mul_f32 v14, 0xbfb8aa3b, v12\n"
    "v_mul_f32 v15, 0xbfb8aa3b, v13\n"
    "v_exp_f32 v14, v14\n"
    "v_exp_f32 v15, v15\n"
    "s_nop 0\n"
    "v_add_f32 v14, 1.0, v14\n"
    "v_add_f32 v15, 1.0, v15\n"
    "v_rcp_f32 v14, v14\n"
    "v_rcp_f32 v15, v15\n"
    "s_nop 0\n"
    "v_mul_f32 v12, v12, v14\n"
    "v_mul_f32 v13, v13, v15\n"
    "v_cvt_pk_bf16_f32 v12, v12, v13\n"
    "ds_write_b32 v4, v12\n"
    "s_waitcnt vmcnt(4) lgkmcnt(0)\n"
    "s_barrier\n"
    "s_add_u32 s74, s74, 0x7f4a7c15\n"
    "s_addc_u32 s75, s75, 0x9e3779b9\n"
    "s_add_u32 m0, s48, 3968\n"
    "ds_read2_b64 v[16:19], v9 offset0:0 offset1:4\n"
    "ds_read_b64_tr_b16 v[72:73], v8 offset:0\n"
    "ds_read_b64_tr_b16 v[74:75], v8 offset:2048\n"
    "buffer_load_dwordx4 v7, s[80:83], s69 offen offset:128 lds\n"
    "s_add_u32 m0, s48, 4992\n"
    "ds_read2_b64 v[20:23], v9 offset0:8 offset1:12\n"
    "ds_read_b64_tr_b16 v[76:77], v8 offset:4096\n"
    "ds_read_b64_tr_b16 v[78:79], v8 offset:6144\n"
    "buffer_load_dwordx4 v6, s[80:83], s69 offen offset:128 lds\n"
    "s_add_u32 m0, s48, 6016\n"
    "ds_read2_b64 v[24:27], v9 offset0:16 offset1:20\n"
    "ds_read_b64_tr_b16 v[80:81], v8 offset:8192\n"
    "ds_read_b64_tr_b16 v[82:83], v8 offset:10240\n"
    "buffer_load_dwordx4 v7, s[80:83], s68 offen offset:128 lds\n"
    "s_add_u32 m0, s48, 7040\n"
    "ds_read2_b64 v[28:31], v9 offset0:24 offset1:28\n"
    "ds_read_b64_tr_b16 v[84:85], v8 offset:12288\n"
    "ds_read_b64_tr_b16 v[86:87], v8 offset:14336\n"
    "buffer_load_dwordx4 v6, s[80:83], s68 offen offset:128 lds\n"
    "s_add_u32 m0, s48, 7936\n"
    "ds_read2_b64 v[32:35], v9 offset0:32 offset1:36\n"
    "ds_read_b64_tr_b16 v[88:89], v8 offset:16384\n"
    "ds_read_b64_tr_b16 v[90:91], v8 offset:18432\n"
    "buffer_load_dwordx4 v7, s[80:83], s69 offen offset:256 lds\n"
    "s_waitcnt lgkmcnt(12)\n"
    "v_mfma_f32_16x16x32_bf16 v[12:15], v[72:75], v[16:19], 0\n"
    "s_add_u32 m0, s48, 8960\n"
    "ds_read2_b64 v[36:39], v9 offset0:40 offset1:44\n"
    "ds_read_b64_tr_b16 v[92:93], v8 offset:20480\n"
    "ds_read_b64_tr_b16 v[94:95], v8 offset:22528\n"
    "buffer_load_dwordx4 v6, s[80:83], s69 offen offset:256 lds\n"
    "s_waitcnt lgkmcnt(12)\n"
    "v_mfma_f32_16x16x32_bf16 v[12:15], v[76:79], v[20:23], v[12:15]\n"
    "s_add_u32 m0, s48, 9984\n"
    "ds_read2_b64 v[40:43], v9 offset0:48 offset1:52\n"
    "ds_read_b64_tr_b16 v[96:97], v8 offset:24576\n"
    "ds_read_b64_tr_b16 v[98:99], v8 offset:26624\n"
    "buffer_load_dwordx4 v7, s[80:83], s68 offen offset:256 lds\n"
    "s_waitcnt lgkmcnt(12)\n"
    "v_mfma_f32_16x16x32_bf16 v[12:15], v[80:83], v[24:27], v[12:15]\n"
    "s_add_u32 m0, s48, 11008\n"
    "ds_read2_b64 v[44:47], v9 offset0:56 offset1:60\n"
    "ds_read_b64_tr_b16 v[100:101], v8 offset:28672\n"
    "ds_read_b64_tr_b16 v[102:103], v8 offset:30720\n"
    "buffer_load_dwordx4 v6, s[80:83], s68 offen offset:256 lds\n"
    "s_waitcnt lgkmcnt(12)\n"
    "v_mfma_f32_16x16x32_bf16 v[12:15], v[84:87], v[28:31], v[12:15]\n"
    "s_add_u32 m0, s48, 11904\n"
    "ds_read2_b64 v[48:51], v9 offset0:64 offset1:68\n"
    "ds_read_b64_tr_b16 v[104:105], v8 offset:32768\n"
    "ds_read_b64_tr_b16 v[106:107], v8 offset:34816\n"
    "buffer_load_dwordx4 v7, s[80:83], s69 offen offset:384 lds\n"
    "s_waitcnt lgkmcnt(12)\n"
    "v_mfma_f32_16x16x32_bf16 v[12:15], v[88:91], v[32:35], v[12:15]\n"
    "s_add_u32 m0, s48, 12928\n"
    "ds_read2_b64 v[52:55], v9 offset0:72 offset1:76\n"
    "ds_read_b64_tr_b16 v[108:109], v8 offset:36864\n"
    "ds_read_b64_tr_b16 v[110:111], v8 offset:38912\n"
    "buffer_load_dwordx4 v6, s[80:83], s69 offen offset:384 lds\n"
    "s_waitcnt lgkmcnt(12)\n"
    "v_mfma_f32_16x16x32_bf16 v[12:15], v[92:95], v[36:39], v[12:15]\n"
    "s_add_u32 m0, s48, 13952\n"
    "ds_read2_b64 v[56:59], v9 offset0:80 offset1:84\n"
    "ds_read_b64_tr_b16 v[112:113], v8 offset:40960\n"
    "ds_read_b64_tr_b16 v[114:115], v8 offset:43008\n"
    "buffer_load_dwordx4 v7, s[80:83], s68 offen offset:384 lds\n"
    "s_waitcnt lgkmcnt(12)\n"
    "v_mfma_f32_16x16x32_bf16 v[12:15], v[96:99], v[40:43], v[12:15]\n"
    "s_add_u32 m0, s48, 14976\n"
    "ds_read2_b64 v[60:63], v9 offset0:88 offset1:92\n"
    "ds_read_b64_tr_b16 v[116:117], v8 offset:45056\n"
    "ds_read_b64_tr_b16 v[118:119], v8 offset:47104\n"
    "buffer_load_dwordx4 v6, s[80:83], s68 offen offset:384 lds\n"
    "s_waitcnt lgkmcnt(12)\n"
    "v_mfma_f32_16x16x32_bf16 v[12:15], v[100:103], v[44:47], v[12:15]\n"
    "ds_read2_b64 v[64:67], v9 offset0:96 offset1:100\n"
    "ds_read_b64_tr_b16 v[120:121], v8 offset:49152\n"
    "ds_read_b64_tr_b16 v[122:123], v8 offset:51200\n"
    "s_waitcnt lgkmcnt(12)\n"
    "v_mfma_f32_16x16x32_bf16 v[12:15], v[104:107], v[48:51], v[12:15]\n"
    "ds_read2_b64 v[68:71], v9 offset0:104 offset1:108\n"
    "ds_read_b64_tr_b16 v[124:125], v8 offset:53248\n"
    "ds_read_b64_tr_b16 v[126:127], v8 offset:55296\n"
    "s_waitcnt lgkmcnt(12)\n"
    "v_mfma_f32_16x16x32_bf16 v[12:15], v[108:111], v[52:55], v[12:15]\n"
    "s_waitcnt lgkmcnt(9)\n"
    "v_mfma_f32_16x16x32_bf16 v[12:15], v[112:115], v[56:59], v[12:15]\n"
    "s_waitcnt lgkmcnt(6)\n"
    "v_mfma_f32_16x16x32_bf16 v[12:15], v[116:119], v[60:63], v[12:15]\n"
    "s_waitcnt lgkmcnt(3)\n"
    "v_mfma_f32_16x16x32_bf16 v[12:15], v[120:123], v[64:67], v[12:15]\n"
    "s_waitcnt lgkmcnt(0)\n"
    "v_mfma_f32_16x16x32_bf16 v[12:15], v[124:127], v[68:71], v[12:15]\n"
    "v_mov_b32 v17, s74\n"
    "v_mov_b32 v19, s74\n"
    "v_lshlrev_b32 v4, 4, v3\n"
    "v_mov_b32 v0, 0x3f803f80\n"
    "v_mov_b32 v1, 0x3f803f80\n"
    "v_mov_b32 v2, 0x3f803f80\n"
    "v_mov_b32 v3, 0x3f803f80\n"
    "s_nop 1\n"
    "v_cvt_pk_bf16_f32 v16, v12, v13\n"
    "v_cvt_pk_bf16_f32 v18, v14, v15\n"
    "s_mov_b32 exec_lo, 0x10001\n"
    "s_mov_b32 exec_hi, 0x10001\n"
    "buffer_store_dwordx4 v[16:19], v4, s[84:87], s88 offen sc1\n"
    "s_mov_b64 exec, -1\n"
    "s_waitcnt vmcnt(13)\n"
    "ds_read_b128 v[128:131], v11 offset:0\n"
    "ds_read_b128 v[132:135], v11 offset:2048\n"
    "ds_read_b128 v[136:139], v10 offset:0\n"
    "ds_read_b128 v[140:143], v10 offset:2048\n"
    "s_add_u32 m0, s48, 15872\n"
    "s_nop 0\n"
    "buffer_load_dwordx4 v7, s[80:83], s69 offen offset:512 lds\n"
    "s_add_u32 m0, s48, 16896\n"
    "s_nop 0\n"
    "buffer_load_dwordx4 v6, s[80:83], s69 offen offset:512 lds\n"
    "s_add_u32 m0, s48, 17920\n"
    "s_nop 0\n"
    "buffer_load_dwordx4 v7, s[80:83], s68 offen offset:512 lds\n"
    "s_add_u32 m0, s48, 18944\n"
    "s_nop 0\n"
    "buffer_load_dwordx4 v6, s[80:83], s68 offen offset:512 lds\n"
    "s_add_u32 m0, s48, 19840\n"
    "s_nop 0\n"
    "buffer_load_dwordx4 v7, s[80:83], s69 offen offset:640 lds\n"
    "s_add_u32 m0, s48, 20864\n"
    "s_nop 0\n"
    "buffer_load_dwordx4 v6, s[80:83], s69 offen offset:640 lds\n"
    "s_add_u32 m0, s48, 21888\n"
    "s_nop 0\n"
    "buffer_load_dwordx4 v7, s[80:83], s68 offen offset:640 lds\n"
    "s_add_u32 m0, s48, 22912\n"
    "s_nop 0\n"
    "buffer_load_dwordx4 v6, s[80:83], s68 offen offset:640 lds\n"
    "s_waitcnt lgkmcnt(0)\n"
    "s_add_u32 m0, s48, 0\n"
    "s_add_u32 s89, s69, 768\n"
    "buffer_load_dwordx4 v7, s[80:83], s89 offen lds\n"
    "s_add_u32 m0, s48, 1024\n"
    "s_add_u32 s89, s69, 768\n"
    "buffer_load_dwordx4 v6, s[80:83], s89 offen lds\n"
    "s_add_u32 m0, s48, 2048\n"
    "s_add_u32 s89, s68, 768\n"
    "buffer_load_dwordx4 v7, s[80:83], s89 offen lds\n"
    "s_add_u32 m0, s48, 3072\n"
    "s_add_u32 s89, s68, 768\n"
    "buffer_load_dwordx4 v6, s[80:83], s89 offen lds\n"
    "s_lshl_b32 s70, s30, 2\n"
    "s_add_u32 s70, s70, s72\n"
    "s_mov_b32 m0, 105984\n"
    "s_mul_i32 s71, s70, 57344\n"
    "s_waitcnt vmcnt(21)\n"
    "ds_read_b128 v[144:147], v11 offset:4096\n"
    "ds_read_b128 v[148:151], v11 offset:6144\n"
    "ds_read_b128 v[152:155], v10 offset:4096\n"
    "ds_read_b128 v[156:159], v10 offset:6144\n"
    "v_mfma_f32_16x16x32_bf16 v[16:19], v[0:3], v[128:131], 0\n"
    "v_lshlrev_b32 v32, 16, v128\n"
    "v_and_b32 v33, s53, v128\n"
    "v_lshlrev_b32 v34, 16, v129\n"
    "v_and_b32 v35, s53, v129\n"
    "v_mfma_f32_16x16x32_bf16 v[24:27], v[128:131], v[128:131], 0\n"
    "v_lshlrev_b32 v36, 16, v130\n"
    "v_and_b32 v37, s53, v130\n"
    "v_lshlrev_b32 v38, 16, v131\n"
    "v_and_b32 v39, s53, v131\n"
    "v_mfma_f32_16x16x32_bf16 v[20:23], v[0:3], v[132:135], 0\n"
    "v_lshlrev_b32 v40, 16, v132\n"
    "v_and_b32 v41, s53, v132\n"
    "v_lshlrev_b32 v42, 16, v133\n"
    "v_and_b32 v43, s53, v133\n"
    "v_mfma_f32_16x16x32_bf16 v[28:31], v[132:135], v[132:135], 0\n"
    "v_lshlrev_b32 v44, 16, v134\n"
    "v_and_b32 v45, s53, v134\n"
    "v_lshlrev_b32 v46, 16, v135\n"
    "v_and_b32 v47, s53, v135\n"
    "v_mfma_f32_16x16x32_bf16 v[16:19], v[0:3], v[136:139], v[16:19]\n"
    "v_lshlrev_b32 v48, 16, v136\n"
    "v_and_b32 v49, s53, v136\n"
    "v_lshlrev_b32 v50, 16, v137\n"
    "v_and_b32 v51, s53, v137\n"
    "v_mfma_f32_16x16x32_bf16 v[24:27], v[136:139], v[136:139], v[24:27]\n"
    "v_lshlrev_b32 v52, 16, v138\n"
    "v_and_b32 v53, s53, v138\n"
    "v_lshlrev_b32 v54, 16, v139\n"
    "v_and_b32 v55, s53, v139\n"
    "v_mfma_f32_16x16x32_bf16 v[20:23], v[0:3], v[140:143], v[20:23]\n"
    "v_lshlrev_b32 v56, 16, v140\n"
    "v_and_b32 v57, s53, v140\n"
    "v_lshlrev_b32 v58, 16, v141\n"
    "v_and_b32 v59, s53, v141\n"
    "v_mfma_f32_16x16x32_bf16 v[28:31], v[140:143], v[140:143], v[28:31]\n"
    "v_lshlrev_b32 v60, 16, v142\n"
    "v_and_b32 v61, s53, v142\n"
    "v_lshlrev_b32 v62, 16, v143\n"
    "v_and_b32 v63, s53, v143\n"
    "s_waitcnt vmcnt(17)\n"
    "ds_read_b128 v[160:163], v11 offset:8192\n"
    "ds_read_b128 v[164:167], v11 offset:10240\n"
    "ds_read_b128 v[168:171], v10 offset:8192\n"
    "ds_read_b128 v[172:175], v10 offset:10240\n"
    "s_waitcnt lgkmcnt(4)\n"
    "v_mfma_f32_16x16x32_bf16 v[16:19], v[0:3], v[144:147], v[16:19]\n"
    "v_lshlrev_b32 v64, 16, v144\n"
    "v_and_b32 v65, s53, v144\n"
    "v_lshlrev_b32 v66, 16, v145\n"
    "v_and_b32 v67, s53, v145\n"
    "v_mfma_f32_16x16x32_bf16 v[24:27], v[144:147], v[144:147], v[24:27]\n"
    "v_lshlrev_b32 v68, 16, v146\n"
    "v_and_b32 v69, s53, v146\n"
    "v_lshlrev_b32 v70, 16, v147\n"
    "v_and_b32 v71, s53, v147\n"
    "v_mfma_f32_16x16x32_bf16 v[20:23], v[0:3], v[148:151], v[20:23]\n"
    "v_lshlrev_b32 v72, 16, v148\n"
    "v_and_b32 v73, s53, v148\n"
    "v_lshlrev_b32 v74, 16, v149\n"
    "v_and_b32 v75, s53, v149\n"
    "v_mfma_f32_16x16x32_bf16 v[28:31], v[148:151], v[148:151], v[28:31]\n"
    "v_lshlrev_b32 v76, 16, v150\n"
    "v_and_b32 v77, s53, v150\n"
    "v_lshlrev_b32 v78, 16, v151\n"
    "v_and_b32 v79, s53, v151\n"
    "v_mfma_f32_16x16x32_bf16 v[16:19], v[0:3], v[152:155], v[16:19]\n"
    "v_lshlrev_b32 v80, 16, v152\n"
    "v_and_b32 v81, s53, v152\n"
    "v_lshlrev_b32 v82, 16, v153\n"
    "v_and_b32 v83, s53, v153\n"
    "v_mfma_f32_16x16x32_bf16 v[24:27], v[152:155], v[152:155], v[24:27]\n"
    "v_lshlrev_b32 v84, 16, v154\n"
    "v_and_b32 v85, s53, v154\n"
    "v_lshlrev_b32 v86, 16, v155\n"
    "v_and_b32 v87, s53, v155\n"
    "v_mfma_f32_16x16x32_bf16 v[20:23], v[0:3], v[156:159], v[20:23]\n"
    "v_lshlrev_b32 v88, 16, v156\n"
    "v_and_b32 v89, s53, v156\n"
    "v_lshlrev_b32 v90, 16, v157\n"
    "v_and_b32 v91, s53, v157\n"
    "v_mfma_f32_16x16x32_bf16 v[28:31], v[156:159], v[156:159], v[28:31]\n"
    "v_lshlrev_b32 v92, 16, v158\n"
    "v_and_b32 v93, s53, v158\n"
    "v_lshlrev_b32 v94, 16, v159\n"
    "v_and_b32 v95, s53, v159\n"
    "s_waitcnt vmcnt(13)\n"
    "ds_read_b128 v[176:179], v11 offset:12288\n"
    "ds_read_b128 v[180:183], v11 offset:14336\n"
    "ds_read_b128 v[184:187], v10 offset:12288\n"
    "ds_read_b128 v[188:191], v10 offset:14336\n"
    "s_waitcnt lgkmcnt(4)\n"
    "v_mfma_f32_16x16x32_bf16 v[16:19], v[0:3], v[160:163], v[16:19]\n"
    "v_lshlrev_b32 v96, 16, v160\n"
    "v_and_b32 v97, s53, v160\n"
    "v_lshlrev_b32 v98, 16, v161\n"
    "v_and_b32 v99, s53, v161\n"
    "v_mfma_f32_16x16x32_bf16 v[24:27], v[160:163], v[160:163], v[24:27]\n"
    "v_lshlrev_b32 v100, 16, v162\n"
    "v_and_b32 v101, s53, v162\n"
    "v_lshlrev_b32 v102, 16, v163\n"
    "v_and_b32 v103, s53, v163\n"
    "v_mfma_f32_16x16x32_bf16 v[20:23], v[0:3], v[164:167], v[20:23]\n"
    "v_lshlrev_b32 v104, 16, v164\n"
    "v_and_b32 v105, s53, v164\n"
    "v_lshlrev_b32 v106, 16, v165\n"
    "v_and_b32 v107, s53, v165\n"
    "v_mfma_f32_16x16x32_bf16 v[28:31], v[164:167], v[164:167], v[28:31]\n"
    "v_lshlrev_b32 v108, 16, v166\n"
    "v_and_b32 v109, s53, v166\n"
    "v_lshlrev_b32 v110, 16, v167\n"
    "v_and_b32 v111, s53, v167\n"
    "v_mfma_f32_16x16x32_bf16 v[16:19], v[0:3], v[168:171], v[16:19]\n"
    "v_lshlrev_b32 v112, 16, v168\n"
    "v_and_b32 v113, s53, v168\n"
    "v_lshlrev_b32 v114, 16, v169\n"
    "v_and_b32 v115, s53, v169\n"
    "v_mfma_f32_16x16x32_bf16 v[24:27], v[168:171], v[168:171], v[24:27]\n"
    "v_lshlrev_b32 v116, 16, v170\n"
    "v_and_b32 v117, s53, v170\n"
    "v_lshlrev_b32 v118, 16, v171\n"
    "v_and_b32 v119, s53, v171\n"
    "v_mfma_f32_16x16x32_bf16 v[20:23], v[0:3], v[172:175], v[20:23]\n"
    "v_lshlrev_b32 v120, 16, v172\n"
    "v_and_b32 v121, s53, v172\n"
    "v_lshlrev_b32 v122, 16, v173\n"
    "v_and_b32 v123, s53, v173\n"
    "v_mfma_f32_16x16x32_bf16 v[28:31], v[172:175], v[172:175], v[28:31]\n"
    "v_lshlrev_b32 v124, 16, v174\n"
    "v_and_b32 v125, s53, v174\n"
    "v_lshlrev_b32 v126, 16, v175\n"
    "v_and_b32 v127, s53, v175\n"
    "s_waitcnt vmcnt(8)\n"
    "ds_read_b128 v[192:195], v11 offset:16384\n"
    "ds_read_b128 v[196:199], v11 offset:18432\n"
    "ds_read_b128 v[200:203], v10 offset:16384\n"
    "ds_read_b128 v[204:207], v10 offset:18432\n"
    "s_mov_b32 exec_hi, 0xffffff\n"
    "buffer_load_dwordx4 v[12:15], v254, s[84:87], s73 offen sc1\n"
    "s_mov_b64 exec, -1\n"
    "buffer_load_dword v253, s[56:59], s71 offen lds\n"
    "s_add_u32 s71, s71, 16384\n"
    "buffer_load_dword v253, s[56:59], s71 offen lds\n"
    "s_add_u32 s71, s71, 16384\n"
    "buffer_load_dword v253, s[56:59], s71 offen lds\n"
    "s_add_u32 s71, s71, 16384\n"
    "buffer_load_dword v253, s[56:59], s71 offen lds\n"
    "s_add_u32 s71, s71, 16384\n"
    "s_waitcnt lgkmcnt(4)\n"
    "v_mfma_f32_16x16x32_bf16 v[16:19], v[0:3], v[176:179], v[16:19]\n"
    "v_mfma_f32_16x16x32_bf16 v[24:27], v[176:179], v[176:179], v[24:27]\n"
    "v_mfma_f32_16x16x32_bf16 v[20:23], v[0:3], v[180:183], v[20:23]\n"
    "v_mfma_f32_16x16x32_bf16 v[28:31], v[180:183], v[180:183], v[28:31]\n"
    "v_mfma_f32_16x16x32_bf16 v[16:19], v[0:3], v[184:187], v[16:19]\n"
    "v_mfma_f32_16x16x32_bf16 v[24:27], v[184:187], v[184:187], v[24:27]\n"
    "v_mfma_f32_16x16x32_bf16 v[20:23], v[0:3], v[188:191], v[20:23]\n"
    "v_mfma_f32_16x16x32_bf16 v[28:31], v[188:191], v[188:191], v[28:31]\n"
    "s_waitcnt vmcnt(9)\n"
    "ds_read_b128 v[208:211], v11 offset:20480\n"
    "ds_read_b128 v[212:215], v11 offset:22528\n"
    "ds_read_b128 v[216:219], v10 offset:20480\n"
    "ds_read_b128 v[220:223], v10 offset:22528\n"
    "s_waitcnt lgkmcnt(4)\n"
    "v_mfma_f32_16x16x32_bf16 v[16:19], v[0:3], v[192:195], v[16:19]\n"
    "v_mfma_f32_16x16x32_bf16 v[24:27], v[192:195], v[192:195], v[24:27]\n"
    "v_mfma_f32_16x16x32_bf16 v[20:23], v[0:3], v[196:199], v[20:23]\n"
    "v_mfma_f32_16x16x32_bf16 v[28:31], v[196:199], v[196:199], v[28:31]\n"
    "v_mfma_f32_16x16x32_bf16 v[16:19], v[0:3], v[200:203], v[16:19]\n"
    "v_mfma_f32_16x16x32_bf16 v[24:27], v[200:203], v[200:203], v[24:27]\n"
    "v_mfma_f32_16x16x32_bf16 v[20:23], v[0:3], v[204:207], v[20:23]\n"
    "v_mfma_f32_16x16x32_bf16 v[28:31], v[204:207], v[204:207], v[28:31]\n"
    "s_waitcnt vmcnt(5)\n"
    "ds_read_b128 v[224:227], v11 offset:0\n"
    "ds_read_b128 v[228:231], v11 offset:2048\n"
    "ds_read_b128 v[232:235], v10 offset:0\n"
    "ds_read_b128 v[236:239], v10 offset:2048\n"
    "s_waitcnt lgkmcnt(4)\n"
    "v_mfma_f32_16x16x32_bf16 v[16:19], v[0:3], v[208:211], v[16:19]\n"
    "v_mfma_f32_16x16x32_bf16 v[24:27], v[208:211], v[208:211], v[24:27]\n"
    "v_mfma_f32_16x16x32_bf16 v[20:23], v[0:3], v[212:215], v[20:23]\n"
    "v_mfma_f32_16x16x32_bf16 v[28:31], v[212:215], v[212:215], v[28:31]\n"
    "v_mfma_f32_16x16x32_bf16 v[16:19], v[0:3], v[216:219], v[16:19]\n"
    "v_mfma_f32_16x16x32_bf16 v[24:27], v[216:219], v[216:219], v[24:27]\n"
    "v_mfma_f32_16x16x32_bf16 v[20:23], v[0:3], v[220:223], v[20:23]\n"
    "v_mfma_f32_16x16x32_bf16 v[28:31], v[220:223], v[220:223], v[28:31]\n"
    "s_waitcnt lgkmcnt(0)\n"
    "v_mfma_f32_16x16x32_bf16 v[16:19], v[0:3], v[224:227], v[16:19]\n"
    "v_mfma_f32_16x16x32_bf16 v[24:27], v[224:227], v[224:227], v[24:27]\n"
    "v_mfma_f32_16x16x32_bf16 v[20:23], v[0:3], v[228:231], v[20:23]\n"
    "v_mfma_f32_16x16x32_bf16 v[28:31], v[228:231], v[228:231], v[28:31]\n"
    "v_mfma_f32_16x16x32_bf16 v[16:19], v[0:3], v[232:235], v[16:19]\n"
    "v_mfma_f32_16x16x32_bf16 v[24:27], v[232:235], v[232:235], v[24:27]\n"
    "v_mfma_f32_16x16x32_bf16 v[20:23], v[0:3], v[236:239], v[20:23]\n"
    "v_mfma_f32_16x16x32_bf16 v[28:31], v[236:239], v[236:239], v[28:31]\n"
    "s_nop 7\n"
    "v_cndmask_b32 v24, v24, v25, s[64:65]\n"
    "v_cndmask_b32 v26, v26, v27, s[64:65]\n"
    "v_cndmask_b32 v24, v24, v26, s[66:67]\n"
    "v_cndmask_b32 v24, 0, v24, s[90:91]\n"
    "v_cndmask_b32 v28, v28, v29, s[64:65]\n"
    "v_cndmask_b32 v30, v30, v31, s[64:65]\n"
    "v_cndmask_b32 v28, v28, v30, s[66:67]\n"
    "v_cndmask_b32 v28, 0, v28, s[90:91]\n"
    "v_mov_b32 v25, v24\n"
    "v_mov_b32 v29, v28\n"
    "v_mul_f32 v16, 0x3b124925, v16\n"
    "s_nop 0\n"
    "v_permlane16_swap_b32 v24, v25\n"
    "v_permlane16_swap_b32 v28, v29\n"
    "s_nop 0\n"
    "v_add_f32 v24, v24, v25\n"
    "v_add_f32 v28, v28, v29\n"
    "v_mov_b32 v25, v24\n"
    "v_mov_b32 v29, v28\n"
    "v_mul_f32 v20, 0x3b124925, v20\n"
    "s_nop 0\n"
    "v_permlane32_swap_b32 v24, v25\n"
    "v_permlane32_swap_b32 v28, v29\n"
    "s_nop 0\n"
    "v_add_f32 v24, v24, v25\n"
    "v_add_f32 v28, v28, v29\n"
    "v_mul_f32 v24, 0x3b124925, v24\n"
    "v_mul_f32 v28, 0x3b124925, v28\n"
    "v_fma_f32 v24, -v16, v16, v24\n"
    "v_fma_f32 v28, -v20, v20, v28\n"
    "v_max_f32 v24, 0, v24\n"
    "v_max_f32 v28, 0, v28\n"
    "v_add_f32 v24, 0x3727c5ac, v24\n"
    "v_add_f32 v28, 0x3727c5ac, v28\n"
    "v_rsq_f32 v244, v24\n"
    "v_rsq_f32 v245, v28\n"
    "s_nop 0\n"
    "v_mul_f32 v246, -v16, v244\n"
    "v_mul_f32 v247, -v20, v245\n"
    "v_pk_fma_f32 v[32:33], v[32:33], v[244:245], v[246:247] op_sel:[0,0,0] op_sel_hi:[1,0,0]\n"
    "v_pk_fma_f32 v[34:35], v[34:35], v[244:245], v[246:247] op_sel:[0,0,0] op_sel_hi:[1,0,0]\n"
    "v_pk_fma_f32 v[36:37], v[36:37], v[244:245], v[246:247] op_sel:[0,0,0] op_sel_hi:[1,0,0]\n"
    "v_pk_fma_f32 v[38:39], v[38:39], v[244:245], v[246:247] op_sel:[0,0,0] op_sel_hi:[1,0,0]\n"
    "v_pk_fma_f32 v[40:41], v[40:41], v[244:245], v[246:247] op_sel:[0,1,1] op_sel_hi:[1,1,1]\n"
    "v_pk_fma_f32 v[42:43], v[42:43], v[244:245], v[246:247] op_sel:[0,1,1] op_sel_hi:[1,1,1]\n"
    "v_pk_fma_f32 v[44:45], v[44:45], v[244:245], v[246:247] op_sel:[0,1,1] op_sel_hi:[1,1,1]\n"
    "v_pk_fma_f32 v[46:47], v[46:47], v[244:245], v[246:247] op_sel:[0,1,1] op_sel_hi:[1,1,1]\n"
    "v_pk_fma_f32 v[48:49], v[48:49], v[244:245], v[246:247] op_sel:[0,0,0] op_sel_hi:[1,0,0]\n"
    "v_pk_fma_f32 v[50:51], v[50:51], v[244:245], v[246:247] op_sel:[0,0,0] op_sel_hi:[1,0,0]\n"
    "v_pk_fma_f32 v[52:53], v[52:53], v[244:245], v[246:247] op_sel:[0,0,0] op_sel_hi:[1,0,0]\n"
    "v_pk_fma_f32 v[54:55], v[54:55], v[244:245], v[246:247] op_sel:[0,0,0] op_sel_hi:[1,0,0]\n"
    "v_pk_fma_f32 v[56:57], v[56:57], v[244:245], v[246:247] op_sel:[0,1,1] op_sel_hi:[1,1,1]\n"
    "v_pk_fma_f32 v[58:59], v[58:59], v[244:245], v[246:247] op_sel:[0,1,1] op_sel_hi:[1,1,1]\n"
    "v_pk_fma_f32 v[60:61], v[60:61], v[244:245], v[246:247] op_sel:[0,1,1] op_sel_hi:[1,1,1]\n"
    "v_pk_fma_f32 v[62:63], v[62:63], v[244:245], v[246:247] op_sel:[0,1,1] op_sel_hi:[1,1,1]\n"
    "v_pk_fma_f32 v[64:65], v[64:65], v[244:245], v[246:247] op_sel:[0,0,0] op_sel_hi:[1,0,0]\n"
    "v_pk_fma_f32 v[66:67], v[66:67], v[244:245], v[246:247] op_sel:[0,0,0] op_sel_hi:[1,0,0]\n"
    "v_pk_fma_f32 v[68:69], v[68:69], v[244:245], v[246:247] op_sel:[0,0,0] op_sel_hi:[1,0,0]\n"
    "v_pk_fma_f32 v[70:71], v[70:71], v[244:245], v[246:247] op_sel:[0,0,0] op_sel_hi:[1,0,0]\n"
    "v_pk_fma_f32 v[72:73], v[72:73], v[244:245], v[246:247] op_sel:[0,1,1] op_sel_hi:[1,1,1]\n"
    "v_pk_fma_f32 v[74:75], v[74:75], v[244:245], v[246:247] op_sel:[0,1,1] op_sel_hi:[1,1,1]\n"
    "v_pk_fma_f32 v[76:77], v[76:77], v[244:245], v[246:247] op_sel:[0,1,1] op_sel_hi:[1,1,1]\n"
    "v_pk_fma_f32 v[78:79], v[78:79], v[244:245], v[246:247] op_sel:[0,1,1] op_sel_hi:[1,1,1]\n"
    "v_pk_fma_f32 v[80:81], v[80:81], v[244:245], v[246:247] op_sel:[0,0,0] op_sel_hi:[1,0,0]\n"
    "v_pk_fma_f32 v[82:83], v[82:83], v[244:245], v[246:247] op_sel:[0,0,0] op_sel_hi:[1,0,0]\n"
    "v_pk_fma_f32 v[84:85], v[84:85], v[244:245], v[246:247] op_sel:[0,0,0] op_sel_hi:[1,0,0]\n"
    "v_pk_fma_f32 v[86:87], v[86:87], v[244:245], v[246:247] op_sel:[0,0,0] op_sel_hi:[1,0,0]\n"
    "v_pk_fma_f32 v[88:89], v[88:89], v[244:245], v[246:247] op_sel:[0,1,1] op_sel_hi:[1,1,1]\n"
    "v_pk_fma_f32 v[90:91], v[90:91], v[244:245], v[246:247] op_sel:[0,1,1] op_sel_hi:[1,1,1]\n"
    "v_pk_fma_f32 v[92:93], v[92:93], v[244:245], v[246:247] op_sel:[0,1,1] op_sel_hi:[1,1,1]\n"
    "v_pk_fma_f32 v[94:95], v[94:95], v[244:245], v[246:247] op_sel:[0,1,1] op_sel_hi:[1,1,1]\n"
    "v_pk_fma_f32 v[96:97], v[96:97], v[244:245], v[246:247] op_sel:[0,0,0] op_sel_hi:[1,0,0]\n"
    "v_pk_fma_f32 v[98:99], v[98:99], v[244:245], v[246:247] op_sel:[0,0,0] op_sel_hi:[1,0,0]\n"
    "v_pk_fma_f32 v[100:101], v[100:101], v[244:245], v[246:247] op_sel:[0,0,0] op_sel_hi:[1,0,0]\n"
    "v_pk_fma_f32 v[102:103], v[102:103], v[244:245], v[246:247] op_sel:[0,0,0] op_sel_hi:[1,0,0]\n"
    "v_pk_fma_f32 v[104:105], v[104:105], v[244:245], v[246:247] op_sel:[0,1,1] op_sel_hi:[1,1,1]\n"
    "v_pk_fma_f32 v[106:107], v[106:107], v[244:245], v[246:247] op_sel:[0,1,1] op_sel_hi:[1,1,1]\n"
    "v_pk_fma_f32 v[108:109], v[108:109], v[244:245], v[246:247] op_sel:[0,1,1] op_sel_hi:[1,1,1]\n"
    "v_pk_fma_f32 v[110:111], v[110:111], v[244:245], v[246:247] op_sel:[0,1,1] op_sel_hi:[1,1,1]\n"
    "v_pk_fma_f32 v[112:113], v[112:113], v[244:245], v[246:247] op_sel:[0,0,0] op_sel_hi:[1,0,0]\n"
    "v_pk_fma_f32 v[114:115], v[114:115], v[244:245], v[246:247] op_sel:[0,0,0] op_sel_hi:[1,0,0]\n"
    "v_pk_fma_f32 v[116:117], v[116:117], v[244:245], v[246:247] op_sel:[0,0,0] op_sel_hi:[1,0,0]\n"
    "v_pk_fma_f32 v[118:119], v[118:119], v[244:245], v[246:247] op_sel:[0,0,0] op_sel_hi:[1,0,0]\n"
    "v_pk_fma_f32 v[120:121], v[120:121], v[244:245], v[246:247] op_sel:[0,1,1] op_sel_hi:[1,1,1]\n"
    "v_pk_fma_f32 v[122:123], v[122:123], v[244:245], v[246:247] op_sel:[0,1,1] op_sel_hi:[1,1,1]\n"
    "v_pk_fma_f32 v[124:125], v[124:125], v[244:245], v[246:247] op_sel:[0,1,1] op_sel_hi:[1,1,1]\n"
    "v_pk_fma_f32 v[126:127], v[126:127], v[244:245], v[246:247] op_sel:[0,1,1] op_sel_hi:[1,1,1]\n"
    "s_cmp_ge_u32 s72, 2\n"
    "s_cselect_b32 s70, 1.0, 0\n"
    "s_mov_b32 s71, 1048576\n"
    "s_mov_b32 exec_hi, 0xffffff\n"
    "s_waitcnt vmcnt(4)\n"
    "s_branch PCHK0_%=\n"
    "POLL0_%=:\n"
    "buffer_load_dwordx4 v[12:15], v254, s[84:87], s73 offen sc1\n"
    "s_waitcnt vmcnt(0)\n"
    "PCHK0_%=:\n"
    "s_sub_u32 s71, s71, 1\n"
    "v_xor_b32 v13, s74, v13\n"
    "v_xor_b32 v15, s74, v15\n"
    "v_or_b32 v13, v13, v15\n"
    "v_cmp_ne_u32 vcc, 0, v13\n"
    "s_cbranch_scc1 PDONE0_%=\n"
    "s_cbranch_vccnz POLL0_%=\n"
    "PDONE0_%=:\n"
    "v_and_b32 v13, 0xffff0000, v12\n"
    "v_lshlrev_b32 v12, 16, v12\n"
    "v_and_b32 v15, 0xffff0000, v14\n"
    "v_lshlrev_b32 v14, 16, v14\n"
    "v_add_f32 v12, s70, v12\n"
    "v_add_f32 v13, s70, v13\n"
    "v_add_f32 v14, s70, v14\n"
    "v_add_f32 v15, s70, v15\n"
    "v_cvt_pk_bf16_f32 v12, 0, v12\n"
    "v_cvt_pk_bf16_f32 v13, 0, v13\n"
    "v_cvt_pk_bf16_f32 v14, 0, v14\n"
    "v_cvt_pk_bf16_f32 v15, 0, v15\n"
    "v_mul_f32 v12, 0x3db8aa3b, v12\n"
    "v_mul_f32 v13, 0x3db8aa3b, v13\n"
    "v_mul_f32 v14, 0x3db8aa3b, v14\n"
    "v_mul_f32 v15, 0x3db8aa3b, v15\n"
    "s_mul_i32 s71, s72, 896\n"
    "s_add_u32 s71, s71, 102400\n"
    "v_add_u32 v10, s71, v254\n"
    "ds_write_b128 v10, v[12:15]\n"
    "s_mov_b64 exec, -1\n"
    "s_lshr_b32 s73, s72, 1\n"
    "s_mul_i32 s73, s73, 16896\n"
    "s_mul_i32 s48, s72, 8448\n"
    "s_lshl_b32 s68, s72, 17\n"
    "s_waitcnt lgkmcnt(0)\n"
    "s_barrier\n"
    "ds_read_b128 v[0:3], v243 offset:0\n"
    "ds_read_b128 v[4:7], v243 offset:16\n"
    "ds_read_b128 v[8:11], v243 offset:1792\n"
    "ds_read_b128 v[12:15], v243 offset:1808\n"
    "ds_read_b128 v[16:19], v243 offset:128\n"
    "ds_read_b128 v[20:23], v243 offset:144\n"
    "ds_read_b128 v[24:27], v243 offset:1920\n"
    "ds_read_b128 v[28:31], v243 offset:1936\n"
    "s_sub_u32 s70, s55, 131072\n"
    "s_add_u32 m0, s48, 33792\n"
    "s_nop 0\n"
    "buffer_load_dwordx4 v255, s[56:59], s68 offen lds\n"
    "s_add_u32 m0, s48, 34848\n"
    "s_add_u32 s68, s68, 8192\n"
    "buffer_load_dwordx4 v255, s[56:59], s68 offen lds\n"
    "s_waitcnt lgkmcnt(4)\n"
    "v_pk_fma_f32 v[32:33], v[32:33], v[8:9], v[0:1]\n"
    "v_pk_fma_f32 v[34:35], v[34:35], v[10:11], v[2:3]\n"
    "v_pk_fma_f32 v[36:37], v[36:37], v[12:13], v[4:5]\n"
    "v_pk_fma_f32 v[38:39], v[38:39], v[14:15], v[6:7]\n"
    "s_add_u32 m0, s48, 35904\n"
    "s_add_u32 s68, s68, 8192\n"
    "v_cvt_pk_bf16_f32 v128, v32, v33\n"
    "v_cvt_pk_bf16_f32 v129, v34, v35\n"
    "v_cvt_pk_bf16_f32 v130, v36, v37\n"
    "v_cvt_pk_bf16_f32 v131, v38, v39\n"
    "buffer_load_dwordx4 v255, s[56:59], s68 offen lds\n"
    "v_pk_fma_f32 v[40:41], v[40:41], v[8:9], v[0:1]\n"
    "v_pk_fma_f32 v[42:43], v[42:43], v[10:11], v[2:3]\n"
    "v_pk_fma_f32 v[44:45], v[44:45], v[12:13], v[4:5]\n"
    "v_pk_fma_f32 v[46:47], v[46:47], v[14:15], v[6:7]\n"
    "ds_read_b128 v[0:3], v243 offset:256\n"
    "ds_read_b128 v[4:7], v243 offset:272\n"
    "ds_read_b128 v[8:11], v243 offset:2048\n"
    "ds_read_b128 v[12:15], v243 offset:2064\n"
    "s_add_u32 m0, s48, 36960\n"
    "s_add_u32 s68, s68, 8192\n"
    "v_cvt_pk_bf16_f32 v132, v40, v41\n"
    "v_cvt_pk_bf16_f32 v133, v42, v43\n"
    "v_cvt_pk_bf16_f32 v134, v44, v45\n"
    "v_cvt_pk_bf16_f32 v135, v46, v47\n"
    "buffer_load_dwordx4 v255, s[56:59], s68 offen lds\n"
    "s_waitcnt lgkmcnt(4)\n"
    "v_pk_fma_f32 v[48:49], v[48:49], v[24:25], v[16:17]\n"
    "v_pk_fma_f32 v[50:51], v[50:51], v[26:27], v[18:19]\n"
    "v_pk_fma_f32 v[52:53], v[52:53], v[28:29], v[20:21]\n"
    "v_pk_fma_f32 v[54:55], v[54:55], v[30:31], v[22:23]\n"
    "s_add_u32 m0, s48, 38016\n"
    "s_add_u32 s68, s68, 40960\n"
    "v_cvt_pk_bf16_f32 v136, v48, v49\n"
    "v_cvt_pk_bf16_f32 v137, v50, v51\n"
    "v_cvt_pk_bf16_f32 v138, v52, v53\n"
    "v_cvt_pk_bf16_f32 v139, v54, v55\n"
    "buffer_load_dwordx4 v255, s[56:59], s68 offen lds\n"
    "v_pk_fma_f32 v[56:57], v[56:57], v[24:25], v[16:17]\n"
    "v_pk_fma_f32 v[58:59], v[58:59], v[26:27], v[18:19]\n"
    "v_pk_fma_f32 v[60:61], v[60:61], v[28:29], v[20:21]\n"
    "v_pk_fma_f32 v[62:63], v[62:63], v[30:31], v[22:23]\n"
    "ds_read_b128 v[16:19], v243 offset:384\n"
    "ds_read_b128 v[20:23], v243 offset:400\n"
    "ds_read_b128 v[24:27], v243 offset:2176\n"
    "ds_read_b128 v[28:31], v243 offset:2192\n"
    "s_add_u32 m0, s48, 39072\n"
    "s_add_u32 s68, s68, 8192\n"
    "v_cvt_pk_bf16_f32 v140, v56, v57\n"
    "v_cvt_pk_bf16_f32 v141, v58, v59\n"
    "v_cvt_pk_bf16_f32 v142, v60, v61\n"
    "v_cvt_pk_bf16_f32 v143, v62, v63\n"
    "buffer_load_dwordx4 v255, s[56:59], s68 offen lds\n"
    "s_waitcnt lgkmcnt(4)\n"
    "v_pk_fma_f32 v[64:65], v[64:65], v[8:9], v[0:1]\n"
    "v_pk_fma_f32 v[66:67], v[66:67], v[10:11], v[2:3]\n"
    "v_pk_fma_f32 v[68:69], v[68:69], v[12:13], v[4:5]\n"
    "v_pk_fma_f32 v[70:71], v[70:71], v[14:15], v[6:7]\n"
    "s_add_u32 m0, s48, 40128\n"
    "s_add_u32 s68, s68, 8192\n"
    "v_cvt_pk_bf16_f32 v144, v64, v65\n"
    "v_cvt_pk_bf16_f32 v145, v66, v67\n"
    "v_cvt_pk_bf16_f32 v146, v68, v69\n"
    "v_cvt_pk_bf16_f32 v147, v70, v71\n"
    "buffer_load_dwordx4 v255, s[56:59], s68 offen lds\n"
    "v_pk_fma_f32 v[72:73], v[72:73], v[8:9], v[0:1]\n"
    "v_pk_fma_f32 v[74:75], v[74:75], v[10:11], v[2:3]\n"
    "v_pk_fma_f32 v[76:77], v[76:77], v[12:13], v[4:5]\n"
    "v_pk_fma_f32 v[78:79], v[78:79], v[14:15], v[6:7]\n"
    "ds_read_b128 v[0:3], v243 offset:512\n"
    "ds_read_b128 v[4:7], v243 offset:528\n"
    "ds_read_b128 v[8:11], v243 offset:2304\n"
    "ds_read_b128 v[12:15], v243 offset:2320\n"
    "s_add_u32 m0, s48, 41184\n"
    "s_add_u32 s68, s68, 8192\n"
    "v_cvt_pk_bf16_f32 v148, v72, v73\n"
    "v_cvt_pk_bf16_f32 v149, v74, v75\n"
    "v_cvt_pk_bf16_f32 v150, v76, v77\n"
    "v_cvt_pk_bf16_f32 v151, v78, v79\n"
    "buffer_load_dwordx4 v255, s[56:59], s68 offen lds\n"
    "s_waitcnt lgkmcnt(4)\n"
    "v_pk_fma_f32 v[80:81], v[80:81], v[24:25], v[16:17]\n"
    "v_pk_fma_f32 v[82:83], v[82:83], v[26:27], v[18:19]\n"
    "v_pk_fma_f32 v[84:85], v[84:85], v[28:29], v[20:21]\n"
    "v_pk_fma_f32 v[86:87], v[86:87], v[30:31], v[22:23]\n"
    "s_add_u32 m0, s48, 67584\n"
    "s_add_u32 s68, s68, 434176\n"
    "v_cvt_pk_bf16_f32 v152, v80, v81\n"
    "v_cvt_pk_bf16_f32 v153, v82, v83\n"
    "v_cvt_pk_bf16_f32 v154, v84, v85\n"
    "v_cvt_pk_bf16_f32 v155, v86, v87\n"
    "buffer_load_dwordx4 v255, s[56:59], s68 offen lds\n"
    "v_pk_fma_f32 v[88:89], v[88:89], v[24:25], v[16:17]\n"
    "v_pk_fma_f32 v[90:91], v[90:91], v[26:27], v[18:19]\n"
    "v_pk_fma_f32 v[92:93], v[92:93], v[28:29], v[20:21]\n"
    "v_pk_fma_f32 v[94:95], v[94:95], v[30:31], v[22:23]\n"
    "ds_read_b128 v[16:19], v243 offset:640\n"
    "ds_read_b128 v[20:23], v243 offset:656\n"
    "ds_read_b128 v[24:27], v243 offset:2432\n"
    "ds_read_b128 v[28:31], v243 offset:2448\n"
    "s_add_u32 m0, s48, 68640\n"
    "s_add_u32 s68, s68, 8192\n"
    "v_cvt_pk_bf16_f32 v156, v88, v89\n"
    "v_cvt_pk_bf16_f32 v157, v90, v91\n"
    "v_cvt_pk_bf16_f32 v158, v92, v93\n"
    "v_cvt_pk_bf16_f32 v159, v94, v95\n"
    "buffer_load_dwordx4 v255, s[56:59], s68 offen lds\n"
    "s_waitcnt lgkmcnt(4)\n"
    "v_pk_fma_f32 v[96:97], v[96:97], v[8:9], v[0:1]\n"
    "v_pk_fma_f32 v[98:99], v[98:99], v[10:11], v[2:3]\n"
    "v_pk_fma_f32 v[100:101], v[100:101], v[12:13], v[4:5]\n"
    "v_pk_fma_f32 v[102:103], v[102:103], v[14:15], v[6:7]\n"
    "s_add_u32 m0, s48, 69696\n"
    "s_add_u32 s68, s68, 8192\n"
    "v_cvt_pk_bf16_f32 v160, v96, v97\n"
    "v_cvt_pk_bf16_f32 v161, v98, v99\n"
    "v_cvt_pk_bf16_f32 v162, v100, v101\n"
    "v_cvt_pk_bf16_f32 v163, v102, v103\n"
    "buffer_load_dwordx4 v255, s[56:59], s68 offen lds\n"
    "v_pk_fma_f32 v[104:105], v[104:105], v[8:9], v[0:1]\n"
    "v_pk_fma_f32 v[106:107], v[106:107], v[10:11], v[2:3]\n"
    "v_pk_fma_f32 v[108:109], v[108:109], v[12:13], v[4:5]\n"
    "v_pk_fma_f32 v[110:111], v[110:111], v[14:15], v[6:7]\n"
    "ds_read_b128 v[0:3], v243 offset:768\n"
    "ds_read_b128 v[4:7], v243 offset:784\n"
    "ds_read_b128 v[8:11], v243 offset:2560\n"
    "ds_read_b128 v[12:15], v243 offset:2576\n"
    "s_add_u32 m0, s48, 70752\n"
    "s_add_u32 s68, s68, 8192\n"
    "v_cvt_pk_bf16_f32 v164, v104, v105\n"
    "v_cvt_pk_bf16_f32 v165, v106, v107\n"
    "v_cvt_pk_bf16_f32 v166, v108, v109\n"
    "v_cvt_pk_bf16_f32 v167, v110, v111\n"
    "buffer_load_dwordx4 v255, s[56:59], s68 offen lds\n"
    "s_waitcnt lgkmcnt(4)\n"
    "v_pk_fma_f32 v[112:113], v[112:113], v[24:25], v[16:17]\n"
    "v_pk_fma_f32 v[114:115], v[114:115], v[26:27], v[18:19]\n"
    "v_pk_fma_f32 v[116:117], v[116:117], v[28:29], v[20:21]\n"
    "v_pk_fma_f32 v[118:119], v[118:119], v[30:31], v[22:23]\n"
    "s_add_u32 m0, s48, 71808\n"
    "s_add_u32 s68, s68, 40960\n"
    "v_cvt_pk_bf16_f32 v168, v112, v113\n"
    "v_cvt_pk_bf16_f32 v169, v114, v115\n"
    "v_cvt_pk_bf16_f32 v170, v116, v117\n"
    "v_cvt_pk_bf16_f32 v171, v118, v119\n"
    "buffer_load_dwordx4 v255, s[56:59], s68 offen lds\n"
    "v_pk_fma_f32 v[120:121], v[120:121], v[24:25], v[16:17]\n"
    "v_pk_fma_f32 v[122:123], v[122:123], v[26:27], v[18:19]\n"
    "v_pk_fma_f32 v[124:125], v[124:125], v[28:29], v[20:21]\n"
    "v_pk_fma_f32 v[126:127], v[126:127], v[30:31], v[22:23]\n"
    "ds_read_b128 v[16:19], v243 offset:896\n"
    "ds_read_b128 v[20:23], v243 offset:912\n"
    "ds_read_b128 v[24:27], v243 offset:2688\n"
    "ds_read_b128 v[28:31], v243 offset:2704\n"
    "s_add_u32 m0, s48, 72864\n"
    "s_add_u32 s68, s68, 8192\n"
    "v_cvt_pk_bf16_f32 v172, v120, v121\n"
    "v_cvt_pk_bf16_f32 v173, v122, v123\n"
    "v_cvt_pk_bf16_f32 v174, v124, v125\n"
    "v_cvt_pk_bf16_f32 v175, v126, v127\n"
    "buffer_load_dwordx4 v255, s[56:59], s68 offen lds\n"
    "v_lshlrev_b32 v32, 16, v176\n"
    "v_and_b32 v33, s53, v176\n"
    "v_lshlrev_b32 v34, 16, v177\n"
    "v_and_b32 v35, s53, v177\n"
    "v_lshlrev_b32 v36, 16, v178\n"
    "v_and_b32 v37, s53, v178\n"
    "v_lshlrev_b32 v38, 16, v179\n"
    "v_and_b32 v39, s53, v179\n"
    "v_pk_fma_f32 v[32:33], v[32:33], v[244:245], v[246:247] op_sel:[0,0,0] op_sel_hi:[1,0,0]\n"
    "v_pk_fma_f32 v[34:35], v[34:35], v[244:245], v[246:247] op_sel:[0,0,0] op_sel_hi:[1,0,0]\n"
    "v_pk_fma_f32 v[36:37], v[36:37], v[244:245], v[246:247] op_sel:[0,0,0] op_sel_hi:[1,0,0]\n"
    "v_pk_fma_f32 v[38:39], v[38:39], v[244:245], v[246:247] op_sel:[0,0,0] op_sel_hi:[1,0,0]\n"
    "s_waitcnt lgkmcnt(4)\n"
    "v_pk_fma_f32 v[32:33], v[32:33], v[8:9], v[0:1]\n"
    "v_pk_fma_f32 v[34:35], v[34:35], v[10:11], v[2:3]\n"
    "v_pk_fma_f32 v[36:37], v[36:37], v[12:13], v[4:5]\n"
    "v_pk_fma_f32 v[38:39], v[38:39], v[14:15], v[6:7]\n"
    "s_add_u32 m0, s48, 73920\n"
    "s_add_u32 s68, s68, 8192\n"
    "v_cvt_pk_bf16_f32 v176, v32, v33\n"
    "v_cvt_pk_bf16_f32 v177, v34, v35\n"
    "v_cvt_pk_bf16_f32 v178, v36, v37\n"
    "v_cvt_pk_bf16_f32 v179, v38, v39\n"
    "buffer_load_dwordx4 v255, s[56:59], s68 offen lds\n"
    "v_lshlrev_b32 v40, 16, v180\n"
    "v_and_b32 v41, s53, v180\n"
    "v_lshlrev_b32 v42, 16, v181\n"
    "v_and_b32 v43, s53, v181\n"
    "v_lshlrev_b32 v44, 16, v182\n"
    "v_and_b32 v45, s53, v182\n"
    "v_lshlrev_b32 v46, 16, v183\n"
    "v_and_b32 v47, s53, v183\n"
    "v_pk_fma_f32 v[40:41], v[40:41], v[244:245], v[246:247] op_sel:[0,1,1] op_sel_hi:[1,1,1]\n"
    "v_pk_fma_f32 v[42:43], v[42:43], v[244:245], v[246:247] op_sel:[0,1,1] op_sel_hi:[1,1,1]\n"
    "v_pk_fma_f32 v[44:45], v[44:45], v[244:245], v[246:247] op_sel:[0,1,1] op_sel_hi:[1,1,1]\n"
    "v_pk_fma_f32 v[46:47], v[46:47], v[244:245], v[246:247] op_sel:[0,1,1] op_sel_hi:[1,1,1]\n"
    "v_pk_fma_f32 v[40:41], v[40:41], v[8:9], v[0:1]\n"
    "v_pk_fma_f32 v[42:43], v[42:43], v[10:11], v[2:3]\n"
    "v_pk_fma_f32 v[44:45], v[44:45], v[12:13], v[4:5]\n"
    "v_pk_fma_f32 v[46:47], v[46:47], v[14:15], v[6:7]\n"
    "ds_read_b128 v[0:3], v243 offset:1024\n"
    "ds_read_b128 v[4:7], v243 offset:1040\n"
    "ds_read_b128 v[8:11], v243 offset:2816\n"
    "ds_read_b128 v[12:15], v243 offset:2832\n"
    "s_add_u32 m0, s48, 74976\n"
    "s_add_u32 s68, s68, 8192\n"
    "v_cvt_pk_bf16_f32 v180, v40, v41\n"
    "v_cvt_pk_bf16_f32 v181, v42, v43\n"
    "v_cvt_pk_bf16_f32 v182, v44, v45\n"
    "v_cvt_pk_bf16_f32 v183, v46, v47\n"
    "buffer_load_dwordx4 v255, s[56:59], s68 offen lds\n"
    "v_lshlrev_b32 v48, 16, v184\n"
    "v_and_b32 v49, s53, v184\n"
    "v_lshlrev_b32 v50, 16, v185\n"
    "v_and_b32 v51, s53, v185\n"
    "v_lshlrev_b32 v52, 16, v186\n"
    "v_and_b32 v53, s53, v186\n"
    "v_lshlrev_b32 v54, 16, v187\n"
    "v_and_b32 v55, s53, v187\n"
    "v_pk_fma_f32 v[48:49], v[48:49], v[244:245], v[246:247] op_sel:[0,0,0] op_sel_hi:[1,0,0]\n"
    "v_pk_fma_f32 v[50:51], v[50:51], v[244:245], v[246:247] op_sel:[0,0,0] op_sel_hi:[1,0,0]\n"
    "v_pk_fma_f32 v[52:53], v[52:53], v[244:245], v[246:247] op_sel:[0,0,0] op_sel_hi:[1,0,0]\n"
    "v_pk_fma_f32 v[54:55], v[54:55], v[244:245], v[246:247] op_sel:[0,0,0] op_sel_hi:[1,0,0]\n"
    "s_waitcnt lgkmcnt(4)\n"
    "v_pk_fma_f32 v[48:49], v[48:49], v[24:25], v[16:17]\n"
    "v_pk_fma_f32 v[50:51], v[50:51], v[26:27], v[18:19]\n"
    "v_pk_fma_f32 v[52:53], v[52:53], v[28:29], v[20:21]\n"
    "v_pk_fma_f32 v[54:55], v[54:55], v[30:31], v[22:23]\n"
    "v_cvt_pk_bf16_f32 v184, v48, v49\n"
    "v_cvt_pk_bf16_f32 v185, v50, v51\n"
    "v_cvt_pk_bf16_f32 v186, v52, v53\n"
    "v_cvt_pk_bf16_f32 v187, v54, v55\n"
    "v_lshlrev_b32 v56, 16, v188\n"
    "v_and_b32 v57, s53, v188\n"
    "v_lshlrev_b32 v58, 16, v189\n"
    "v_and_b32 v59, s53, v189\n"
    "v_lshlrev_b32 v60, 16, v190\n"
    "v_and_b32 v61, s53, v190\n"
    "v_lshlrev_b32 v62, 16, v191\n"
    "v_and_b32 v63, s53, v191\n"
    "v_pk_fma_f32 v[56:57], v[56:57], v[244:245], v[246:247] op_sel:[0,1,1] op_sel_hi:[1,1,1]\n"
    "v_pk_fma_f32 v[58:59], v[58:59], v[244:245], v[246:247] op_sel:[0,1,1] op_sel_hi:[1,1,1]\n"
    "v_pk_fma_f32 v[60:61], v[60:61], v[244:245], v[246:247] op_sel:[0,1,1] op_sel_hi:[1,1,1]\n"
    "v_pk_fma_f32 v[62:63], v[62:63], v[244:245], v[246:247] op_sel:[0,1,1] op_sel_hi:[1,1,1]\n"
    "v_pk_fma_f32 v[56:57], v[56:57], v[24:25], v[16:17]\n"
    "v_pk_fma_f32 v[58:59], v[58:59], v[26:27], v[18:19]\n"
    "v_pk_fma_f32 v[60:61], v[60:61], v[28:29], v[20:21]\n"
    "v_pk_fma_f32 v[62:63], v[62:63], v[30:31], v[22:23]\n"
    "ds_read_b128 v[16:19], v243 offset:1152\n"
    "ds_read_b128 v[20:23], v243 offset:1168\n"
    "ds_read_b128 v[24:27], v243 offset:2944\n"
    "ds_read_b128 v[28:31], v243 offset:2960\n"
    "v_cvt_pk_bf16_f32 v188, v56, v57\n"
    "v_cvt_pk_bf16_f32 v189, v58, v59\n"
    "v_cvt_pk_bf16_f32 v190, v60, v61\n"
    "v_cvt_pk_bf16_f32 v191, v62, v63\n"
    "v_lshlrev_b32 v64, 16, v192\n"
    "v_and_b32 v65, s53, v192\n"
    "v_lshlrev_b32 v66, 16, v193\n"
    "v_and_b32 v67, s53, v193\n"
    "v_lshlrev_b32 v68, 16, v194\n"
    "v_and_b32 v69, s53, v194\n"
    "v_lshlrev_b32 v70, 16, v195\n"
    "v_and_b32 v71, s53, v195\n"
    "v_pk_fma_f32 v[64:65], v[64:65], v[244:245], v[246:247] op_sel:[0,0,0] op_sel_hi:[1,0,0]\n"
    "v_pk_fma_f32 v[66:67], v[66:67], v[244:245], v[246:247] op_sel:[0,0,0] op_sel_hi:[1,0,0]\n"
    "v_pk_fma_f32 v[68:69], v[68:69], v[244:245], v[246:247] op_sel:[0,0,0] op_sel_hi:[1,0,0]\n"
    "v_pk_fma_f32 v[70:71], v[70:71], v[244:245], v[246:247] op_sel:[0,0,0] op_sel_hi:[1,0,0]\n"
    "s_waitcnt lgkmcnt(4)\n"
    "v_pk_fma_f32 v[64:65], v[64:65], v[8:9], v[0:1]\n"
    "v_pk_fma_f32 v[66:67], v[66:67], v[10:11], v[2:3]\n"
    "v_pk_fma_f32 v[68:69], v[68:69], v[12:13], v[4:5]\n"
    "v_pk_fma_f32 v[70:71], v[70:71], v[14:15], v[6:7]\n"
    "v_cvt_pk_bf16_f32 v192, v64, v65\n"
    "v_cvt_pk_bf16_f32 v193, v66, v67\n"
    "v_cvt_pk_bf16_f32 v194, v68, v69\n"
    "v_cvt_pk_bf16_f32 v195, v70, v71\n"
    "v_lshlrev_b32 v72, 16, v196\n"
    "v_and_b32 v73, s53, v196\n"
    "v_lshlrev_b32 v74, 16, v197\n"
    "v_and_b32 v75, s53, v197\n"
    "v_lshlrev_b32 v76, 16, v198\n"
    "v_and_b32 v77, s53, v198\n"
    "v_lshlrev_b32 v78, 16, v199\n"
    "v_and_b32 v79, s53, v199\n"
    "v_pk_fma_f32 v[72:73], v[72:73], v[244:245], v[246:247] op_sel:[0,1,1] op_sel_hi:[1,1,1]\n"
    "v_pk_fma_f32 v[74:75], v[74:75], v[244:245], v[246:247] op_sel:[0,1,1] op_sel_hi:[1,1,1]\n"
    "v_pk_fma_f32 v[76:77], v[76:77], v[244:245], v[246:247] op_sel:[0,1,1] op_sel_hi:[1,1,1]\n"
    "v_pk_fma_f32 v[78:79], v[78:79], v[244:245], v[246:247] op_sel:[0,1,1] op_sel_hi:[1,1,1]\n"
    "v_pk_fma_f32 v[72:73], v[72:73], v[8:9], v[0:1]\n"
    "v_pk_fma_f32 v[74:75], v[74:75], v[10:11], v[2:3]\n"
    "v_pk_fma_f32 v[76:77], v[76:77], v[12:13], v[4:5]\n"
    "v_pk_fma_f32 v[78:79], v[78:79], v[14:15], v[6:7]\n"
    "v_cvt_pk_bf16_f32 v196, v72, v73\n"
    "v_cvt_pk_bf16_f32 v197, v74, v75\n"
    "v_cvt_pk_bf16_f32 v198, v76, v77\n"
    "v_cvt_pk_bf16_f32 v199, v78, v79\n"
    "v_lshlrev_b32 v80, 16, v200\n"
    "v_and_b32 v81, s53, v200\n"
    "v_lshlrev_b32 v82, 16, v201\n"
    "v_and_b32 v83, s53, v201\n"
    "v_lshlrev_b32 v84, 16, v202\n"
    "v_and_b32 v85, s53, v202\n"
    "v_lshlrev_b32 v86, 16, v203\n"
    "v_and_b32 v87, s53, v203\n"
    "v_pk_fma_f32 v[80:81], v[80:81], v[244:245], v[246:247] op_sel:[0,0,0] op_sel_hi:[1,0,0]\n"
    "v_pk_fma_f32 v[82:83], v[82:83], v[244:245], v[246:247] op_sel:[0,0,0] op_sel_hi:[1,0,0]\n"
    "v_pk_fma_f32 v[84:85], v[84:85], v[244:245], v[246:247] op_sel:[0,0,0] op_sel_hi:[1,0,0]\n"
    "v_pk_fma_f32 v[86:87], v[86:87], v[244:245], v[246:247] op_sel:[0,0,0] op_sel_hi:[1,0,0]\n"
    "s_waitcnt lgkmcnt(0)\n"
    "v_pk_fma_f32 v[80:81], v[80:81], v[24:25], v[16:17]\n"
    "v_pk_fma_f32 v[82:83], v[82:83], v[26:27], v[18:19]\n"
    "v_pk_fma_f32 v[84:85], v[84:85], v[28:29], v[20:21]\n"
    "v_pk_fma_f32 v[86:87], v[86:87], v[30:31], v[22:23]\n"
    "s_waitcnt vmcnt(8)\n"
    "s_barrier\n"
    "ds_read_b64_tr_b16 a[128:129], v240 offset:33792\n"
    "ds_read_b64_tr_b16 a[130:131], v240 offset:34304\n"
    "ds_read_b64_tr_b16 a[132:133], v240 offset:50688\n"
    "ds_read_b64_tr_b16 a[134:135], v240 offset:51200\n"
    "ds_read_b64_tr_b16 a[136:137], v240 offset:33824\n"
    "ds_read_b64_tr_b16 a[138:139], v240 offset:34336\n"
    "ds_read_b64_tr_b16 a[140:141], v240 offset:50720\n"
    "ds_read_b64_tr_b16 a[142:143], v240 offset:51232\n"
    "ds_read_b64_tr_b16 a[144:145], v240 offset:33856\n"
    "ds_read_b64_tr_b16 a[146:147], v240 offset:34368\n"
    "ds_read_b64_tr_b16 a[148:149], v240 offset:50752\n"
    "ds_read_b64_tr_b16 a[150:151], v240 offset:51264\n"
    "v_cvt_pk_bf16_f32 v200, v80, v81\n"
    "v_cvt_pk_bf16_f32 v201, v82, v83\n"
    "v_cvt_pk_bf16_f32 v202, v84, v85\n"
    "v_cvt_pk_bf16_f32 v203, v86, v87\n"
    "v_lshlrev_b32 v88, 16, v204\n"
    "v_and_b32 v89, s53, v204\n"
    "v_lshlrev_b32 v90, 16, v205\n"
    "v_and_b32 v91, s53, v205\n"
    "v_lshlrev_b32 v92, 16, v206\n"
    "v_and_b32 v93, s53, v206\n"
    "v_lshlrev_b32 v94, 16, v207\n"
    "v_and_b32 v95, s53, v207\n"
    "v_pk_fma_f32 v[88:89], v[88:89], v[244:245], v[246:247] op_sel:[0,1,1] op_sel_hi:[1,1,1]\n"
    "v_pk_fma_f32 v[90:91], v[90:91], v[244:245], v[246:247] op_sel:[0,1,1] op_sel_hi:[1,1,1]\n"
    "v_pk_fma_f32 v[92:93], v[92:93], v[244:245], v[246:247] op_sel:[0,1,1] op_sel_hi:[1,1,1]\n"
    "v_pk_fma_f32 v[94:95], v[94:95], v[244:245], v[246:247] op_sel:[0,1,1] op_sel_hi:[1,1,1]\n"
    "v_pk_fma_f32 v[88:89], v[88:89], v[24:25], v[16:17]\n"
    "v_pk_fma_f32 v[90:91], v[90:91], v[26:27], v[18:19]\n"
    "v_pk_fma_f32 v[92:93], v[92:93], v[28:29], v[20:21]\n"
    "v_pk_fma_f32 v[94:95], v[94:95], v[30:31], v[22:23]\n"
    "v_cvt_pk_bf16_f32 v204, v88, v89\n"
    "v_cvt_pk_bf16_f32 v205, v90, v91\n"
    "v_cvt_pk_bf16_f32 v206, v92, v93\n"
    "v_cvt_pk_bf16_f32 v207, v94, v95\n"
    "s_waitcnt lgkmcnt(8)\n"
    "v_mfma_f32_16x16x32_bf16 v[0:3], a[128:131], v[128:131], 0\n"
    "s_add_u32 m0, s48, 130048\n"
    "ds_read_b64_tr_b16 a[152:153], v240 offset:33888\n"
    "v_mfma_f32_16x16x32_bf16 v[0:3], a[132:135], v[136:139], v[0:3]\n"
    "s_add_u32 s68, s68, 434176\n"
    "ds_read_b64_tr_b16 a[154:155], v240 offset:34400\n"
    "v_mfma_f32_16x16x32_bf16 v[4:7], a[132:135], v[140:143], 0\n"
    "ds_read_b64_tr_b16 a[156:157], v240 offset:50784\n"
    "buffer_load_dwordx4 v255, s[56:59], s68 offen lds\n"
    "v_mfma_f32_16x16x32_bf16 v[4:7], a[128:131], v[132:135], v[4:7]\n"
    "s_waitcnt lgkmcnt(7)\n"
    "ds_read_b64_tr_b16 a[158:159], v240 offset:51296\n"
    "v_mfma_f32_16x16x32_bf16 v[12:15], a[136:139], v[132:135], 0\n"
    "s_add_u32 m0, s48, 131104\n"
    "ds_read_b64_tr_b16 a[160:161], v240 offset:33920\n"
    "v_mfma_f32_16x16x32_bf16 v[12:15], a[140:143], v[140:143], v[12:15]\n"
    "s_add_u32 s68, s68, 8192\n"
    "ds_read_b64_tr_b16 a[162:163], v240 offset:34432\n"
    "v_mfma_f32_16x16x32_bf16 v[8:11], a[140:143], v[136:139], 0\n"
    "ds_read_b64_tr_b16 a[164:165], v240 offset:50816\n"
    "buffer_load_dwordx4 v255, s[56:59], s68 offen lds\n"
    "v_mfma_f32_16x16x32_bf16 v[8:11], a[136:139], v[128:131], v[8:11]\n"
    "s_waitcnt lgkmcnt(7)\n"
    "ds_read_b64_tr_b16 a[166:167], v240 offset:51328\n"
    "v_mfma_f32_16x16x32_bf16 v[16:19], a[144:147], v[128:131], 0\n"
    "s_add_u32 m0, s48, 132160\n"
    "ds_read_b64_tr_b16 a[168:169], v240 offset:33952\n"
    "v_mfma_f32_16x16x32_bf16 v[16:19], a[148:151], v[136:139], v[16:19]\n"
    "s_add_u32 s68, s68, 8192\n"
    "ds_read_b64_tr_b16 a[170:171], v240 offset:34464\n"
    "v_mfma_f32_16x16x32_bf16 v[20:23], a[148:151], v[140:143], 0\n"
    "ds_read_b64_tr_b16 a[172:173], v240 offset:50848\n"
    "buffer_load_dwordx4 v255, s[56:59], s68 offen lds\n"
    "v_mfma_f32_16x16x32_bf16 v[20:23], a[144:147], v[132:135], v[20:23]\n"
    "s_waitcnt lgkmcnt(7)\n"
    "ds_read_b64_tr_b16 a[174:175], v240 offset:51360\n"
    "v_mfma_f32_16x16x32_bf16 v[28:31], a[152:155], v[132:135], 0\n"
    "s_add_u32 m0, s48, 133216\n"
    "ds_read_b64_tr_b16 a[176:177], v240 offset:33984\n"
    "v_mfma_f32_16x16x32_bf16 v[28:31], a[156:159], v[140:143], v[28:31]\n"
    "s_add_u32 s68, s68, 8192\n"
    "ds_read_b64_tr_b16 a[178:179], v240 offset:34496\n"
    "v_mfma_f32_16x16x32_bf16 v[24:27], a[156:159], v[136:139], 0\n"
    "ds_read_b64_tr_b16 a[180:181], v240 offset:50880\n"
    "buffer_load_dwordx4 v255, s[56:59], s68 offen lds\n"
    "v_mfma_f32_16x16x32_bf16 v[24:27], a[152:155], v[128:131], v[24:27]\n"
    "s_waitcnt lgkmcnt(7)\n"
    "ds_read_b64_tr_b16 a[182:183], v240 offset:51392\n"
    "v_mfma_f32_16x16x32_bf16 v[32:35], a[160:163], v[128:131], 0\n"
    "s_add_u32 m0, s48, 134272\n"
    "ds_read_b64_tr_b16 a[184:185], v240 offset:34016\n"
    "v_mfma_f32_16x16x32_bf16 v[32:35], a[164:167], v[136:139], v[32:35]\n"
    "s_add_u32 s68, s68, 40960\n"
    "ds_read_b64_tr_b16 a[186:187], v240 offset:34528\n"
    "v_mfma_f32_16x16x32_bf16 v[36:39], a[164:167], v[140:143], 0\n"
    "ds_read_b64_tr_b16 a[188:189], v240 offset:50912\n"
    "buffer_load_dwordx4 v255, s[56:59], s68 offen lds\n"
    "v_mfma_f32_16x16x32_bf16 v[36:39], a[160:163], v[132:135], v[36:39]\n"
    "s_waitcnt lgkmcnt(7)\n"
    "ds_read_b64_tr_b16 a[190:191], v240 offset:51424\n"
    "v_mfma_f32_16x16x32_bf16 v[44:47], a[168:171], v[132:135], 0\n"
    "s_add_u32 m0, s48, 135328\n"
    "ds_read_b64_tr_b16 a[128:129], v240 offset:34048\n"
    "v_mfma_f32_16x16x32_bf16 v[44:47], a[172:175], v[140:143], v[44:47]\n"
    "s_add_u32 s68, s68, 8192\n"
    "ds_read_b64_tr_b16 a[130:131], v240 offset:34560\n"
    "v_mfma_f32_16x16x32_bf16 v[40:43], a[172:175], v[136:139], 0\n"
    "ds_read_b64_tr_b16 a[132:133], v240 offset:50944\n"
    "buffer_load_dwordx4 v255, s[56:59], s68 offen lds\n"
    "v_mfma_f32_16x16x32_bf16 v[40:43], a[168:171], v[128:131], v[40:43]\n"
    "s_waitcnt lgkmcnt(7)\n"
    "ds_read_b64_tr_b16 a[134:135], v240 offset:51456\n"
    "v_mfma_f32_16x16x32_bf16 v[48:51], a[176:179], v[128:131], 0\n"
    "s_add_u32 m0, s48, 136384\n"
    "ds_read_b64_tr_b16 a[136:137], v240 offset:34080\n"
    "v_mfma_f32_16x16x32_bf16 v[48:51], a[180:183], v[136:139], v[48:51]\n"
    "s_add_u32 s68, s68, 8192\n"
    "ds_read_b64_tr_b16 a[138:139], v240 offset:34592\n"
    "v_mfma_f32_16x16x32_bf16 v[52:55], a[180:183], v[140:143], 0\n"
    "ds_read_b64_tr_b16 a[140:141], v240 offset:50976\n"
    "buffer_load_dwordx4 v255, s[56:59], s68 offen lds\n"
    "v_mfma_f32_16x16x32_bf16 v[52:55], a[176:179], v[132:135], v[52:55]\n"
    "s_waitcnt lgkmcnt(7)\n"
    "ds_read_b64_tr_b16 a[142:143], v240 offset:51488\n"
    "v_mfma_f32_16x16x32_bf16 v[60:63], a[184:187], v[132:135], 0\n"
    "s_add_u32 m0, s48, 137440\n"
    "ds_read_b64_tr_b16 a[144:145], v240 offset:34112\n"
    "v_mfma_f32_16x16x32_bf16 v[60:63], a[188:191], v[140:143], v[60:63]\n"
    "s_add_u32 s68, s68, 8192\n"
    "ds_read_b64_tr_b16 a[146:147], v240 offset:34624\n"
    "v_mfma_f32_16x16x32_bf16 v[56:59], a[188:191], v[136:139], 0\n"
    "ds_read_b64_tr_b16 a[148:149], v240 offset:51008\n"
    "buffer_load_dwordx4 v255, s[56:59], s68 offen lds\n"
    "v_mfma_f32_16x16x32_bf16 v[56:59], a[184:187], v[128:131], v[56:59]\n"
    "s_waitcnt lgkmcnt(7)\n"
    "ds_read_b64_tr_b16 a[150:151], v240 offset:51520\n"
    "v_mfma_f32_16x16x32_bf16 v[64:67], a[128:131], v[128:131], 0\n"
    "s_mov_b32 m0, 114688\n"
    "ds_read_b64_tr_b16 a[152:153], v240 offset:34144\n"
    "v_mfma_f32_16x16x32_bf16 v[64:67], a[132:135], v[136:139], v[64:67]\n"
    "ds_read_b64_tr_b16 a[154:155], v240 offset:34656\n"
    "buffer_load_dword v254, s[40:43], s70 offen lds\n"
    "v_mfma_f32_16x16x32_bf16 v[68:71], a[132:135], v[140:143], 0\n"
    "ds_read_b64_tr_b16 a[156:157], v240 offset:51040\n"
    "v_mfma_f32_16x16x32_bf16 v[68:71], a[128:131], v[132:135], v[68:71]\n"
    "s_waitcnt lgkmcnt(7)\n"
    "ds_read_b64_tr_b16 a[158:159], v240 offset:51552\n"
    "v_mfma_f32_16x16x32_bf16 v[76:79], a[136:139], v[132:135], 0\n"
    "s_mov_b32 m0, 114688\n"
    "ds_read_b64_tr_b16 a[160:161], v240 offset:34176\n"
    "v_mfma_f32_16x16x32_bf16 v[76:79], a[140:143], v[140:143], v[76:79]\n"
    "ds_read_b64_tr_b16 a[162:163], v240 offset:34688\n"
    "buffer_load_dword v254, s[44:47], s70 offen lds\n"
    "v_mfma_f32_16x16x32_bf16 v[72:75], a[140:143], v[136:139], 0\n"
    "ds_read_b64_tr_b16 a[164:165], v240 offset:51072\n"
    "v_mfma_f32_16x16x32_bf16 v[72:75], a[136:139], v[128:131], v[72:75]\n"
    "s_waitcnt lgkmcnt(7)\n"
    "ds_read_b64_tr_b16 a[166:167], v240 offset:51584\n"
    "v_mfma_f32_16x16x32_bf16 v[80:83], a[144:147], v[128:131], 0\n"
    "ds_read_b64_tr_b16 a[168:169], v240 offset:34208\n"
    "v_mfma_f32_16x16x32_bf16 v[80:83], a[148:151], v[136:139], v[80:83]\n"
    "ds_read_b64_tr_b16 a[170:171], v240 offset:34720\n"
    "v_mfma_f32_16x16x32_bf16 v[84:87], a[148:151], v[140:143], 0\n"
    "ds_read_b64_tr_b16 a[172:173], v240 offset:51104\n"
    "v_mfma_f32_16x16x32_bf16 v[84:87], a[144:147], v[132:135], v[84:87]\n"
    "s_waitcnt lgkmcnt(7)\n"
    "ds_read_b64_tr_b16 a[174:175], v240 offset:51616\n"
    "v_mfma_f32_16x16x32_bf16 v[92:95], a[152:155], v[132:135], 0\n"
    "ds_read_b64_tr_b16 a[176:177], v240 offset:34240\n"
    "v_mfma_f32_16x16x32_bf16 v[92:95], a[156:159], v[140:143], v[92:95]\n"
    "ds_read_b64_tr_b16 a[178:179], v240 offset:34752\n"
    "v_mfma_f32_16x16x32_bf16 v[88:91], a[156:159], v[136:139], 0\n"
    "ds_read_b64_tr_b16 a[180:181], v240 offset:51136\n"
    "v_mfma_f32_16x16x32_bf16 v[88:91], a[152:155], v[128:131], v[88:91]\n"
    "s_waitcnt lgkmcnt(7)\n"
    "ds_read_b64_tr_b16 a[182:183], v240 offset:51648\n"
    "v_mfma_f32_16x16x32_bf16 v[96:99], a[160:163], v[128:131], 0\n"
    "ds_read_b64_tr_b16 a[184:185], v240 offset:34272\n"
    "v_mfma_f32_16x16x32_bf16 v[96:99], a[164:167], v[136:139], v[96:99]\n"
    "ds_read_b64_tr_b16 a[186:187], v240 offset:34784\n"
    "v_mfma_f32_16x16x32_bf16 v[100:103], a[164:167], v[140:143], 0\n"
    "s_waitcnt vmcnt(10)\n"
    "s_barrier\n"
    "v_mfma_f32_16x16x32_bf16 v[100:103], a[160:163], v[132:135], v[100:103]\n"
    "s_waitcnt lgkmcnt(6)\n"
    "ds_read_b64_tr_b16 a[188:189], v240 offset:51168\n"
    "ds_read_b64_tr_b16 a[190:191], v240 offset:51680\n"
    "v_mfma_f32_16x16x32_bf16 v[108:111], a[168:171], v[132:135], 0\n"
    "ds_read_b64_tr_b16 a[128:129], v241 offset:0\n"
    "v_mfma_f32_16x16x32_bf16 v[108:111], a[172:175], v[140:143], v[108:111]\n"
    "ds_read_b64_tr_b16 a[130:131], v241 offset:512\n"
    "v_mfma_f32_16x16x32_bf16 v[104:107], a[172:175], v[136:139], 0\n"
    "ds_read_b64_tr_b16 a[132:133], v241 offset:16896\n"
    "v_mfma_f32_16x16x32_bf16 v[104:107], a[168:171], v[128:131], v[104:107]\n"
    "s_waitcnt lgkmcnt(7)\n"
    "ds_read_b64_tr_b16 a[134:135], v241 offset:17408\n"
    "v_mfma_f32_16x16x32_bf16 v[112:115], a[176:179], v[128:131], 0\n"
    "ds_read_b64_tr_b16 a[136:137], v241 offset:32\n"
    "v_mfma_f32_16x16x32_bf16 v[112:115], a[180:183], v[136:139], v[112:115]\n"
    "ds_read_b64_tr_b16 a[138:139], v241 offset:544\n"
    "v_mfma_f32_16x16x32_bf16 v[116:119], a[180:183], v[140:143], 0\n"
    "ds_read_b64_tr_b16 a[140:141], v241 offset:16928\n"
    "v_mfma_f32_16x16x32_bf16 v[116:119], a[176:179], v[132:135], v[116:119]\n"
    "s_waitcnt lgkmcnt(7)\n"
    "ds_read_b64_tr_b16 a[142:143], v241 offset:17440\n"
    "v_mfma_f32_16x16x32_bf16 v[124:127], a[184:187], v[132:135], 0\n"
    "ds_read_b64_tr_b16 a[144:145], v241 offset:64\n"
    "v_mfma_f32_16x16x32_bf16 v[124:127], a[188:191], v[140:143], v[124:127]\n"
    "ds_read_b64_tr_b16 a[146:147], v241 offset:576\n"
    "v_mfma_f32_16x16x32_bf16 v[120:123], a[188:191], v[136:139], 0\n"
    "ds_read_b64_tr_b16 a[148:149], v241 offset:16960\n"
    "v_mfma_f32_16x16x32_bf16 v[120:123], a[184:187], v[128:131], v[120:123]\n"
    "s_waitcnt lgkmcnt(7)\n"
    "ds_read_b64_tr_b16 a[150:151], v241 offset:17472\n"
    "v_mfma_f32_16x16x32_bf16 v[0:3], a[128:131], v[144:147], v[0:3]\n"
    "s_add_u32 m0, s48, 0\n"
    "ds_read_b64_tr_b16 a[152:153], v241 offset:96\n"
    "ds_read_b128 v[128:131], v243 offset:1280\n"
    "v_mfma_f32_16x16x32_bf16 v[0:3], a[132:135], v[152:155], v[0:3]\n"
    "s_add_u32 s68, s68, 434176\n"
    "ds_read_b64_tr_b16 a[154:155], v241 offset:608\n"
    "ds_read_b128 v[132:135], v243 offset:3072\n"
    "v_mfma_f32_16x16x32_bf16 v[4:7], a[132:135], v[156:159], v[4:7]\n"
    "ds_read_b64_tr_b16 a[156:157], v241 offset:16992\n"
    "buffer_load_dwordx4 v255, s[56:59], s68 offen lds\n"
    "v_mfma_f32_16x16x32_bf16 v[4:7], a[128:131], v[148:151], v[4:7]\n"
    "s_waitcnt lgkmcnt(9)\n"
    "ds_read_b64_tr_b16 a[158:159], v241 offset:17504\n"
    "ds_read_b128 v[136:139], v243 offset:1296\n"
    "v_mfma_f32_16x16x32_bf16 v[12:15], a[136:139], v[148:151], v[12:15]\n"
    "s_add_u32 m0, s48, 1056\n"
    "ds_read_b64_tr_b16 a[160:161], v241 offset:128\n"
    "ds_read_b128 v[140:143], v243 offset:3088\n"
    "v_mfma_f32_16x16x32_bf16 v[12:15], a[140:143], v[156:159], v[12:15]\n"
    "s_add_u32 s68, s68, 8192\n"
    "ds_read_b64_tr_b16 a[162:163], v241 offset:640\n"
    "v_mfma_f32_16x16x32_bf16 v[8:11], a[140:143], v[152:155], v[8:11]\n"
    "ds_read_b64_tr_b16 a[164:165], v241 offset:17024\n"
    "buffer_load_dwordx4 v255, s[56:59], s68 offen lds\n"
    "v_mfma_f32_16x16x32_bf16 v[8:11], a[136:139], v[144:147], v[8:11]\n"
    "s_waitcnt lgkmcnt(11)\n"
    "ds_read_b64_tr_b16 a[166:167], v241 offset:17536\n"
    "v_mfma_f32_16x16x32_bf16 v[16:19], a[144:147], v[144:147], v[16:19]\n"
    "s_add_u32 m0, s48, 2112\n"
    "ds_read_b64_tr_b16 a[168:169], v241 offset:160\n"
    "v_lshlrev_b32 v251, 16, v208\n"
    "v_mfma_f32_16x16x32_bf16 v[16:19], a[148:151], v[152:155], v[16:19]\n"
    "s_add_u32 s68, s68, 8192\n"
    "ds_read_b64_tr_b16 a[170:171], v241 offset:672\n"
    "v_and_b32 v253, s53, v208\n"
    "v_mfma_f32_16x16x32_bf16 v[20:23], a[148:151], v[156:159], v[20:23]\n"
    "ds_read_b64_tr_b16 a[172:173], v241 offset:17056\n"
    "buffer_load_dwordx4 v255, s[56:59], s68 offen lds\n"
    "v_mfma_f32_16x16x32_bf16 v[20:23], a[144:147], v[148:151], v[20:23]\n"
    "s_waitcnt lgkmcnt(9)\n"
    "ds_read_b64_tr_b16 a[174:175], v241 offset:17568\n"
    "v_fma_f32 v251, v251, v244, v246\n"
    "v_mfma_f32_16x16x32_bf16 v[28:31], a[152:155], v[148:151], v[28:31]\n"
    "s_add_u32 m0, s48, 3168\n"
    "ds_read_b64_tr_b16 a[176:177], v241 offset:192\n"
    "v_fma_f32 v253, v253, v244, v246\n"
    "v_mfma_f32_16x16x32_bf16 v[28:31], a[156:159], v[156:159], v[28:31]\n"
    "s_add_u32 s68, s68, 8192\n"
    "ds_read_b64_tr_b16 a[178:179], v241 offset:704\n"
    "v_mfma_f32_16x16x32_bf16 v[24:27], a[156:159], v[152:155], v[24:27]\n"
    "ds_read_b64_tr_b16 a[180:181], v241 offset:17088\n"
    "buffer_load_dwordx4 v255, s[56:59], s68 offen lds\n"
    "v_mfma_f32_16x16x32_bf16 v[24:27], a[152:155], v[144:147], v[24:27]\n"
    "s_waitcnt lgkmcnt(7)\n"
    "ds_read_b64_tr_b16 a[182:183], v241 offset:17600\n"
    "v_fma_f32 v251, v251, v132, v128\n"
    "v_mfma_f32_16x16x32_bf16 v[32:35], a[160:163], v[144:147], v[32:35]\n"
    "s_add_u32 m0, s48, 4224\n"
    "ds_read_b64_tr_b16 a[184:185], v241 offset:224\n"
    "v_fma_f32 v253, v253, v133, v129\n"
    "v_mfma_f32_16x16x32_bf16 v[32:35], a[164:167], v[152:155], v[32:35]\n"
    "s_add_u32 s68, s68, 40960\n"
    "ds_read_b64_tr_b16 a[186:187], v241 offset:736\n"
    "v_cvt_pk_bf16_f32 v208, v251, v253\n"
    "v_mfma_f32_16x16x32_bf16 v[36:39], a[164:167], v[156:159], v[36:39]\n"
    "ds_read_b64_tr_b16 a[188:189], v241 offset:17120\n"
    "buffer_load_dwordx4 v255, s[56:59], s68 offen lds\n"
    "v_mfma_f32_16x16x32_bf16 v[36:39], a[160:163], v[148:151], v[36:39]\n"
    "s_waitcnt lgkmcnt(7)\n"
    "ds_read_b64_tr_b16 a[190:191], v241 offset:17632\n"
    "v_lshlrev_b32 v251, 16, v209\n"
    "v_mfma_f32_16x16x32_bf16 v[44:47], a[168:171], v[148:151], v[44:47]\n"
    "s_add_u32 m0, s48, 5280\n"
    "ds_read_b64_tr_b16 a[128:129], v241 offset:256\n"
    "v_and_b32 v253, s53, v209\n"
    "v_mfma_f32_16x16x32_bf16 v[44:47], a[172:175], v[156:159], v[44:47]\n"
    "s_add_u32 s68, s68, 8192\n"
    "ds_read_b64_tr_b16 a[130:131], v241 offset:768\n"
    "v_fma_f32 v251, v251, v244, v246\n"
    "v_mfma_f32_16x16x32_bf16 v[40:43], a[172:175], v[152:155], v[40:43]\n"
    "ds_read_b64_tr_b16 a[132:133], v241 offset:17152\n"
    "buffer_load_dwordx4 v255, s[56:59], s68 offen lds\n"
    "v_mfma_f32_16x16x32_bf16 v[40:43], a[168:171], v[144:147], v[40:43]\n"
    "s_waitcnt lgkmcnt(7)\n"
    "ds_read_b64_tr_b16 a[134:135], v241 offset:17664\n"
    "v_fma_f32 v253, v253, v244, v246\n"
    "v_mfma_f32_16x16x32_bf16 v[48:51], a[176:179], v[144:147], v[48:51]\n"
    "s_add_u32 m0, s48, 6336\n"
    "ds_read_b64_tr_b16 a[136:137], v241 offset:288\n"
    "v_fma_f32 v251, v251, v134, v130\n"
    "v_mfma_f32_16x16x32_bf16 v[48:51], a[180:183], v[152:155], v[48:51]\n"
    "s_add_u32 s68, s68, 8192\n"
    "ds_read_b64_tr_b16 a[138:139], v241 offset:800\n"
    "v_fma_f32 v253, v253, v135, v131\n"
    "v_mfma_f32_16x16x32_bf16 v[52:55], a[180:183], v[156:159], v[52:55]\n"
    "ds_read_b64_tr_b16 a[140:141], v241 offset:17184\n"
    "buffer_load_dwordx4 v255, s[56:59], s68 offen lds\n"
    "v_mfma_f32_16x16x32_bf16 v[52:55], a[176:179], v[148:151], v[52:55]\n"
    "s_waitcnt lgkmcnt(7)\n"
    "ds_read_b64_tr_b16 a[142:143], v241 offset:17696\n"
    "v_cvt_pk_bf16_f32 v209, v251, v253\n"
    "v_mfma_f32_16x16x32_bf16 v[60:63], a[184:187], v[148:151], v[60:63]\n"
    "s_add_u32 m0, s48, 7392\n"
    "ds_read_b64_tr_b16 a[144:145], v241 offset:320\n"
    "v_lshlrev_b32 v251, 16, v212\n"
    "v_mfma_f32_16x16x32_bf16 v[60:63], a[188:191], v[156:159], v[60:63]\n"
    "s_add_u32 s68, s68, 8192\n"
    "ds_read_b64_tr_b16 a[146:147], v241 offset:832\n"
    "v_and_b32 v253, s53, v212\n"
    "v_mfma_f32_16x16x32_bf16 v[56:59], a[188:191], v[152:155], v[56:59]\n"
    "ds_read_b64_tr_b16 a[148:149], v241 offset:17216\n"
    "buffer_load_dwordx4 v255, s[56:59], s68 offen lds\n"
    "v_mfma_f32_16x16x32_bf16 v[56:59], a[184:187], v[144:147], v[56:59]\n"
    "s_waitcnt lgkmcnt(7)\n"
    "ds_read_b64_tr_b16 a[150:151], v241 offset:17728\n"
    "v_fma_f32 v251, v251, v245, v247\n"
    "v_mfma_f32_16x16x32_bf16 v[64:67], a[128:131], v[144:147], v[64:67]\n"
    "s_mov_b32 m0, 114688\n"
    "ds_read_b64_tr_b16 a[152:153], v241 offset:352\n"
    "v_fma_f32 v253, v253, v245, v247\n"
    "v_mfma_f32_16x16x32_bf16 v[64:67], a[132:135], v[152:155], v[64:67]\n"
    "s_add_u32 s70, s70, 65536\n"
    "ds_read_b64_tr_b16 a[154:155], v241 offset:864\n"
    "v_fma_f32 v251, v251, v132, v128\n"
    "v_mfma_f32_16x16x32_bf16 v[68:71], a[132:135], v[156:159], v[68:71]\n"
    "ds_read_b64_tr_b16 a[156:157], v241 offset:17248\n"
    "buffer_load_dword v254, s[40:43], s70 offen lds\n"
    "v_mfma_f32_16x16x32_bf16 v[68:71], a[128:131], v[148:151], v[68:71]\n"
    "s_waitcnt lgkmcnt(7)\n"
    "ds_read_b64_tr_b16 a[158:159], v241 offset:17760\n"
    "v_fma_f32 v253, v253, v133, v129\n"
    "v_mfma_f32_16x16x32_bf16 v[76:79], a[136:139], v[148:151], v[76:79]\n"
    "s_mov_b32 m0, 114688\n"
    "ds_read_b64_tr_b16 a[160:161], v241 offset:384\n"
    "v_cvt_pk_bf16_f32 v212, v251, v253\n"
    "v_mfma_f32_16x16x32_bf16 v[76:79], a[140:143], v[156:159], v[76:79]\n"
    "ds_read_b64_tr_b16 a[162:163], v241 offset:896\n"
    "buffer_load_dword v254, s[44:47], s70 offen lds\n"
    "v_mfma_f32_16x16x32_bf16 v[72:75], a[140:143], v[152:155], v[72:75]\n"
    "ds_read_b64_tr_b16 a[164:165], v241 offset:17280\n"
    "v_lshlrev_b32 v251, 16, v213\n"
    "v_mfma_f32_16x16x32_bf16 v[72:75], a[136:139], v[144:147], v[72:75]\n"
    "s_waitcnt lgkmcnt(7)\n"
    "ds_read_b64_tr_b16 a[166:167], v241 offset:17792\n"
    "v_and_b32 v253, s53, v213\n"
    "v_mfma_f32_16x16x32_bf16 v[80:83], a[144:147], v[144:147], v[80:83]\n"
    "ds_read_b64_tr_b16 a[168:169], v241 offset:416\n"
    "v_fma_f32 v251, v251, v245, v247\n"
    "v_mfma_f32_16x16x32_bf16 v[80:83], a[148:151], v[152:155], v[80:83]\n"
    "ds_read_b64_tr_b16 a[170:171], v241 offset:928\n"
    "v_fma_f32 v253, v253, v245, v247\n"
    "v_mfma_f32_16x16x32_bf16 v[84:87], a[148:151], v[156:159], v[84:87]\n"
    "ds_read_b64_tr_b16 a[172:173], v241 offset:17312\n"
    "v_fma_f32 v251, v251, v134, v130\n"
    "v_mfma_f32_16x16x32_bf16 v[84:87], a[144:147], v[148:151], v[84:87]\n"
    "s_waitcnt lgkmcnt(7)\n"
    "ds_read_b64_tr_b16 a[174:175], v241 offset:17824\n"
    "v_fma_f32 v253, v253, v135, v131\n"
    "v_mfma_f32_16x16x32_bf16 v[92:95], a[152:155], v[148:151], v[92:95]\n"
    "ds_read_b64_tr_b16 a[176:177], v241 offset:448\n"
    "v_cvt_pk_bf16_f32 v213, v251, v253\n"
    "v_mfma_f32_16x16x32_bf16 v[92:95], a[156:159], v[156:159], v[92:95]\n"
    "ds_read_b64_tr_b16 a[178:179], v241 offset:960\n"
    "ds_read_b128 v[128:131], v243 offset:1408\n"
    "v_mfma_f32_16x16x32_bf16 v[88:91], a[156:159], v[152:155], v[88:91]\n"
    "ds_read_b64_tr_b16 a[180:181], v241 offset:17344\n"
    "ds_read_b128 v[132:135], v243 offset:3200\n"
    "v_mfma_f32_16x16x32_bf16 v[88:91], a[152:155], v[144:147], v[88:91]\n"
    "s_waitcnt lgkmcnt(9)\n"
    "ds_read_b64_tr_b16 a[182:183], v241 offset:17856\n"
    "v_lshlrev_b32 v251, 16, v210\n"
    "v_mfma_f32_16x16x32_bf16 v[96:99], a[160:163], v[144:147], v[96:99]\n"
    "ds_read_b64_tr_b16 a[184:185], v241 offset:480\n"
    "v_and_b32 v253, s53, v210\n"
    "v_mfma_f32_16x16x32_bf16 v[96:99], a[164:167], v[152:155], v[96:99]\n"
    "ds_read_b64_tr_b16 a[186:187], v241 offset:992\n"
    "v_fma_f32 v251, v251, v244, v246\n"
    "v_mfma_f32_16x16x32_bf16 v[100:103], a[164:167], v[156:159], v[100:103]\n"
    "s_waitcnt vmcnt(12)\n"
    "s_barrier\n"
    "v_mfma_f32_16x16x32_bf16 v[100:103], a[160:163], v[148:151], v[100:103]\n"
    "s_waitcnt lgkmcnt(8)\n"
    "ds_read_b64_tr_b16 a[188:189], v241 offset:17376\n"
    "ds_read_b64_tr_b16 a[190:191], v241 offset:17888\n"
    "v_mfma_f32_16x16x32_bf16 v[108:111], a[168:171], v[148:151], v[108:111]\n"
    "ds_read_b64_tr_b16 a[128:129], v242 offset:0\n"
    "v_fma_f32 v253, v253, v244, v246\n"
    "v_mfma_f32_16x16x32_bf16 v[108:111], a[172:175], v[156:159], v[108:111]\n"
    "ds_read_b64_tr_b16 a[130:131], v242 offset:512\n"
    "v_fma_f32 v251, v251, v140, v136\n"
    "v_mfma_f32_16x16x32_bf16 v[104:107], a[172:175], v[152:155], v[104:107]\n"
    "ds_read_b64_tr_b16 a[132:133], v242 offset:16896\n"
    "v_fma_f32 v253, v253, v141, v137\n"
    "v_mfma_f32_16x16x32_bf16 v[104:107], a[168:171], v[144:147], v[104:107]\n"
    "s_waitcnt lgkmcnt(7)\n"
    "ds_read_b64_tr_b16 a[134:135], v242 offset:17408\n"
    "v_cvt_pk_bf16_f32 v210, v251, v253\n"
    "v_mfma_f32_16x16x32_bf16 v[112:115], a[176:179], v[144:147], v[112:115]\n"
    "ds_read_b64_tr_b16 a[136:137], v242 offset:32\n"
    "v_lshlrev_b32 v251, 16, v211\n"
    "v_mfma_f32_16x16x32_bf16 v[112:115], a[180:183], v[152:155], v[112:115]\n"
    "ds_read_b64_tr_b16 a[138:139], v242 offset:544\n"
    "v_and_b32 v253, s53, v211\n"
    "v_mfma_f32_16x16x32_bf16 v[116:119], a[180:183], v[156:159], v[116:119]\n"
    "ds_read_b64_tr_b16 a[140:141], v242 offset:16928\n"
    "v_fma_f32 v251, v251, v244, v246\n"
    "v_mfma_f32_16x16x32_bf16 v[116:119], a[176:179], v[148:151], v[116:119]\n"
    "s_waitcnt lgkmcnt(7)\n"
    "ds_read_b64_tr_b16 a[142:143], v242 offset:17440\n"
    "v_fma_f32 v253, v253, v244, v246\n"
    "v_mfma_f32_16x16x32_bf16 v[124:127], a[184:187], v[148:151], v[124:127]\n"
    "ds_read_b64_tr_b16 a[144:145], v242 offset:64\n"
    "v_fma_f32 v251, v251, v142, v138\n"
    "v_mfma_f32_16x16x32_bf16 v[124:127], a[188:191], v[156:159], v[124:127]\n"
    "ds_read_b64_tr_b16 a[146:147], v242 offset:576\n"
    "v_fma_f32 v253, v253, v143, v139\n"
    "v_mfma_f32_16x16x32_bf16 v[120:123], a[188:191], v[152:155], v[120:123]\n"
    "ds_read_b64_tr_b16 a[148:149], v242 offset:16960\n"
    "v_cvt_pk_bf16_f32 v211, v251, v253\n"
    "v_mfma_f32_16x16x32_bf16 v[120:123], a[184:187], v[144:147], v[120:123]\n"
    "s_waitcnt lgkmcnt(7)\n"
    "ds_read_b64_tr_b16 a[150:151], v242 offset:17472\n"
    "v_lshlrev_b32 v251, 16, v214\n"
    "v_mfma_f32_16x16x32_bf16 v[0:3], a[128:131], v[160:163], v[0:3]\n"
    "s_add_u32 m0, s48, 33792\n"
    "ds_read_b64_tr_b16 a[152:153], v242 offset:96\n"
    "v_and_b32 v253, s53, v214\n"
    "v_mfma_f32_16x16x32_bf16 v[0:3], a[132:135], v[168:171], v[0:3]\n"
    "s_add_u32 s68, s68, 434176\n"
    "ds_read_b64_tr_b16 a[154:155], v242 offset:608\n"
    "v_fma_f32 v251, v251, v245, v247\n"
    "v_mfma_f32_16x16x32_bf16 v[4:7], a[132:135], v[172:175], v[4:7]\n"
    "ds_read_b64_tr_b16 a[156:157], v242 offset:16992\n"
    "buffer_load_dwordx4 v255, s[56:59], s68 offen lds\n"
    "v_mfma_f32_16x16x32_bf16 v[4:7], a[128:131], v[164:167], v[4:7]\n"
    "s_waitcnt lgkmcnt(7)\n"
    "ds_read_b64_tr_b16 a[158:159], v242 offset:17504\n"
    "v_fma_f32 v253, v253, v245, v247\n"
    "v_mfma_f32_16x16x32_bf16 v[12:15], a[136:139], v[164:167], v[12:15]\n"
    "s_add_u32 m0, s48, 34848\n"
    "ds_read_b64_tr_b16 a[160:161], v242 offset:128\n"
    "v_fma_f32 v251, v251, v140, v136\n"
    "v_mfma_f32_16x16x32_bf16 v[12:15], a[140:143], v[172:175], v[12:15]\n"
    "s_add_u32 s68, s68, 8192\n"
    "ds_read_b64_tr_b16 a[162:163], v242 offset:640\n"
    "v_fma_f32 v253, v253, v141, v137\n"
    "v_mfma_f32_16x16x32_bf16 v[8:11], a[140:143], v[168:171], v[8:11]\n"
    "ds_read_b64_tr_b16 a[164:165], v242 offset:17024\n"
    "buffer_load_dwordx4 v255, s[56:59], s68 offen lds\n"
    "v_mfma_f32_16x16x32_bf16 v[8:11], a[136:139], v[160:163], v[8:11]\n"
    "s_waitcnt lgkmcnt(7)\n"
    "ds_read_b64_tr_b16 a[166:167], v242 offset:17536\n"
    "v_cvt_pk_bf16_f32 v214, v251, v253\n"
    "v_mfma_f32_16x16x32_bf16 v[16:19], a[144:147], v[160:163], v[16:19]\n"
    "s_add_u32 m0, s48, 35904\n"
    "ds_read_b64_tr_b16 a[168:169], v242 offset:160\n"
    "v_lshlrev_b32 v251, 16, v215\n"
    "v_mfma_f32_16x16x32_bf16 v[16:19], a[148:151], v[168:171], v[16:19]\n"
    "s_add_u32 s68, s68, 8192\n"
    "ds_read_b64_tr_b16 a[170:171], v242 offset:672\n"
    "v_and_b32 v253, s53, v215\n"
    "v_mfma_f32_16x16x32_bf16 v[20:23], a[148:151], v[172:175], v[20:23]\n"
    "ds_read_b64_tr_b16 a[172:173], v242 offset:17056\n"
    "buffer_load_dwordx4 v255, s[56:59], s68 offen lds\n"
    "v_mfma_f32_16x16x32_bf16 v[20:23], a[144:147], v[164:167], v[20:23]\n"
    "s_waitcnt lgkmcnt(7)\n"
    "ds_read_b64_tr_b16 a[174:175], v242 offset:17568\n"
    "v_fma_f32 v251, v251, v245, v247\n"
    "v_mfma_f32_16x16x32_bf16 v[28:31], a[152:155], v[164:167], v[28:31]\n"
    "s_add_u32 m0, s48, 36960\n"
    "ds_read_b64_tr_b16 a[176:177], v242 offset:192\n"
    "v_fma_f32 v253, v253, v245, v247\n"
    "v_mfma_f32_16x16x32_bf16 v[28:31], a[156:159], v[172:175], v[28:31]\n"
    "s_add_u32 s68, s68, 8192\n"
    "ds_read_b64_tr_b16 a[178:179], v242 offset:704\n"
    "v_fma_f32 v251, v251, v142, v138\n"
    "v_mfma_f32_16x16x32_bf16 v[24:27], a[156:159], v[168:171], v[24:27]\n"
    "ds_read_b64_tr_b16 a[180:181], v242 offset:17088\n"
    "buffer_load_dwordx4 v255, s[56:59], s68 offen lds\n"
    "v_mfma_f32_16x16x32_bf16 v[24:27], a[152:155], v[160:163], v[24:27]\n"
    "s_waitcnt lgkmcnt(7)\n"
    "ds_read_b64_tr_b16 a[182:183], v242 offset:17600\n"
    "v_fma_f32 v253, v253, v143, v139\n"
    "v_mfma_f32_16x16x32_bf16 v[32:35], a[160:163], v[160:163], v[32:35]\n"
    "s_add_u32 m0, s48, 38016\n"
    "ds_read_b64_tr_b16 a[184:185], v242 offset:224\n"
    "v_cvt_pk_bf16_f32 v215, v251, v253\n"
    "v_mfma_f32_16x16x32_bf16 v[32:35], a[164:167], v[168:171], v[32:35]\n"
    "s_add_u32 s68, s68, 40960\n"
    "ds_read_b64_tr_b16 a[186:187], v242 offset:736\n"
    "ds_read_b128 v[136:139], v243 offset:1424\n"
    "v_mfma_f32_16x16x32_bf16 v[36:39], a[164:167], v[172:175], v[36:39]\n"
    "ds_read_b64_tr_b16 a[188:189], v242 offset:17120\n"
    "buffer_load_dwordx4 v255, s[56:59], s68 offen lds\n"
    "v_mfma_f32_16x16x32_bf16 v[36:39], a[160:163], v[164:167], v[36:39]\n"
    "s_waitcnt lgkmcnt(8)\n"
    "ds_read_b64_tr_b16 a[190:191], v242 offset:17632\n"
    "ds_read_b128 v[140:143], v243 offset:3216\n"
    "v_mfma_f32_16x16x32_bf16 v[44:47], a[168:171], v[164:167], v[44:47]\n"
    "s_add_u32 m0, s48, 39072\n"
    "ds_read_b64_tr_b16 a[128:129], v242 offset:256\n"
    "v_lshlrev_b32 v251, 16, v216\n"
    "v_mfma_f32_16x16x32_bf16 v[44:47], a[172:175], v[172:175], v[44:47]\n"
    "s_add_u32 s68, s68, 8192\n"
    "ds_read_b64_tr_b16 a[130:131], v242 offset:768\n"
    "v_and_b32 v253, s53, v216\n"
    "v_mfma_f32_16x16x32_bf16 v[40:43], a[172:175], v[168:171], v[40:43]\n"
    "ds_read_b64_tr_b16 a[132:133], v242 offset:17152\n"
    "buffer_load_dwordx4 v255, s[56:59], s68 offen lds\n"
    "v_mfma_f32_16x16x32_bf16 v[40:43], a[168:171], v[160:163], v[40:43]\n"
    "s_waitcnt lgkmcnt(9)\n"
    "ds_read_b64_tr_b16 a[134:135], v242 offset:17664\n"
    "v_fma_f32 v251, v251, v244, v246\n"
    "v_mfma_f32_16x16x32_bf16 v[48:51], a[176:179], v[160:163], v[48:51]\n"
    "s_add_u32 m0, s48, 40128\n"
    "ds_read_b64_tr_b16 a[136:137], v242 offset:288\n"
    "v_fma_f32 v253, v253, v244, v246\n"
    "v_mfma_f32_16x16x32_bf16 v[48:51], a[180:183], v[168:171], v[48:51]\n"
    "s_add_u32 s68, s68, 8192\n"
    "ds_read_b64_tr_b16 a[138:139], v242 offset:800\n"
    "v_mfma_f32_16x16x32_bf16 v[52:55], a[180:183], v[172:175], v[52:55]\n"
    "ds_read_b64_tr_b16 a[140:141], v242 offset:17184\n"
    "buffer_load_dwordx4 v255, s[56:59], s68 offen lds\n"
    "v_mfma_f32_16x16x32_bf16 v[52:55], a[176:179], v[164:167], v[52:55]\n"
    "s_waitcnt lgkmcnt(8)\n"
    "ds_read_b64_tr_b16 a[142:143], v242 offset:17696\n"
    "v_fma_f32 v251, v251, v132, v128\n"
    "v_mfma_f32_16x16x32_bf16 v[60:63], a[184:187], v[164:167], v[60:63]\n"
    "s_add_u32 m0, s48, 41184\n"
    "ds_read_b64_tr_b16 a[144:145], v242 offset:320\n"
    "v_fma_f32 v253, v253, v133, v129\n"
    "v_mfma_f32_16x16x32_bf16 v[60:63], a[188:191], v[172:175], v[60:63]\n"
    "s_add_u32 s68, s68, 8192\n"
    "ds_read_b64_tr_b16 a[146:147], v242 offset:832\n"
    "v_cvt_pk_bf16_f32 v216, v251, v253\n"
    "v_mfma_f32_16x16x32_bf16 v[56:59], a[188:191], v[168:171], v[56:59]\n"
    "ds_read_b64_tr_b16 a[148:149], v242 offset:17216\n"
    "buffer_load_dwordx4 v255, s[56:59], s68 offen lds\n"
    "v_mfma_f32_16x16x32_bf16 v[56:59], a[184:187], v[160:163], v[56:59]\n"
    "s_waitcnt lgkmcnt(7)\n"
    "ds_read_b64_tr_b16 a[150:151], v242 offset:17728\n"
    "v_lshlrev_b32 v251, 16, v217\n"
    "v_mfma_f32_16x16x32_bf16 v[64:67], a[128:131], v[160:163], v[64:67]\n"
    "s_mov_b32 m0, 114688\n"
    "ds_read_b64_tr_b16 a[152:153], v242 offset:352\n"
    "v_and_b32 v253, s53, v217\n"
    "v_mfma_f32_16x16x32_bf16 v[64:67], a[132:135], v[168:171], v[64:67]\n"
    "s_add_u32 s70, s70, 65536\n"
    "ds_read_b64_tr_b16 a[154:155], v242 offset:864\n"
    "v_fma_f32 v251, v251, v244, v246\n"
    "v_mfma_f32_16x16x32_bf16 v[68:71], a[132:135], v[172:175], v[68:71]\n"
    "ds_read_b64_tr_b16 a[156:157], v242 offset:17248\n"
    "buffer_load_dword v254, s[40:43], s70 offen lds\n"
    "v_mfma_f32_16x16x32_bf16 v[68:71], a[128:131], v[164:167], v[68:71]\n"
    "s_waitcnt lgkmcnt(7)\n"
    "ds_read_b64_tr_b16 a[158:159], v242 offset:17760\n"
    "v_fma_f32 v253, v253, v244, v246\n"
    "v_mfma_f32_16x16x32_bf16 v[76:79], a[136:139], v[164:167], v[76:79]\n"
    "ds_read_b64_tr_b16 a[160:161], v242 offset:384\n"
    "v_fma_f32 v251, v251, v134, v130\n"
    "v_mfma_f32_16x16x32_bf16 v[76:79], a[140:143], v[172:175], v[76:79]\n"
    "ds_read_b64_tr_b16 a[162:163], v242 offset:896\n"
    "v_fma_f32 v253, v253, v135, v131\n"
    "v_mfma_f32_16x16x32_bf16 v[72:75], a[140:143], v[168:171], v[72:75]\n"
    "ds_read_b64_tr_b16 a[164:165], v242 offset:17280\n"
    "v_cvt_pk_bf16_f32 v217, v251, v253\n"
    "v_mfma_f32_16x16x32_bf16 v[72:75], a[136:139], v[160:163], v[72:75]\n"
    "s_waitcnt lgkmcnt(7)\n"
    "ds_read_b64_tr_b16 a[166:167], v242 offset:17792\n"
    "v_lshlrev_b32 v251, 16, v220\n"
    "v_mfma_f32_16x16x32_bf16 v[80:83], a[144:147], v[160:163], v[80:83]\n"
    "ds_read_b64_tr_b16 a[168:169], v242 offset:416\n"
    "v_and_b32 v253, s53, v220\n"
    "v_mfma_f32_16x16x32_bf16 v[80:83], a[148:151], v[168:171], v[80:83]\n"
    "ds_read_b64_tr_b16 a[170:171], v242 offset:928\n"
    "v_fma_f32 v251, v251, v245, v247\n"
    "v_mfma_f32_16x16x32_bf16 v[84:87], a[148:151], v[172:175], v[84:87]\n"
    "ds_read_b64_tr_b16 a[172:173], v242 offset:17312\n"
    "v_fma_f32 v253, v253, v245, v247\n"
    "v_mfma_f32_16x16x32_bf16 v[84:87], a[144:147], v[164:167], v[84:87]\n"
    "s_waitcnt lgkmcnt(7)\n"
    "ds_read_b64_tr_b16 a[174:175], v242 offset:17824\n"
    "v_fma_f32 v251, v251, v132, v128\n"
    "v_mfma_f32_16x16x32_bf16 v[92:95], a[152:155], v[164:167], v[92:95]\n"
    "ds_read_b64_tr_b16 a[176:177], v242 offset:448\n"
    "v_fma_f32 v253, v253, v133, v129\n"
    "v_mfma_f32_16x16x32_bf16 v[92:95], a[156:159], v[172:175], v[92:95]\n"
    "ds_read_b64_tr_b16 a[178:179], v242 offset:960\n"
    "v_cvt_pk_bf16_f32 v220, v251, v253\n"
    "v_mfma_f32_16x16x32_bf16 v[88:91], a[156:159], v[168:171], v[88:91]\n"
    "ds_read_b64_tr_b16 a[180:181], v242 offset:17344\n"
    "v_lshlrev_b32 v251, 16, v221\n"
    "v_mfma_f32_16x16x32_bf16 v[88:91], a[152:155], v[160:163], v[88:91]\n"
    "s_waitcnt lgkmcnt(7)\n"
    "ds_read_b64_tr_b16 a[182:183], v242 offset:17856\n"
    "v_and_b32 v253, s53, v221\n"
    "v_mfma_f32_16x16x32_bf16 v[96:99], a[160:163], v[160:163], v[96:99]\n"
    "ds_read_b64_tr_b16 a[184:185], v242 offset:480\n"
    "v_fma_f32 v251, v251, v245, v247\n"
    "v_mfma_f32_16x16x32_bf16 v[96:99], a[164:167], v[168:171], v[96:99]\n"
    "ds_read_b64_tr_b16 a[186:187], v242 offset:992\n"
    "v_fma_f32 v253, v253, v245, v247\n"
    "v_mfma_f32_16x16x32_bf16 v[100:103], a[164:167], v[172:175], v[100:103]\n"
    "s_waitcnt vmcnt(11)\n"
    "s_barrier\n"
    "v_mfma_f32_16x16x32_bf16 v[100:103], a[160:163], v[164:167], v[100:103]\n"
    "s_waitcnt lgkmcnt(6)\n"
    "ds_read_b64_tr_b16 a[188:189], v242 offset:17376\n"
    "ds_read_b64_tr_b16 a[190:191], v242 offset:17888\n"
    "v_mfma_f32_16x16x32_bf16 v[108:111], a[168:171], v[164:167], v[108:111]\n"
    "ds_read_b64_tr_b16 a[128:129], v240 offset:0\n"
    "v_fma_f32 v251, v251, v134, v130\n"
    "v_mfma_f32_16x16x32_bf16 v[108:111], a[172:175], v[172:175], v[108:111]\n"
    "ds_read_b64_tr_b16 a[130:131], v240 offset:512\n"
    "v_fma_f32 v253, v253, v135, v131\n"
    "v_mfma_f32_16x16x32_bf16 v[104:107], a[172:175], v[168:171], v[104:107]\n"
    "ds_read_b64_tr_b16 a[132:133], v240 offset:16896\n"
    "v_cvt_pk_bf16_f32 v221, v251, v253\n"
    "v_mfma_f32_16x16x32_bf16 v[104:107], a[168:171], v[160:163], v[104:107]\n"
    "s_waitcnt lgkmcnt(7)\n"
    "ds_read_b64_tr_b16 a[134:135], v240 offset:17408\n"
    "ds_read_b128 v[128:131], v243 offset:1536\n"
    "v_mfma_f32_16x16x32_bf16 v[112:115], a[176:179], v[160:163], v[112:115]\n"
    "ds_read_b64_tr_b16 a[136:137], v240 offset:32\n"
    "ds_read_b128 v[132:135], v243 offset:3328\n"
    "v_mfma_f32_16x16x32_bf16 v[112:115], a[180:183], v[168:171], v[112:115]\n"
    "ds_read_b64_tr_b16 a[138:139], v240 offset:544\n"
    "v_lshlrev_b32 v251, 16, v218\n"
    "v_mfma_f32_16x16x32_bf16 v[116:119], a[180:183], v[172:175], v[116:119]\n"
    "ds_read_b64_tr_b16 a[140:141], v240 offset:16928\n"
    "v_and_b32 v253, s53, v218\n"
    "v_mfma_f32_16x16x32_bf16 v[116:119], a[176:179], v[164:167], v[116:119]\n"
    "s_waitcnt lgkmcnt(9)\n"
    "ds_read_b64_tr_b16 a[142:143], v240 offset:17440\n"
    "v_fma_f32 v251, v251, v244, v246\n"
    "v_mfma_f32_16x16x32_bf16 v[124:127], a[184:187], v[164:167], v[124:127]\n"
    "ds_read_b64_tr_b16 a[144:145], v240 offset:64\n"
    "v_fma_f32 v253, v253, v244, v246\n"
    "v_mfma_f32_16x16x32_bf16 v[124:127], a[188:191], v[172:175], v[124:127]\n"
    "ds_read_b64_tr_b16 a[146:147], v240 offset:576\n"
    "v_fma_f32 v251, v251, v140, v136\n"
    "v_mfma_f32_16x16x32_bf16 v[120:123], a[188:191], v[168:171], v[120:123]\n"
    "ds_read_b64_tr_b16 a[148:149], v240 offset:16960\n"
    "v_fma_f32 v253, v253, v141, v137\n"
    "v_mfma_f32_16x16x32_bf16 v[120:123], a[184:187], v[160:163], v[120:123]\n"
    "s_waitcnt lgkmcnt(9)\n"
    "ds_read_b64_tr_b16 a[150:151], v240 offset:17472\n"
    "v_cvt_pk_bf16_f32 v218, v251, v253\n"
    "v_mfma_f32_16x16x32_bf16 v[0:3], a[128:131], v[176:179], v[0:3]\n"
    "s_add_u32 m0, s48, 67584\n"
    "ds_read_b64_tr_b16 a[152:153], v240 offset:96\n"
    "v_lshlrev_b32 v251, 16, v219\n"
    "v_mfma_f32_16x16x32_bf16 v[0:3], a[132:135], v[184:187], v[0:3]\n"
    "s_add_u32 s68, s68, 434176\n"
    "ds_read_b64_tr_b16 a[154:155], v240 offset:608\n"
    "v_and_b32 v253, s53, v219\n"
    "v_mfma_f32_16x16x32_bf16 v[4:7], a[132:135], v[188:191], v[4:7]\n"
    "ds_read_b64_tr_b16 a[156:157], v240 offset:16992\n"
    "buffer_load_dwordx4 v255, s[56:59], s68 offen lds\n"
    "v_mfma_f32_16x16x32_bf16 v[4:7], a[128:131], v[180:183], v[4:7]\n"
    "s_waitcnt lgkmcnt(7)\n"
    "ds_read_b64_tr_b16 a[158:159], v240 offset:17504\n"
    "v_fma_f32 v251, v251, v244, v246\n"
    "v_mfma_f32_16x16x32_bf16 v[12:15], a[136:139], v[180:183], v[12:15]\n"
    "s_add_u32 m0, s48, 68640\n"
    "ds_read_b64_tr_b16 a[160:161], v240 offset:128\n"
    "v_fma_f32 v253, v253, v244, v246\n"
    "v_mfma_f32_16x16x32_bf16 v[12:15], a[140:143], v[188:191], v[12:15]\n"
    "s_add_u32 s68, s68, 8192\n"
    "ds_read_b64_tr_b16 a[162:163], v240 offset:640\n"
    "v_fma_f32 v251, v251, v142, v138\n"
    "v_mfma_f32_16x16x32_bf16 v[8:11], a[140:143], v[184:187], v[8:11]\n"
    "ds_read_b64_tr_b16 a[164:165], v240 offset:17024\n"
    "buffer_load_dwordx4 v255, s[56:59], s68 offen lds\n"
    "v_mfma_f32_16x16x32_bf16 v[8:11], a[136:139], v[176:179], v[8:11]\n"
    "s_waitcnt lgkmcnt(7)\n"
    "ds_read_b64_tr_b16 a[166:167], v240 offset:17536\n"
    "v_fma_f32 v253, v253, v143, v139\n"
    "v_mfma_f32_16x16x32_bf16 v[16:19], a[144:147], v[176:179], v[16:19]\n"
    "s_add_u32 m0, s48, 69696\n"
    "ds_read_b64_tr_b16 a[168:169], v240 offset:160\n"
    "v_cvt_pk_bf16_f32 v219, v251, v253\n"
    "v_mfma_f32_16x16x32_bf16 v[16:19], a[148:151], v[184:187], v[16:19]\n"
    "s_add_u32 s68, s68, 8192\n"
    "ds_read_b64_tr_b16 a[170:171], v240 offset:672\n"
    "v_lshlrev_b32 v251, 16, v222\n"
    "v_mfma_f32_16x16x32_bf16 v[20:23], a[148:151], v[188:191], v[20:23]\n"
    "ds_read_b64_tr_b16 a[172:173], v240 offset:17056\n"
    "buffer_load_dwordx4 v255, s[56:59], s68 offen lds\n"
    "v_mfma_f32_16x16x32_bf16 v[20:23], a[144:147], v[180:183], v[20:23]\n"
    "s_waitcnt lgkmcnt(7)\n"
    "ds_read_b64_tr_b16 a[174:175], v240 offset:17568\n"
    "v_and_b32 v253, s53, v222\n"
    "v_mfma_f32_16x16x32_bf16 v[28:31], a[152:155], v[180:183], v[28:31]\n"
    "s_add_u32 m0, s48, 70752\n"
    "ds_read_b64_tr_b16 a[176:177], v240 offset:192\n"
    "v_fma_f32 v251, v251, v245, v247\n"
    "v_mfma_f32_16x16x32_bf16 v[28:31], a[156:159], v[188:191], v[28:31]\n"
    "s_add_u32 s68, s68, 8192\n"
    "ds_read_b64_tr_b16 a[178:179], v240 offset:704\n"
    "v_fma_f32 v253, v253, v245, v247\n"
    "v_mfma_f32_16x16x32_bf16 v[24:27], a[156:159], v[184:187], v[24:27]\n"
    "ds_read_b64_tr_b16 a[180:181], v240 offset:17088\n"
    "buffer_load_dwordx4 v255, s[56:59], s68 offen lds\n"
    "v_mfma_f32_16x16x32_bf16 v[24:27], a[152:155], v[176:179], v[24:27]\n"
    "s_waitcnt lgkmcnt(7)\n"
    "ds_read_b64_tr_b16 a[182:183], v240 offset:17600\n"
    "v_fma_f32 v251, v251, v140, v136\n"
    "v_mfma_f32_16x16x32_bf16 v[32:35], a[160:163], v[176:179], v[32:35]\n"
    "s_add_u32 m0, s48, 71808\n"
    "ds_read_b64_tr_b16 a[184:185], v240 offset:224\n"
    "v_fma_f32 v253, v253, v141, v137\n"
    "v_mfma_f32_16x16x32_bf16 v[32:35], a[164:167], v[184:187], v[32:35]\n"
    "s_add_u32 s68, s68, 40960\n"
    "ds_read_b64_tr_b16 a[186:187], v240 offset:736\n"
    "v_cvt_pk_bf16_f32 v222, v251, v253\n"
    "v_mfma_f32_16x16x32_bf16 v[36:39], a[164:167], v[188:191], v[36:39]\n"
    "ds_read_b64_tr_b16 a[188:189], v240 offset:17120\n"
    "buffer_load_dwordx4 v255, s[56:59], s68 offen lds\n"
    "v_mfma_f32_16x16x32_bf16 v[36:39], a[160:163], v[180:183], v[36:39]\n"
    "s_waitcnt lgkmcnt(7)\n"
    "ds_read_b64_tr_b16 a[190:191], v240 offset:17632\n"
    "v_lshlrev_b32 v251, 16, v223\n"
    "v_mfma_f32_16x16x32_bf16 v[44:47], a[168:171], v[180:183], v[44:47]\n"
    "s_add_u32 m0, s48, 72864\n"
    "ds_read_b64_tr_b16 a[128:129], v240 offset:256\n"
    "v_and_b32 v253, s53, v223\n"
    "v_mfma_f32_16x16x32_bf16 v[44:47], a[172:175], v[188:191], v[44:47]\n"
    "s_add_u32 s68, s68, 8192\n"
    "ds_read_b64_tr_b16 a[130:131], v240 offset:768\n"
    "v_fma_f32 v251, v251, v245, v247\n"
    "v_mfma_f32_16x16x32_bf16 v[40:43], a[172:175], v[184:187], v[40:43]\n"
    "ds_read_b64_tr_b16 a[132:133], v240 offset:17152\n"
    "buffer_load_dwordx4 v255, s[56:59], s68 offen lds\n"
    "v_mfma_f32_16x16x32_bf16 v[40:43], a[168:171], v[176:179], v[40:43]\n"
    "s_waitcnt lgkmcnt(7)\n"
    "ds_read_b64_tr_b16 a[134:135], v240 offset:17664\n"
    "v_fma_f32 v253, v253, v245, v247\n"
    "v_mfma_f32_16x16x32_bf16 v[48:51], a[176:179], v[176:179], v[48:51]\n"
    "s_add_u32 m0, s48, 73920\n"
    "ds_read_b64_tr_b16 a[136:137], v240 offset:288\n"
    "v_fma_f32 v251, v251, v142, v138\n"
    "v_mfma_f32_16x16x32_bf16 v[48:51], a[180:183], v[184:187], v[48:51]\n"
    "s_add_u32 s68, s68, 8192\n"
    "ds_read_b64_tr_b16 a[138:139], v240 offset:800\n"
    "v_fma_f32 v253, v253, v143, v139\n"
    "v_mfma_f32_16x16x32_bf16 v[52:55], a[180:183], v[188:191], v[52:55]\n"
    "ds_read_b64_tr_b16 a[140:141], v240 offset:17184\n"
    "buffer_load_dwordx4 v255, s[56:59], s68 offen lds\n"
    "v_mfma_f32_16x16x32_bf16 v[52:55], a[176:179], v[180:183], v[52:55]\n"
    "s_waitcnt lgkmcnt(7)\n"
    "ds_read_b64_tr_b16 a[142:143], v240 offset:17696\n"
    "v_cvt_pk_bf16_f32 v223, v251, v253\n"
    "v_mfma_f32_16x16x32_bf16 v[60:63], a[184:187], v[180:183], v[60:63]\n"
    "s_add_u32 m0, s48, 74976\n"
    "ds_read_b64_tr_b16 a[144:145], v240 offset:320\n"
    "ds_read_b128 v[136:139], v243 offset:1552\n"
    "v_mfma_f32_16x16x32_bf16 v[60:63], a[188:191], v[188:191], v[60:63]\n"
    "s_add_u32 s68, s68, 8192\n"
    "ds_read_b64_tr_b16 a[146:147], v240 offset:832\n"
    "ds_read_b128 v[140:143], v243 offset:3344\n"
    "v_mfma_f32_16x16x32_bf16 v[56:59], a[188:191], v[184:187], v[56:59]\n"
    "ds_read_b64_tr_b16 a[148:149], v240 offset:17216\n"
    "buffer_load_dwordx4 v255, s[56:59], s68 offen lds\n"
    "v_mfma_f32_16x16x32_bf16 v[56:59], a[184:187], v[176:179], v[56:59]\n"
    "s_waitcnt lgkmcnt(9)\n"
    "ds_read_b64_tr_b16 a[150:151], v240 offset:17728\n"
    "v_lshlrev_b32 v251, 16, v224\n"
    "v_mfma_f32_16x16x32_bf16 v[64:67], a[128:131], v[176:179], v[64:67]\n"
    "ds_read_b64_tr_b16 a[152:153], v240 offset:352\n"
    "v_and_b32 v253, s53, v224\n"
    "v_mfma_f32_16x16x32_bf16 v[64:67], a[132:135], v[184:187], v[64:67]\n"
    "ds_read_b64_tr_b16 a[154:155], v240 offset:864\n"
    "v_fma_f32 v251, v251, v244, v246\n"
    "v_mfma_f32_16x16x32_bf16 v[68:71], a[132:135], v[188:191], v[68:71]\n"
    "ds_read_b64_tr_b16 a[156:157], v240 offset:17248\n"
    "v_fma_f32 v253, v253, v244, v246\n"
    "v_mfma_f32_16x16x32_bf16 v[68:71], a[128:131], v[180:183], v[68:71]\n"
    "s_waitcnt lgkmcnt(9)\n"
    "ds_read_b64_tr_b16 a[158:159], v240 offset:17760\n"
    "v_mfma_f32_16x16x32_bf16 v[76:79], a[136:139], v[180:183], v[76:79]\n"
    "ds_read_b64_tr_b16 a[160:161], v240 offset:384\n"
    "v_fma_f32 v251, v251, v132, v128\n"
    "v_mfma_f32_16x16x32_bf16 v[76:79], a[140:143], v[188:191], v[76:79]\n"
    "ds_read_b64_tr_b16 a[162:163], v240 offset:896\n"
    "v_fma_f32 v253, v253, v133, v129\n"
    "v_mfma_f32_16x16x32_bf16 v[72:75], a[140:143], v[184:187], v[72:75]\n"
    "ds_read_b64_tr_b16 a[164:165], v240 offset:17280\n"
    "v_cvt_pk_bf16_f32 v224, v251, v253\n"
    "v_mfma_f32_16x16x32_bf16 v[72:75], a[136:139], v[176:179], v[72:75]\n"
    "s_waitcnt lgkmcnt(7)\n"
    "ds_read_b64_tr_b16 a[166:167], v240 offset:17792\n"
    "v_lshlrev_b32 v251, 16, v225\n"
    "v_mfma_f32_16x16x32_bf16 v[80:83], a[144:147], v[176:179], v[80:83]\n"
    "ds_read_b64_tr_b16 a[168:169], v240 offset:416\n"
    "v_and_b32 v253, s53, v225\n"
    "v_mfma_f32_16x16x32_bf16 v[80:83], a[148:151], v[184:187], v[80:83]\n"
    "ds_read_b64_tr_b16 a[170:171], v240 offset:928\n"
    "v_fma_f32 v251, v251, v244, v246\n"
    "v_mfma_f32_16x16x32_bf16 v[84:87], a[148:151], v[188:191], v[84:87]\n"
    "ds_read_b64_tr_b16 a[172:173], v240 offset:17312\n"
    "v_fma_f32 v253, v253, v244, v246\n"
    "v_mfma_f32_16x16x32_bf16 v[84:87], a[144:147], v[180:183], v[84:87]\n"
    "s_waitcnt lgkmcnt(7)\n"
    "ds_read_b64_tr_b16 a[174:175], v240 offset:17824\n"
    "v_fma_f32 v251, v251, v134, v130\n"
    "v_mfma_f32_16x16x32_bf16 v[92:95], a[152:155], v[180:183], v[92:95]\n"
    "ds_read_b64_tr_b16 a[176:177], v240 offset:448\n"
    "v_fma_f32 v253, v253, v135, v131\n"
    "v_mfma_f32_16x16x32_bf16 v[92:95], a[156:159], v[188:191], v[92:95]\n"
    "ds_read_b64_tr_b16 a[178:179], v240 offset:960\n"
    "v_cvt_pk_bf16_f32 v225, v251, v253\n"
    "v_mfma_f32_16x16x32_bf16 v[88:91], a[156:159], v[184:187], v[88:91]\n"
    "ds_read_b64_tr_b16 a[180:181], v240 offset:17344\n"
    "v_lshlrev_b32 v251, 16, v228\n"
    "v_mfma_f32_16x16x32_bf16 v[88:91], a[152:155], v[176:179], v[88:91]\n"
    "s_waitcnt lgkmcnt(7)\n"
    "ds_read_b64_tr_b16 a[182:183], v240 offset:17856\n"
    "v_and_b32 v253, s53, v228\n"
    "v_mfma_f32_16x16x32_bf16 v[96:99], a[160:163], v[176:179], v[96:99]\n"
    "ds_read_b64_tr_b16 a[184:185], v240 offset:480\n"
    "v_fma_f32 v251, v251, v245, v247\n"
    "v_mfma_f32_16x16x32_bf16 v[96:99], a[164:167], v[184:187], v[96:99]\n"
    "ds_read_b64_tr_b16 a[186:187], v240 offset:992\n"
    "v_fma_f32 v253, v253, v245, v247\n"
    "v_mfma_f32_16x16x32_bf16 v[100:103], a[164:167], v[188:191], v[100:103]\n"
    "s_waitcnt vmcnt(9)\n"
    "s_barrier\n"
    "v_mfma_f32_16x16x32_bf16 v[100:103], a[160:163], v[180:183], v[100:103]\n"
    "s_waitcnt lgkmcnt(6)\n"
    "ds_read_b64_tr_b16 a[188:189], v240 offset:17376\n"
    "ds_read_b64_tr_b16 a[190:191], v240 offset:17888\n"
    "v_mfma_f32_16x16x32_bf16 v[108:111], a[168:171], v[180:183], v[108:111]\n"
    "ds_read_b64_tr_b16 a[128:129], v240 offset:33792\n"
    "v_fma_f32 v251, v251, v132, v128\n"
    "v_mfma_f32_16x16x32_bf16 v[108:111], a[172:175], v[188:191], v[108:111]\n"
    "ds_read_b64_tr_b16 a[130:131], v240 offset:34304\n"
    "v_fma_f32 v253, v253, v133, v129\n"
    "v_mfma_f32_16x16x32_bf16 v[104:107], a[172:175], v[184:187], v[104:107]\n"
    "ds_read_b64_tr_b16 a[132:133], v240 offset:50688\n"
    "v_cvt_pk_bf16_f32 v228, v251, v253\n"
    "v_mfma_f32_16x16x32_bf16 v[104:107], a[168:171], v[176:179], v[104:107]\n"
    "s_waitcnt lgkmcnt(7)\n"
    "ds_read_b64_tr_b16 a[134:135], v240 offset:51200\n"
    "v_lshlrev_b32 v251, 16, v229\n"
    "v_mfma_f32_16x16x32_bf16 v[112:115], a[176:179], v[176:179], v[112:115]\n"
    "ds_read_b64_tr_b16 a[136:137], v240 offset:33824\n"
    "v_and_b32 v253, s53, v229\n"
    "v_mfma_f32_16x16x32_bf16 v[112:115], a[180:183], v[184:187], v[112:115]\n"
    "ds_read_b64_tr_b16 a[138:139], v240 offset:34336\n"
    "v_fma_f32 v251, v251, v245, v247\n"
    "v_mfma_f32_16x16x32_bf16 v[116:119], a[180:183], v[188:191], v[116:119]\n"
    "ds_read_b64_tr_b16 a[140:141], v240 offset:50720\n"
    "v_fma_f32 v253, v253, v245, v247\n"
    "v_mfma_f32_16x16x32_bf16 v[116:119], a[176:179], v[180:183], v[116:119]\n"
    "s_waitcnt lgkmcnt(7)\n"
    "ds_read_b64_tr_b16 a[142:143], v240 offset:51232\n"
    "v_fma_f32 v251, v251, v134, v130\n"
    "v_mfma_f32_16x16x32_bf16 v[124:127], a[184:187], v[180:183], v[124:127]\n"
    "ds_read_b64_tr_b16 a[144:145], v240 offset:33856\n"
    "v_fma_f32 v253, v253, v135, v131\n"
    "v_mfma_f32_16x16x32_bf16 v[124:127], a[188:191], v[188:191], v[124:127]\n"
    "ds_read_b64_tr_b16 a[146:147], v240 offset:34368\n"
    "v_cvt_pk_bf16_f32 v229, v251, v253\n"
    "v_mfma_f32_16x16x32_bf16 v[120:123], a[188:191], v[184:187], v[120:123]\n"
    "ds_read_b64_tr_b16 a[148:149], v240 offset:50752\n"
    "ds_read_b128 v[128:131], v243 offset:1664\n"
    "v_mfma_f32_16x16x32_bf16 v[120:123], a[184:187], v[176:179], v[120:123]\n"
    "s_waitcnt lgkmcnt(8)\n"
    "ds_read_b64_tr_b16 a[150:151], v240 offset:51264\n"
    "ds_read_b128 v[132:135], v243 offset:3456\n"
    "v_mfma_f32_16x16x32_bf16 v[0:3], a[128:131], v[192:195], v[0:3]\n"
    "s_add_u32 m0, s48, 130048\n"
    "ds_read_b64_tr_b16 a[152:153], v240 offset:33888\n"
    "v_lshlrev_b32 v251, 16, v226\n"
    "v_mfma_f32_16x16x32_bf16 v[0:3], a[132:135], v[200:203], v[0:3]\n"
    "s_add_u32 s68, s68, 434176\n"
    "ds_read_b64_tr_b16 a[154:155], v240 offset:34400\n"
    "v_and_b32 v253, s53, v226\n"
    "v_mfma_f32_16x16x32_bf16 v[4:7], a[132:135], v[204:207], v[4:7]\n"
    "ds_read_b64_tr_b16 a[156:157], v240 offset:50784\n"
    "buffer_load_dwordx4 v255, s[56:59], s68 offen lds\n"
    "v_mfma_f32_16x16x32_bf16 v[4:7], a[128:131], v[196:199], v[4:7]\n"
    "s_waitcnt lgkmcnt(9)\n"
    "ds_read_b64_tr_b16 a[158:159], v240 offset:51296\n"
    "v_fma_f32 v251, v251, v244, v246\n"
    "v_mfma_f32_16x16x32_bf16 v[12:15], a[136:139], v[196:199], v[12:15]\n"
    "s_add_u32 m0, s48, 131104\n"
    "ds_read_b64_tr_b16 a[160:161], v240 offset:33920\n"
    "v_fma_f32 v253, v253, v244, v246\n"
    "v_mfma_f32_16x16x32_bf16 v[12:15], a[140:143], v[204:207], v[12:15]\n"
    "s_add_u32 s68, s68, 8192\n"
    "ds_read_b64_tr_b16 a[162:163], v240 offset:34432\n"
    "v_mfma_f32_16x16x32_bf16 v[8:11], a[140:143], v[200:203], v[8:11]\n"
    "ds_read_b64_tr_b16 a[164:165], v240 offset:50816\n"
    "buffer_load_dwordx4 v255, s[56:59], s68 offen lds\n"
    "v_mfma_f32_16x16x32_bf16 v[8:11], a[136:139], v[192:195], v[8:11]\n"
    "s_waitcnt lgkmcnt(8)\n"
    "ds_read_b64_tr_b16 a[166:167], v240 offset:51328\n"
    "v_fma_f32 v251, v251, v140, v136\n"
    "v_mfma_f32_16x16x32_bf16 v[16:19], a[144:147], v[192:195], v[16:19]\n"
    "s_add_u32 m0, s48, 132160\n"
    "ds_read_b64_tr_b16 a[168:169], v240 offset:33952\n"
    "v_fma_f32 v253, v253, v141, v137\n"
    "v_mfma_f32_16x16x32_bf16 v[16:19], a[148:151], v[200:203], v[16:19]\n"
    "s_add_u32 s68, s68, 8192\n"
    "ds_read_b64_tr_b16 a[170:171], v240 offset:34464\n"
    "v_cvt_pk_bf16_f32 v226, v251, v253\n"
    "v_mfma_f32_16x16x32_bf16 v[20:23], a[148:151], v[204:207], v[20:23]\n"
    "ds_read_b64_tr_b16 a[172:173], v240 offset:50848\n"
    "buffer_load_dwordx4 v255, s[56:59], s68 offen lds\n"
    "v_mfma_f32_16x16x32_bf16 v[20:23], a[144:147], v[196:199], v[20:23]\n"
    "s_waitcnt lgkmcnt(7)\n"
    "ds_read_b64_tr_b16 a[174:175], v240 offset:51360\n"
    "v_lshlrev_b32 v251, 16, v227\n"
    "v_mfma_f32_16x16x32_bf16 v[28:31], a[152:155], v[196:199], v[28:31]\n"
    "s_add_u32 m0, s48, 133216\n"
    "ds_read_b64_tr_b16 a[176:177], v240 offset:33984\n"
    "v_and_b32 v253, s53, v227\n"
    "v_mfma_f32_16x16x32_bf16 v[28:31], a[156:159], v[204:207], v[28:31]\n"
    "s_add_u32 s68, s68, 8192\n"
    "ds_read_b64_tr_b16 a[178:179], v240 offset:34496\n"
    "v_fma_f32 v251, v251, v244, v246\n"
    "v_mfma_f32_16x16x32_bf16 v[24:27], a[156:159], v[200:203], v[24:27]\n"
    "ds_read_b64_tr_b16 a[180:181], v240 offset:50880\n"
    "buffer_load_dwordx4 v255, s[56:59], s68 offen lds\n"
    "v_mfma_f32_16x16x32_bf16 v[24:27], a[152:155], v[192:195], v[24:27]\n"
    "s_waitcnt lgkmcnt(7)\n"
    "ds_read_b64_tr_b16 a[182:183], v240 offset:51392\n"
    "v_fma_f32 v253, v253, v244, v246\n"
    "v_mfma_f32_16x16x32_bf16 v[32:35], a[160:163], v[192:195], v[32:35]\n"
    "s_add_u32 m0, s48, 134272\n"
    "ds_read_b64_tr_b16 a[184:185], v240 offset:34016\n"
    "v_fma_f32 v251, v251, v142, v138\n"
    "v_mfma_f32_16x16x32_bf16 v[32:35], a[164:167], v[200:203], v[32:35]\n"
    "s_add_u32 s68, s68, 40960\n"
    "ds_read_b64_tr_b16 a[186:187], v240 offset:34528\n"
    "v_fma_f32 v253, v253, v143, v139\n"
    "v_mfma_f32_16x16x32_bf16 v[36:39], a[164:167], v[204:207], v[36:39]\n"
    "ds_read_b64_tr_b16 a[188:189], v240 offset:50912\n"
    "buffer_load_dwordx4 v255, s[56:59], s68 offen lds\n"
    "v_mfma_f32_16x16x32_bf16 v[36:39], a[160:163], v[196:199], v[36:39]\n"
    "s_waitcnt lgkmcnt(7)\n"
    "ds_read_b64_tr_b16 a[190:191], v240 offset:51424\n"
    "v_cvt_pk_bf16_f32 v227, v251, v253\n"
    "v_mfma_f32_16x16x32_bf16 v[44:47], a[168:171], v[196:199], v[44:47]\n"
    "s_add_u32 m0, s48, 135328\n"
    "ds_read_b64_tr_b16 a[128:129], v240 offset:34048\n"
    "v_lshlrev_b32 v251, 16, v230\n"
    "v_mfma_f32_16x16x32_bf16 v[44:47], a[172:175], v[204:207], v[44:47]\n"
    "s_add_u32 s68, s68, 8192\n"
    "ds_read_b64_tr_b16 a[130:131], v240 offset:34560\n"
    "v_and_b32 v253, s53, v230\n"
    "v_mfma_f32_16x16x32_bf16 v[40:43], a[172:175], v[200:203], v[40:43]\n"
    "ds_read_b64_tr_b16 a[132:133], v240 offset:50944\n"
    "buffer_load_dwordx4 v255, s[56:59], s68 offen lds\n"
    "v_mfma_f32_16x16x32_bf16 v[40:43], a[168:171], v[192:195], v[40:43]\n"
    "s_waitcnt lgkmcnt(7)\n"
    "ds_read_b64_tr_b16 a[134:135], v240 offset:51456\n"
    "v_fma_f32 v251, v251, v245, v247\n"
    "v_mfma_f32_16x16x32_bf16 v[48:51], a[176:179], v[192:195], v[48:51]\n"
    "s_add_u32 m0, s48, 136384\n"
    "ds_read_b64_tr_b16 a[136:137], v240 offset:34080\n"
    "v_fma_f32 v253, v253, v245, v247\n"
    "v_mfma_f32_16x16x32_bf16 v[48:51], a[180:183], v[200:203], v[48:51]\n"
    "s_add_u32 s68, s68, 8192\n"
    "ds_read_b64_tr_b16 a[138:139], v240 offset:34592\n"
    "v_fma_f32 v251, v251, v140, v136\n"
    "v_mfma_f32_16x16x32_bf16 v[52:55], a[180:183], v[204:207], v[52:55]\n"
    "ds_read_b64_tr_b16 a[140:141], v240 offset:50976\n"
    "buffer_load_dwordx4 v255, s[56:59], s68 offen lds\n"
    "v_mfma_f32_16x16x32_bf16 v[52:55], a[176:179], v[196:199], v[52:55]\n"
    "s_waitcnt lgkmcnt(7)\n"
    "ds_read_b64_tr_b16 a[142:143], v240 offset:51488\n"
    "v_fma_f32 v253, v253, v141, v137\n"
    "v_mfma_f32_16x16x32_bf16 v[60:63], a[184:187], v[196:199], v[60:63]\n"
    "s_add_u32 m0, s48, 137440\n"
    "ds_read_b64_tr_b16 a[144:145], v240 offset:34112\n"
    "v_cvt_pk_bf16_f32 v230, v251, v253\n"
    "v_mfma_f32_16x16x32_bf16 v[60:63], a[188:191], v[204:207], v[60:63]\n"
    "s_add_u32 s68, s68, 8192\n"
    "ds_read_b64_tr_b16 a[146:147], v240 offset:34624\n"
    "v_lshlrev_b32 v251, 16, v231\n"
    "v_mfma_f32_16x16x32_bf16 v[56:59], a[188:191], v[200:203], v[56:59]\n"
    "ds_read_b64_tr_b16 a[148:149], v240 offset:51008\n"
    "buffer_load_dwordx4 v255, s[56:59], s68 offen lds\n"
    "v_mfma_f32_16x16x32_bf16 v[56:59], a[184:187], v[192:195], v[56:59]\n"
    "s_waitcnt lgkmcnt(7)\n"
    "ds_read_b64_tr_b16 a[150:151], v240 offset:51520\n"
    "v_and_b32 v253, s53, v231\n"
    "v_mfma_f32_16x16x32_bf16 v[64:67], a[128:131], v[192:195], v[64:67]\n"
    "ds_read_b64_tr_b16 a[152:153], v240 offset:34144\n"
    "v_fma_f32 v251, v251, v245, v247\n"
    "v_mfma_f32_16x16x32_bf16 v[64:67], a[132:135], v[200:203], v[64:67]\n"
    "ds_read_b64_tr_b16 a[154:155], v240 offset:34656\n"
    "v_fma_f32 v253, v253, v245, v247\n"
    "v_mfma_f32_16x16x32_bf16 v[68:71], a[132:135], v[204:207], v[68:71]\n"
    "ds_read_b64_tr_b16 a[156:157], v240 offset:51040\n"
    "v_fma_f32 v251, v251, v142, v138\n"
    "v_mfma_f32_16x16x32_bf16 v[68:71], a[128:131], v[196:199], v[68:71]\n"
    "s_waitcnt lgkmcnt(7)\n"
    "ds_read_b64_tr_b16 a[158:159], v240 offset:51552\n"
    "v_fma_f32 v253, v253, v143, v139\n"
    "v_mfma_f32_16x16x32_bf16 v[76:79], a[136:139], v[196:199], v[76:79]\n"
    "ds_read_b64_tr_b16 a[160:161], v240 offset:34176\n"
    "v_cvt_pk_bf16_f32 v231, v251, v253\n"
    "v_mfma_f32_16x16x32_bf16 v[76:79], a[140:143], v[204:207], v[76:79]\n"
    "ds_read_b64_tr_b16 a[162:163], v240 offset:34688\n"
    "ds_read_b128 v[136:139], v243 offset:1680\n"
    "v_mfma_f32_16x16x32_bf16 v[72:75], a[140:143], v[200:203], v[72:75]\n"
    "ds_read_b64_tr_b16 a[164:165], v240 offset:51072\n"
    "ds_read_b128 v[140:143], v243 offset:3472\n"
    "v_mfma_f32_16x16x32_bf16 v[72:75], a[136:139], v[192:195], v[72:75]\n"
    "s_waitcnt lgkmcnt(9)\n"
    "ds_read_b64_tr_b16 a[166:167], v240 offset:51584\n"
    "v_lshlrev_b32 v251, 16, v232\n"
    "v_mfma_f32_16x16x32_bf16 v[80:83], a[144:147], v[192:195], v[80:83]\n"
    "ds_read_b64_tr_b16 a[168:169], v240 offset:34208\n"
    "v_and_b32 v253, s53, v232\n"
    "v_mfma_f32_16x16x32_bf16 v[80:83], a[148:151], v[200:203], v[80:83]\n"
    "ds_read_b64_tr_b16 a[170:171], v240 offset:34720\n"
    "v_fma_f32 v251, v251, v244, v246\n"
    "v_mfma_f32_16x16x32_bf16 v[84:87], a[148:151], v[204:207], v[84:87]\n"
    "ds_read_b64_tr_b16 a[172:173], v240 offset:51104\n"
    "v_fma_f32 v253, v253, v244, v246\n"
    "v_mfma_f32_16x16x32_bf16 v[84:87], a[144:147], v[196:199], v[84:87]\n"
    "s_waitcnt lgkmcnt(9)\n"
    "ds_read_b64_tr_b16 a[174:175], v240 offset:51616\n"
    "v_mfma_f32_16x16x32_bf16 v[92:95], a[152:155], v[196:199], v[92:95]\n"
    "ds_read_b64_tr_b16 a[176:177], v240 offset:34240\n"
    "v_fma_f32 v251, v251, v132, v128\n"
    "v_mfma_f32_16x16x32_bf16 v[92:95], a[156:159], v[204:207], v[92:95]\n"
    "ds_read_b64_tr_b16 a[178:179], v240 offset:34752\n"
    "v_fma_f32 v253, v253, v133, v129\n"
    "v_mfma_f32_16x16x32_bf16 v[88:91], a[156:159], v[200:203], v[88:91]\n"
    "ds_read_b64_tr_b16 a[180:181], v240 offset:51136\n"
    "v_cvt_pk_bf16_f32 v232, v251, v253\n"
    "v_mfma_f32_16x16x32_bf16 v[88:91], a[152:155], v[192:195], v[88:91]\n"
    "s_waitcnt lgkmcnt(7)\n"
    "ds_read_b64_tr_b16 a[182:183], v240 offset:51648\n"
    "v_lshlrev_b32 v251, 16, v233\n"
    "v_mfma_f32_16x16x32_bf16 v[96:99], a[160:163], v[192:195], v[96:99]\n"
    "ds_read_b64_tr_b16 a[184:185], v240 offset:34272\n"
    "v_and_b32 v253, s53, v233\n"
    "v_mfma_f32_16x16x32_bf16 v[96:99], a[164:167], v[200:203], v[96:99]\n"
    "ds_read_b64_tr_b16 a[186:187], v240 offset:34784\n"
    "v_fma_f32 v251, v251, v244, v246\n"
    "v_mfma_f32_16x16x32_bf16 v[100:103], a[164:167], v[204:207], v[100:103]\n"
    "s_waitcnt vmcnt(8)\n"
    "s_barrier\n"
    "v_mfma_f32_16x16x32_bf16 v[100:103], a[160:163], v[196:199], v[100:103]\n"
    "s_waitcnt lgkmcnt(6)\n"
    "ds_read_b64_tr_b16 a[188:189], v240 offset:51168\n"
    "ds_read_b64_tr_b16 a[190:191], v240 offset:51680\n"
    "v_mfma_f32_16x16x32_bf16 v[108:111], a[168:171], v[196:199], v[108:111]\n"
    "s_add_u32 m0, s48, 0\n"
    "ds_read_b64_tr_b16 a[128:129], v241 offset:0\n"
    "v_fma_f32 v253, v253, v244, v246\n"
    "v_mfma_f32_16x16x32_bf16 v[108:111], a[172:175], v[204:207], v[108:111]\n"
    "ds_read_b64_tr_b16 a[130:131], v241 offset:512\n"
    "buffer_load_dwordx4 v254, s[40:43], s49 offen offset:0 lds\n"
    "v_mfma_f32_16x16x32_bf16 v[104:107], a[172:175], v[200:203], v[104:107]\n"
    "s_add_u32 m0, s48, 32\n"
    "ds_read_b64_tr_b16 a[132:133], v241 offset:16896\n"
    "v_fma_f32 v251, v251, v134, v130\n"
    "v_mfma_f32_16x16x32_bf16 v[104:107], a[168:171], v[192:195], v[104:107]\n"
    "s_waitcnt lgkmcnt(7)\n"
    "ds_read_b64_tr_b16 a[134:135], v241 offset:17408\n"
    "buffer_load_dwordx4 v254, s[40:43], s49 offen offset:1024 lds\n"
    "v_mfma_f32_16x16x32_bf16 v[112:115], a[176:179], v[192:195], v[112:115]\n"
    "ds_read_b64_tr_b16 a[136:137], v241 offset:32\n"
    "v_fma_f32 v253, v253, v135, v131\n"
    "v_mfma_f32_16x16x32_bf16 v[112:115], a[180:183], v[200:203], v[112:115]\n"
    "s_add_u32 m0, s48, 64\n"
    "ds_read_b64_tr_b16 a[138:139], v241 offset:544\n"
    "v_cvt_pk_bf16_f32 v233, v251, v253\n"
    "v_mfma_f32_16x16x32_bf16 v[116:119], a[180:183], v[204:207], v[116:119]\n"
    "ds_read_b64_tr_b16 a[140:141], v241 offset:16928\n"
    "buffer_load_dwordx4 v254, s[40:43], s49 offen offset:2048 lds\n"
    "v_mfma_f32_16x16x32_bf16 v[116:119], a[176:179], v[196:199], v[116:119]\n"
    "s_waitcnt lgkmcnt(7)\n"
    "ds_read_b64_tr_b16 a[142:143], v241 offset:17440\n"
    "v_lshlrev_b32 v251, 16, v236\n"
    "v_mfma_f32_16x16x32_bf16 v[124:127], a[184:187], v[196:199], v[124:127]\n"
    "s_add_u32 m0, s48, 96\n"
    "ds_read_b64_tr_b16 a[144:145], v241 offset:64\n"
    "v_and_b32 v253, s53, v236\n"
    "v_mfma_f32_16x16x32_bf16 v[124:127], a[188:191], v[204:207], v[124:127]\n"
    "ds_read_b64_tr_b16 a[146:147], v241 offset:576\n"
    "buffer_load_dwordx4 v254, s[40:43], s49 offen offset:3072 lds\n"
    "v_mfma_f32_16x16x32_bf16 v[120:123], a[188:191], v[200:203], v[120:123]\n"
    "ds_read_b64_tr_b16 a[148:149], v241 offset:16960\n"
    "v_fma_f32 v251, v251, v245, v247\n"
    "v_mfma_f32_16x16x32_bf16 v[120:123], a[184:187], v[192:195], v[120:123]\n"
    "s_waitcnt lgkmcnt(7)\n"
    "ds_read_b64_tr_b16 a[150:151], v241 offset:17472\n"
    "v_fma_f32 v253, v253, v245, v247\n"
    "v_mfma_f32_16x16x32_bf16 v[0:3], a[128:131], v[208:211], v[0:3]\n"
    "ds_read_b64_tr_b16 a[152:153], v241 offset:96\n"
    "v_fma_f32 v251, v251, v132, v128\n"
    "v_mfma_f32_16x16x32_bf16 v[0:3], a[132:135], v[216:219], v[0:3]\n"
    "s_nop 0\n"
    "s_barrier\n"
    "v_mfma_f32_16x16x32_bf16 v[4:7], a[132:135], v[220:223], v[4:7]\n"
    "s_add_u32 m0, s48, 4224\n"
    "ds_read_b64_tr_b16 a[154:155], v241 offset:608\n"
    "ds_read_b64_tr_b16 a[156:157], v241 offset:16992\n"
    "v_mfma_f32_16x16x32_bf16 v[4:7], a[128:131], v[212:215], v[4:7]\n"
    "s_waitcnt lgkmcnt(7)\n"
    "ds_read_b64_tr_b16 a[158:159], v241 offset:17504\n"
    "buffer_load_dwordx4 v254, s[40:43], s50 offen offset:0 lds\n"
    "v_mfma_f32_16x16x32_bf16 v[12:15], a[136:139], v[212:215], v[12:15]\n"
    "s_add_u32 m0, s48, 4256\n"
    "ds_read_b64_tr_b16 a[160:161], v241 offset:128\n"
    "v_fma_f32 v253, v253, v133, v129\n"
    "v_mfma_f32_16x16x32_bf16 v[12:15], a[140:143], v[220:223], v[12:15]\n"
    "ds_read_b64_tr_b16 a[162:163], v241 offset:640\n"
    "buffer_load_dwordx4 v254, s[40:43], s50 offen offset:1024 lds\n"
    "v_mfma_f32_16x16x32_bf16 v[8:11], a[140:143], v[216:219], v[8:11]\n"
    "ds_read_b64_tr_b16 a[164:165], v241 offset:17024\n"
    "v_cvt_pk_bf16_f32 v236, v251, v253\n"
    "v_mfma_f32_16x16x32_bf16 v[8:11], a[136:139], v[208:211], v[8:11]\n"
    "s_waitcnt lgkmcnt(7)\n"
    "ds_read_b64_tr_b16 a[166:167], v241 offset:17536\n"
    "v_lshlrev_b32 v251, 16, v237\n"
    "v_mfma_f32_16x16x32_bf16 v[16:19], a[144:147], v[208:211], v[16:19]\n"
    "s_add_u32 m0, s48, 4288\n"
    "ds_read_b64_tr_b16 a[168:169], v241 offset:160\n"
    "v_and_b32 v253, s53, v237\n"
    "v_mfma_f32_16x16x32_bf16 v[16:19], a[148:151], v[216:219], v[16:19]\n"
    "ds_read_b64_tr_b16 a[170:171], v241 offset:672\n"
    "buffer_load_dwordx4 v254, s[40:43], s50 offen offset:2048 lds\n"
    "v_mfma_f32_16x16x32_bf16 v[20:23], a[148:151], v[220:223], v[20:23]\n"
    "s_add_u32 m0, s48, 4320\n"
    "ds_read_b64_tr_b16 a[172:173], v241 offset:17056\n"
    "v_fma_f32 v251, v251, v245, v247\n"
    "v_mfma_f32_16x16x32_bf16 v[20:23], a[144:147], v[212:215], v[20:23]\n"
    "s_waitcnt lgkmcnt(7)\n"
    "ds_read_b64_tr_b16 a[174:175], v241 offset:17568\n"
    "buffer_load_dwordx4 v254, s[40:43], s50 offen offset:3072 lds\n"
    "v_mfma_f32_16x16x32_bf16 v[28:31], a[152:155], v[212:215], v[28:31]\n"
    "s_add_u32 s49, s49, 32768\n"
    "ds_read_b64_tr_b16 a[176:177], v241 offset:192\n"
    "v_fma_f32 v253, v253, v245, v247\n"
    "v_mfma_f32_16x16x32_bf16 v[28:31], a[156:159], v[220:223], v[28:31]\n"
    "s_add_u32 s50, s50, 32768\n"
    "ds_read_b64_tr_b16 a[178:179], v241 offset:704\n"
    "v_fma_f32 v251, v251, v134, v130\n"
    "v_mfma_f32_16x16x32_bf16 v[24:27], a[156:159], v[216:219], v[24:27]\n"
    "s_add_u32 m0, s48, 33792\n"
    "ds_read_b64_tr_b16 a[180:181], v241 offset:17088\n"
    "v_fma_f32 v253, v253, v135, v131\n"
    "v_mfma_f32_16x16x32_bf16 v[24:27], a[152:155], v[208:211], v[24:27]\n"
    "s_waitcnt lgkmcnt(7)\n"
    "ds_read_b64_tr_b16 a[182:183], v241 offset:17600\n"
    "buffer_load_dwordx4 v254, s[40:43], s49 offen offset:0 lds\n"
    "v_mfma_f32_16x16x32_bf16 v[32:35], a[160:163], v[208:211], v[32:35]\n"
    "s_add_u32 m0, s48, 33824\n"
    "ds_read_b64_tr_b16 a[184:185], v241 offset:224\n"
    "v_cvt_pk_bf16_f32 v237, v251, v253\n"
    "v_mfma_f32_16x16x32_bf16 v[32:35], a[164:167], v[216:219], v[32:35]\n"
    "ds_read_b64_tr_b16 a[186:187], v241 offset:736\n"
    "buffer_load_dwordx4 v254, s[40:43], s49 offen offset:1024 lds\n"
    "v_mfma_f32_16x16x32_bf16 v[36:39], a[164:167], v[220:223], v[36:39]\n"
    "ds_read_b64_tr_b16 a[188:189], v241 offset:17120\n"
    "v_lshlrev_b32 v251, 16, v234\n"
    "v_mfma_f32_16x16x32_bf16 v[36:39], a[160:163], v[212:215], v[36:39]\n"
    "s_waitcnt lgkmcnt(7)\n"
    "ds_read_b64_tr_b16 a[190:191], v241 offset:17632\n"
    "v_and_b32 v253, s53, v234\n"
    "v_mfma_f32_16x16x32_bf16 v[44:47], a[168:171], v[212:215], v[44:47]\n"
    "s_add_u32 m0, s48, 33856\n"
    "ds_read_b64_tr_b16 a[128:129], v241 offset:256\n"
    "v_fma_f32 v251, v251, v244, v246\n"
    "v_mfma_f32_16x16x32_bf16 v[44:47], a[172:175], v[220:223], v[44:47]\n"
    "ds_read_b64_tr_b16 a[130:131], v241 offset:768\n"
    "buffer_load_dwordx4 v254, s[40:43], s49 offen offset:2048 lds\n"
    "v_mfma_f32_16x16x32_bf16 v[40:43], a[172:175], v[216:219], v[40:43]\n"
    "s_add_u32 m0, s48, 33888\n"
    "ds_read_b64_tr_b16 a[132:133], v241 offset:17152\n"
    "v_fma_f32 v253, v253, v244, v246\n"
    "v_mfma_f32_16x16x32_bf16 v[40:43], a[168:171], v[208:211], v[40:43]\n"
    "s_waitcnt lgkmcnt(7)\n"
    "ds_read_b64_tr_b16 a[134:135], v241 offset:17664\n"
    "buffer_load_dwordx4 v254, s[40:43], s49 offen offset:3072 lds\n"
    "v_mfma_f32_16x16x32_bf16 v[48:51], a[176:179], v[208:211], v[48:51]\n"
    "ds_read_b64_tr_b16 a[136:137], v241 offset:288\n"
    "v_fma_f32 v251, v251, v140, v136\n"
    "v_mfma_f32_16x16x32_bf16 v[48:51], a[180:183], v[216:219], v[48:51]\n"
    "s_add_u32 m0, s48, 38016\n"
    "ds_read_b64_tr_b16 a[138:139], v241 offset:800\n"
    "v_fma_f32 v253, v253, v141, v137\n"
    "v_mfma_f32_16x16x32_bf16 v[52:55], a[180:183], v[220:223], v[52:55]\n"
    "ds_read_b64_tr_b16 a[140:141], v241 offset:17184\n"
    "buffer_load_dwordx4 v254, s[40:43], s50 offen offset:0 lds\n"
    "v_mfma_f32_16x16x32_bf16 v[52:55], a[176:179], v[212:215], v[52:55]\n"
    "s_waitcnt lgkmcnt(7)\n"
    "ds_read_b64_tr_b16 a[142:143], v241 offset:17696\n"
    "v_cvt_pk_bf16_f32 v234, v251, v253\n"
    "v_mfma_f32_16x16x32_bf16 v[60:63], a[184:187], v[212:215], v[60:63]\n"
    "s_add_u32 m0, s48, 38048\n"
    "ds_read_b64_tr_b16 a[144:145], v241 offset:320\n"
    "v_lshlrev_b32 v251, 16, v235\n"
    "v_mfma_f32_16x16x32_bf16 v[60:63], a[188:191], v[220:223], v[60:63]\n"
    "ds_read_b64_tr_b16 a[146:147], v241 offset:832\n"
    "buffer_load_dwordx4 v254, s[40:43], s50 offen offset:1024 lds\n"
    "v_mfma_f32_16x16x32_bf16 v[56:59], a[188:191], v[216:219], v[56:59]\n"
    "ds_read_b64_tr_b16 a[148:149], v241 offset:17216\n"
    "v_and_b32 v253, s53, v235\n"
    "v_mfma_f32_16x16x32_bf16 v[56:59], a[184:187], v[208:211], v[56:59]\n"
    "s_waitcnt lgkmcnt(7)\n"
    "ds_read_b64_tr_b16 a[150:151], v241 offset:17728\n"
    "v_fma_f32 v251, v251, v244, v246\n"
    "v_mfma_f32_16x16x32_bf16 v[64:67], a[128:131], v[208:211], v[64:67]\n"
    "s_add_u32 m0, s48, 38080\n"
    "ds_read_b64_tr_b16 a[152:153], v241 offset:352\n"
    "v_fma_f32 v253, v253, v244, v246\n"
    "v_mfma_f32_16x16x32_bf16 v[64:67], a[132:135], v[216:219], v[64:67]\n"
    "ds_read_b64_tr_b16 a[154:155], v241 offset:864\n"
    "buffer_load_dwordx4 v254, s[40:43], s50 offen offset:2048 lds\n"
    "v_mfma_f32_16x16x32_bf16 v[68:71], a[132:135], v[220:223], v[68:71]\n"
    "s_add_u32 m0, s48, 38112\n"
    "ds_read_b64_tr_b16 a[156:157], v241 offset:17248\n"
    "v_fma_f32 v251, v251, v142, v138\n"
    "v_mfma_f32_16x16x32_bf16 v[68:71], a[128:131], v[212:215], v[68:71]\n"
    "s_waitcnt lgkmcnt(7)\n"
    "ds_read_b64_tr_b16 a[158:159], v241 offset:17760\n"
    "buffer_load_dwordx4 v254, s[40:43], s50 offen offset:3072 lds\n"
    "v_mfma_f32_16x16x32_bf16 v[76:79], a[136:139], v[212:215], v[76:79]\n"
    "s_add_u32 s49, s49, 32768\n"
    "ds_read_b64_tr_b16 a[160:161], v241 offset:384\n"
    "v_fma_f32 v253, v253, v143, v139\n"
    "v_mfma_f32_16x16x32_bf16 v[76:79], a[140:143], v[220:223], v[76:79]\n"
    "s_add_u32 s50, s50, 32768\n"
    "ds_read_b64_tr_b16 a[162:163], v241 offset:896\n"
    "v_cvt_pk_bf16_f32 v235, v251, v253\n"
    "v_mfma_f32_16x16x32_bf16 v[72:75], a[140:143], v[216:219], v[72:75]\n"
    "ds_read_b64_tr_b16 a[164:165], v241 offset:17280\n"
    "v_lshlrev_b32 v251, 16, v238\n"
    "v_mfma_f32_16x16x32_bf16 v[72:75], a[136:139], v[208:211], v[72:75]\n"
    "s_waitcnt lgkmcnt(7)\n"
    "ds_read_b64_tr_b16 a[166:167], v241 offset:17792\n"
    "v_and_b32 v253, s53, v238\n"
    "v_mfma_f32_16x16x32_bf16 v[80:83], a[144:147], v[208:211], v[80:83]\n"
    "ds_read_b64_tr_b16 a[168:169], v241 offset:416\n"
    "v_fma_f32 v251, v251, v245, v247\n"
    "v_mfma_f32_16x16x32_bf16 v[80:83], a[148:151], v[216:219], v[80:83]\n"
    "ds_read_b64_tr_b16 a[170:171], v241 offset:928\n"
    "v_fma_f32 v253, v253, v245, v247\n"
    "v_mfma_f32_16x16x32_bf16 v[84:87], a[148:151], v[220:223], v[84:87]\n"
    "ds_read_b64_tr_b16 a[172:173], v241 offset:17312\n"
    "v_fma_f32 v251, v251, v140, v136\n"
    "v_mfma_f32_16x16x32_bf16 v[84:87], a[144:147], v[212:215], v[84:87]\n"
    "s_waitcnt lgkmcnt(7)\n"
    "ds_read_b64_tr_b16 a[174:175], v241 offset:17824\n"
    "v_fma_f32 v253, v253, v141, v137\n"
    "v_mfma_f32_16x16x32_bf16 v[92:95], a[152:155], v[212:215], v[92:95]\n"
    "ds_read_b64_tr_b16 a[176:177], v241 offset:448\n"
    "v_cvt_pk_bf16_f32 v238, v251, v253\n"
    "v_mfma_f32_16x16x32_bf16 v[92:95], a[156:159], v[220:223], v[92:95]\n"
    "ds_read_b64_tr_b16 a[178:179], v241 offset:960\n"
    "v_lshlrev_b32 v251, 16, v239\n"
    "v_mfma_f32_16x16x32_bf16 v[88:91], a[156:159], v[216:219], v[88:91]\n"
    "ds_read_b64_tr_b16 a[180:181], v241 offset:17344\n"
    "v_and_b32 v253, s53, v239\n"
    "v_mfma_f32_16x16x32_bf16 v[88:91], a[152:155], v[208:211], v[88:91]\n"
    "s_waitcnt lgkmcnt(7)\n"
    "ds_read_b64_tr_b16 a[182:183], v241 offset:17856\n"
    "v_fma_f32 v251, v251, v245, v247\n"
    "v_mfma_f32_16x16x32_bf16 v[96:99], a[160:163], v[208:211], v[96:99]\n"
    "ds_read_b64_tr_b16 a[184:185], v241 offset:480\n"
    "v_fma_f32 v253, v253, v245, v247\n"
    "v_mfma_f32_16x16x32_bf16 v[96:99], a[164:167], v[216:219], v[96:99]\n"
    "ds_read_b64_tr_b16 a[186:187], v241 offset:992\n"
    "v_fma_f32 v251, v251, v142, v138\n"
    "v_mfma_f32_16x16x32_bf16 v[100:103], a[164:167], v[220:223], v[100:103]\n"
    "s_waitcnt vmcnt(16)\n"
    "s_barrier\n"
    "v_mfma_f32_16x16x32_bf16 v[100:103], a[160:163], v[212:215], v[100:103]\n"
    "s_waitcnt lgkmcnt(6)\n"
    "ds_read_b64_tr_b16 a[188:189], v241 offset:17376\n"
    "ds_read_b64_tr_b16 a[190:191], v241 offset:17888\n"
    "v_mfma_f32_16x16x32_bf16 v[108:111], a[168:171], v[212:215], v[108:111]\n"
    "ds_read_b64_tr_b16 a[128:129], v242 offset:0\n"
    "v_fma_f32 v253, v253, v143, v139\n"
    "v_mfma_f32_16x16x32_bf16 v[108:111], a[172:175], v[220:223], v[108:111]\n"
    "ds_read_b64_tr_b16 a[130:131], v242 offset:512\n"
    "v_cvt_pk_bf16_f32 v239, v251, v253\n"
    "v_mfma_f32_16x16x32_bf16 v[104:107], a[172:175], v[216:219], v[104:107]\n"
    "ds_read_b64_tr_b16 a[132:133], v242 offset:16896\n"
    "v_add_u32 v253, s73, v241\n"
    "v_mfma_f32_16x16x32_bf16 v[104:107], a[168:171], v[208:211], v[104:107]\n"
    "s_waitcnt lgkmcnt(7)\n"
    "ds_read_b64_tr_b16 a[134:135], v242 offset:17408\n"
    "v_mfma_f32_16x16x32_bf16 v[112:115], a[176:179], v[208:211], v[112:115]\n"
    "ds_read_b64_tr_b16 a[136:137], v242 offset:32\n"
    "v_mfma_f32_16x16x32_bf16 v[112:115], a[180:183], v[216:219], v[112:115]\n"
    "ds_read_b64_tr_b16 a[138:139], v242 offset:544\n"
    "v_mfma_f32_16x16x32_bf16 v[116:119], a[180:183], v[220:223], v[116:119]\n"
    "ds_read_b64_tr_b16 a[140:141], v242 offset:16928\n"
    "v_mfma_f32_16x16x32_bf16 v[116:119], a[176:179], v[212:215], v[116:119]\n"
    "s_waitcnt lgkmcnt(7)\n"
    "ds_read_b64_tr_b16 a[142:143], v242 offset:17440\n"
    "v_mfma_f32_16x16x32_bf16 v[124:127], a[184:187], v[212:215], v[124:127]\n"
    "ds_read_b64_tr_b16 a[144:145], v242 offset:64\n"
    "v_mfma_f32_16x16x32_bf16 v[124:127], a[188:191], v[220:223], v[124:127]\n"
    "ds_read_b64_tr_b16 a[146:147], v242 offset:576\n"
    "v_mfma_f32_16x16x32_bf16 v[120:123], a[188:191], v[216:219], v[120:123]\n"
    "ds_read_b64_tr_b16 a[148:149], v242 offset:16960\n"
    "v_mfma_f32_16x16x32_bf16 v[120:123], a[184:187], v[208:211], v[120:123]\n"
    "s_waitcnt lgkmcnt(7)\n"
    "ds_read_b64_tr_b16 a[150:151], v242 offset:17472\n"
    "v_mfma_f32_16x16x32_bf16 v[0:3], a[128:131], v[224:227], v[0:3]\n"
    "ds_read_b64_tr_b16 a[152:153], v242 offset:96\n"
    "v_mfma_f32_16x16x32_bf16 v[0:3], a[132:135], v[232:235], v[0:3]\n"
    "s_nop 0\n"
    "s_barrier\n"
    "v_mfma_f32_16x16x32_bf16 v[4:7], a[132:135], v[236:239], v[4:7]\n"
    "ds_read_b64_tr_b16 a[154:155], v242 offset:608\n"
    "ds_read_b64_tr_b16 a[156:157], v242 offset:16992\n"
    "v_mfma_f32_16x16x32_bf16 v[4:7], a[128:131], v[228:231], v[4:7]\n"
    "s_waitcnt lgkmcnt(7)\n"
    "ds_read_b64_tr_b16 a[158:159], v242 offset:17504\n"
    "v_mfma_f32_16x16x32_bf16 v[12:15], a[136:139], v[228:231], v[12:15]\n"
    "s_mov_b32 s53, s55\n"
    "ds_read_b64_tr_b16 a[160:161], v242 offset:128\n"
    "v_mfma_f32_16x16x32_bf16 v[12:15], a[140:143], v[236:239], v[12:15]\n"
    "ds_read_b64_tr_b16 a[162:163], v242 offset:640\n"
    "v_mfma_f32_16x16x32_bf16 v[8:11], a[140:143], v[232:235], v[8:11]\n"
    "ds_read_b64_tr_b16 a[164:165], v242 offset:17024\n"
    "v_mfma_f32_16x16x32_bf16 v[8:11], a[136:139], v[224:227], v[8:11]\n"
    "s_waitcnt lgkmcnt(7)\n"
    "ds_read_b64_tr_b16 a[166:167], v242 offset:17536\n"
    "v_mfma_f32_16x16x32_bf16 v[16:19], a[144:147], v[224:227], v[16:19]\n"
    "ds_read_b64_tr_b16 a[168:169], v242 offset:160\n"
    "v_mfma_f32_16x16x32_bf16 v[16:19], a[148:151], v[232:235], v[16:19]\n"
    "ds_read_b64_tr_b16 a[170:171], v242 offset:672\n"
    "v_cvt_pk_bf16_f32 v0, v0, v1\n"
    "v_cvt_pk_bf16_f32 v1, v2, v3\n"
    "v_mfma_f32_16x16x32_bf16 v[20:23], a[148:151], v[236:239], v[20:23]\n"
    "ds_read_b64_tr_b16 a[172:173], v242 offset:17056\n"
    "ds_write_b64 v250, v[0:1] offset:0\n"
    "v_mfma_f32_16x16x32_bf16 v[20:23], a[144:147], v[228:231], v[20:23]\n"
    "s_waitcnt lgkmcnt(8)\n"
    "ds_read_b64_tr_b16 a[174:175], v242 offset:17568\n"
    "v_cvt_pk_bf16_f32 v4, v4, v5\n"
    "v_mfma_f32_16x16x32_bf16 v[28:31], a[152:155], v[228:231], v[28:31]\n"
    "ds_read_b64_tr_b16 a[176:177], v242 offset:192\n"
    "v_cvt_pk_bf16_f32 v5, v6, v7\n"
    "v_mfma_f32_16x16x32_bf16 v[28:31], a[156:159], v[236:239], v[28:31]\n"
    "ds_read_b64_tr_b16 a[178:179], v242 offset:704\n"
    "ds_write_b64 v250, v[4:5] offset:4096\n"
    "v_cvt_pk_bf16_f32 v8, v8, v9\n"
    "v_mfma_f32_16x16x32_bf16 v[24:27], a[156:159], v[232:235], v[24:27]\n"
    "ds_read_b64_tr_b16 a[180:181], v242 offset:17088\n"
    "v_cvt_pk_bf16_f32 v9, v10, v11\n"
    "v_mfma_f32_16x16x32_bf16 v[24:27], a[152:155], v[224:227], v[24:27]\n"
    "s_waitcnt lgkmcnt(9)\n"
    "ds_read_b64_tr_b16 a[182:183], v242 offset:17600\n"
    "ds_write_b64 v250, v[8:9] offset:512\n"
    "v_mfma_f32_16x16x32_bf16 v[32:35], a[160:163], v[224:227], v[32:35]\n"
    "ds_read_b64_tr_b16 a[184:185], v242 offset:224\n"
    "v_cvt_pk_bf16_f32 v12, v12, v13\n"
    "v_mfma_f32_16x16x32_bf16 v[32:35], a[164:167], v[232:235], v[32:35]\n"
    "ds_read_b64_tr_b16 a[186:187], v242 offset:736\n"
    "v_cvt_pk_bf16_f32 v13, v14, v15\n"
    "v_mfma_f32_16x16x32_bf16 v[36:39], a[164:167], v[236:239], v[36:39]\n"
    "ds_read_b64_tr_b16 a[188:189], v242 offset:17120\n"
    "ds_write_b64 v250, v[12:13] offset:4608\n"
    "v_mfma_f32_16x16x32_bf16 v[36:39], a[160:163], v[228:231], v[36:39]\n"
    "s_waitcnt lgkmcnt(10)\n"
    "ds_read_b64_tr_b16 a[190:191], v242 offset:17632\n"
    "v_cvt_pk_bf16_f32 v16, v16, v17\n"
    "v_mfma_f32_16x16x32_bf16 v[44:47], a[168:171], v[228:231], v[44:47]\n"
    "ds_read_b64_tr_b16 a[128:129], v242 offset:256\n"
    "v_cvt_pk_bf16_f32 v17, v18, v19\n"
    "v_mfma_f32_16x16x32_bf16 v[44:47], a[172:175], v[236:239], v[44:47]\n"
    "ds_read_b64_tr_b16 a[130:131], v242 offset:768\n"
    "ds_write_b64 v250, v[16:17] offset:8192\n"
    "v_cvt_pk_bf16_f32 v20, v20, v21\n"
    "v_mfma_f32_16x16x32_bf16 v[40:43], a[172:175], v[232:235], v[40:43]\n"
    "ds_read_b64_tr_b16 a[132:133], v242 offset:17152\n"
    "v_cvt_pk_bf16_f32 v21, v22, v23\n"
    "v_mfma_f32_16x16x32_bf16 v[40:43], a[168:171], v[224:227], v[40:43]\n"
    "s_waitcnt lgkmcnt(10)\n"
    "ds_read_b64_tr_b16 a[134:135], v242 offset:17664\n"
    "ds_write_b64 v250, v[20:21] offset:12288\n"
    "v_mfma_f32_16x16x32_bf16 v[48:51], a[176:179], v[224:227], v[48:51]\n"
    "ds_read_b64_tr_b16 a[136:137], v242 offset:288\n"
    "v_cvt_pk_bf16_f32 v24, v24, v25\n"
    "v_mfma_f32_16x16x32_bf16 v[48:51], a[180:183], v[232:235], v[48:51]\n"
    "ds_read_b64_tr_b16 a[138:139], v242 offset:800\n"
    "v_cvt_pk_bf16_f32 v25, v26, v27\n"
    "v_mfma_f32_16x16x32_bf16 v[52:55], a[180:183], v[236:239], v[52:55]\n"
    "ds_read_b64_tr_b16 a[140:141], v242 offset:17184\n"
    "ds_write_b64 v250, v[24:25] offset:8704\n"
    "v_mfma_f32_16x16x32_bf16 v[52:55], a[176:179], v[228:231], v[52:55]\n"
    "s_waitcnt lgkmcnt(10)\n"
    "ds_read_b64_tr_b16 a[142:143], v242 offset:17696\n"
    "v_cvt_pk_bf16_f32 v28, v28, v29\n"
    "v_mfma_f32_16x16x32_bf16 v[60:63], a[184:187], v[228:231], v[60:63]\n"
    "ds_read_b64_tr_b16 a[144:145], v242 offset:320\n"
    "v_cvt_pk_bf16_f32 v29, v30, v31\n"
    "v_mfma_f32_16x16x32_bf16 v[60:63], a[188:191], v[236:239], v[60:63]\n"
    "ds_read_b64_tr_b16 a[146:147], v242 offset:832\n"
    "ds_write_b64 v250, v[28:29] offset:12800\n"
    "v_cvt_pk_bf16_f32 v32, v32, v33\n"
    "v_mfma_f32_16x16x32_bf16 v[56:59], a[188:191], v[232:235], v[56:59]\n"
    "ds_read_b64_tr_b16 a[148:149], v242 offset:17216\n"
    "v_cvt_pk_bf16_f32 v33, v34, v35\n"
    "v_mfma_f32_16x16x32_bf16 v[56:59], a[184:187], v[224:227], v[56:59]\n"
    "s_waitcnt lgkmcnt(10)\n"
    "ds_read_b64_tr_b16 a[150:151], v242 offset:17728\n"
    "ds_write_b64 v250, v[32:33] offset:16384\n"
    "v_mfma_f32_16x16x32_bf16 v[64:67], a[128:131], v[224:227], v[64:67]\n"
    "ds_read_b64_tr_b16 a[152:153], v242 offset:352\n"
    "v_cvt_pk_bf16_f32 v36, v36, v37\n"
    "v_mfma_f32_16x16x32_bf16 v[64:67], a[132:135], v[232:235], v[64:67]\n"
    "ds_read_b64_tr_b16 a[154:155], v242 offset:864\n"
    "v_cvt_pk_bf16_f32 v37, v38, v39\n"
    "v_mfma_f32_16x16x32_bf16 v[68:71], a[132:135], v[236:239], v[68:71]\n"
    "ds_read_b64_tr_b16 a[156:157], v242 offset:17248\n"
    "ds_write_b64 v250, v[36:37] offset:20480\n"
    "v_mfma_f32_16x16x32_bf16 v[68:71], a[128:131], v[228:231], v[68:71]\n"
    "s_waitcnt lgkmcnt(10)\n"
    "ds_read_b64_tr_b16 a[158:159], v242 offset:17760\n"
    "v_cvt_pk_bf16_f32 v40, v40, v41\n"
    "v_mfma_f32_16x16x32_bf16 v[76:79], a[136:139], v[228:231], v[76:79]\n"
    "ds_read_b64_tr_b16 a[160:161], v242 offset:384\n"
    "v_cvt_pk_bf16_f32 v41, v42, v43\n"
    "v_mfma_f32_16x16x32_bf16 v[76:79], a[140:143], v[236:239], v[76:79]\n"
    "ds_read_b64_tr_b16 a[162:163], v242 offset:896\n"
    "ds_write_b64 v250, v[40:41] offset:16896\n"
    "v_cvt_pk_bf16_f32 v44, v44, v45\n"
    "v_mfma_f32_16x16x32_bf16 v[72:75], a[140:143], v[232:235], v[72:75]\n"
    "ds_read_b64_tr_b16 a[164:165], v242 offset:17280\n"
    "v_cvt_pk_bf16_f32 v45, v46, v47\n"
    "v_mfma_f32_16x16x32_bf16 v[72:75], a[136:139], v[224:227], v[72:75]\n"
    "s_waitcnt lgkmcnt(10)\n"
    "ds_read_b64_tr_b16 a[166:167], v242 offset:17792\n"
    "ds_write_b64 v250, v[44:45] offset:20992\n"
    "v_mfma_f32_16x16x32_bf16 v[80:83], a[144:147], v[224:227], v[80:83]\n"
    "ds_read_b64_tr_b16 a[168:169], v242 offset:416\n"
    "v_cvt_pk_bf16_f32 v48, v48, v49\n"
    "v_mfma_f32_16x16x32_bf16 v[80:83], a[148:151], v[232:235], v[80:83]\n"
    "ds_read_b64_tr_b16 a[170:171], v242 offset:928\n"
    "v_cvt_pk_bf16_f32 v49, v50, v51\n"
    "v_mfma_f32_16x16x32_bf16 v[84:87], a[148:151], v[236:239], v[84:87]\n"
    "ds_read_b64_tr_b16 a[172:173], v242 offset:17312\n"
    "ds_write_b64 v250, v[48:49] offset:24576\n"
    "v_mfma_f32_16x16x32_bf16 v[84:87], a[144:147], v[228:231], v[84:87]\n"
    "s_waitcnt lgkmcnt(10)\n"
    "ds_read_b64_tr_b16 a[174:175], v242 offset:17824\n"
    "v_cvt_pk_bf16_f32 v52, v52, v53\n"
    "v_mfma_f32_16x16x32_bf16 v[92:95], a[152:155], v[228:231], v[92:95]\n"
    "ds_read_b64_tr_b16 a[176:177], v242 offset:448\n"
    "v_cvt_pk_bf16_f32 v53, v54, v55\n"
    "v_mfma_f32_16x16x32_bf16 v[92:95], a[156:159], v[236:239], v[92:95]\n"
    "ds_read_b64_tr_b16 a[178:179], v242 offset:960\n"
    "ds_write_b64 v250, v[52:53] offset:28672\n"
    "v_cvt_pk_bf16_f32 v56, v56, v57\n"
    "v_mfma_f32_16x16x32_bf16 v[88:91], a[156:159], v[232:235], v[88:91]\n"
    "ds_read_b64_tr_b16 a[180:181], v242 offset:17344\n"
    "v_cvt_pk_bf16_f32 v57, v58, v59\n"
    "v_mfma_f32_16x16x32_bf16 v[88:91], a[152:155], v[224:227], v[88:91]\n"
    "s_waitcnt lgkmcnt(10)\n"
    "ds_read_b64_tr_b16 a[182:183], v242 offset:17856\n"
    "ds_write_b64 v250, v[56:57] offset:25088\n"
    "v_mfma_f32_16x16x32_bf16 v[96:99], a[160:163], v[224:227], v[96:99]\n"
    "ds_read_b64_tr_b16 a[184:185], v242 offset:480\n"
    "v_cvt_pk_bf16_f32 v60, v60, v61\n"
    "v_mfma_f32_16x16x32_bf16 v[96:99], a[164:167], v[232:235], v[96:99]\n"
    "ds_read_b64_tr_b16 a[186:187], v242 offset:992\n"
    "v_cvt_pk_bf16_f32 v61, v62, v63\n"
    "v_mfma_f32_16x16x32_bf16 v[100:103], a[164:167], v[236:239], v[100:103]\n"
    "ds_read_b64_tr_b16 a[188:189], v242 offset:17376\n"
    "ds_write_b64 v250, v[60:61] offset:29184\n"
    "v_mfma_f32_16x16x32_bf16 v[100:103], a[160:163], v[228:231], v[100:103]\n"
    "s_waitcnt lgkmcnt(10)\n"
    "ds_read_b64_tr_b16 a[190:191], v242 offset:17888\n"
    "v_mfma_f32_16x16x32_bf16 v[108:111], a[168:171], v[228:231], v[108:111]\n"
    "v_mfma_f32_16x16x32_bf16 v[108:111], a[172:175], v[236:239], v[108:111]\n"
    "v_mfma_f32_16x16x32_bf16 v[104:107], a[172:175], v[232:235], v[104:107]\n"
    "v_mfma_f32_16x16x32_bf16 v[104:107], a[168:171], v[224:227], v[104:107]\n"
    "s_waitcnt lgkmcnt(6)\n"
    "v_mfma_f32_16x16x32_bf16 v[112:115], a[176:179], v[224:227], v[112:115]\n"
    "s_waitcnt vmcnt(0) lgkmcnt(0)\n"
    "s_barrier\n"
    "v_mfma_f32_16x16x32_bf16 v[112:115], a[180:183], v[232:235], v[112:115]\n"
    "ds_read_b128 v[208:211], v252 offset:0\n"
    "ds_read_b128 v[212:215], v252 offset:64\n"
    "v_mfma_f32_16x16x32_bf16 v[116:119], a[180:183], v[236:239], v[116:119]\n"
    "ds_read_b128 a[0:3], v248 offset:0\n"
    "ds_read_b128 a[4:7], v248 offset:4096\n"
    "v_mfma_f32_16x16x32_bf16 v[116:119], a[176:179], v[228:231], v[116:119]\n"
    "ds_read_b128 a[8:11], v249 offset:0\n"
    "ds_read_b128 a[12:15], v249 offset:4096\n"
    "v_mfma_f32_16x16x32_bf16 v[124:127], a[184:187], v[228:231], v[124:127]\n"
    "ds_read_b128 a[16:19], v248 offset:8192\n"
    "ds_read_b128 a[20:23], v248 offset:12288\n"
    "v_mfma_f32_16x16x32_bf16 v[124:127], a[188:191], v[236:239], v[124:127]\n"
    "ds_read_b128 a[24:27], v249 offset:8192\n"
    "ds_read_b128 a[28:31], v249 offset:12288\n"
    "v_mfma_f32_16x16x32_bf16 v[120:123], a[188:191], v[232:235], v[120:123]\n"
    "ds_read_b128 v[216:219], v252 offset:128\n"
    "ds_read_b128 v[220:223], v252 offset:192\n"
    "v_mfma_f32_16x16x32_bf16 v[120:123], a[184:187], v[224:227], v[120:123]\n"
    "s_waitcnt lgkmcnt(2)\n"
    "v_mfma_f32_16x16x32_bf16 v[0:3], v[208:211], a[0:3], 0\n"
    "s_lshl_b32 s70, s30, 2\n"
    "ds_read_b128 a[44:47], v249 offset:20480\n"
    "ds_read_b128 a[40:43], v249 offset:16384\n"
    "v_mfma_f32_16x16x32_bf16 v[0:3], v[212:215], a[16:19], v[0:3]\n"
    "s_add_u32 s70, s70, s72\n"
    "ds_read_b128 a[36:39], v248 offset:20480\n"
    "ds_read_b128 a[32:35], v248 offset:16384\n"
    "v_mfma_f32_16x16x32_bf16 v[4:7], v[212:215], a[20:23], 0\n"
    "s_lshl_b32 s68, s70, 10\n"
    "ds_read_b128 a[60:63], v249 offset:28672\n"
    "ds_read_b128 a[56:59], v249 offset:24576\n"
    "v_mfma_f32_16x16x32_bf16 v[4:7], v[208:211], a[4:7], v[4:7]\n"
    "s_lshl_b32 s70, %1, 2\n"
    "ds_read_b128 a[52:55], v248 offset:28672\n"
    "ds_read_b128 a[48:51], v248 offset:24576\n"
    "v_mfma_f32_16x16x32_bf16 v[8:11], v[208:211], a[8:11], 0\n"
    "s_add_u32 s70, s70, s72\n"
    "ds_read_b128 v[224:227], v252 offset:512\n"
    "v_mfma_f32_16x16x32_bf16 v[8:11], v[212:215], a[24:27], v[8:11]\n"
    "s_lshl_b32 s69, s70, 10\n"
    "ds_read_b128 v[228:231], v252 offset:576\n"
    "v_mfma_f32_16x16x32_bf16 v[12:15], v[212:215], a[28:31], 0\n"
    "s_mov_b64 s[76:77], s[26:27]\n"
    "v_cvt_pk_bf16_f32 v64, v64, v65\n"
    "v_cvt_pk_bf16_f32 v65, v66, v67\n"
    "v_mfma_f32_16x16x32_bf16 v[12:15], v[208:211], a[12:15], v[12:15]\n"
    "s_waitcnt lgkmcnt(2)\n"
    "ds_write_b64 v250, v[64:65] offset:32768\n"
    "v_cvt_pk_bf16_f32 v68, v68, v69\n"
    "v_mfma_f32_16x16x32_bf16 v[12:15], v[216:219], a[44:47], v[12:15]\n"
    "s_mov_b32 s78, 733184\n"
    "v_cvt_pk_bf16_f32 v69, v70, v71\n"
    "ds_write_b64 v250, v[68:69] offset:36864\n"
    "v_mfma_f32_16x16x32_bf16 v[12:15], v[220:223], a[60:63], v[12:15]\n"
    "s_mov_b32 s79, 0x20000\n"
    "ds_read_b128 v[232:235], v252 offset:640\n"
    "v_cvt_pk_bf16_f32 v72, v72, v73\n"
    "v_mfma_f32_16x16x32_bf16 v[8:11], v[220:223], a[56:59], v[8:11]\n"
    "s_add_u32 s80, s69, 77824\n"
    "v_cvt_pk_bf16_f32 v73, v74, v75\n"
    "ds_write_b64 v250, v[72:73] offset:33280\n"
    "v_mfma_f32_16x16x32_bf16 v[8:11], v[216:219], a[40:43], v[8:11]\n"
    "s_mov_b32 s81, 0x100000\n"
    "v_cvt_pk_bf16_f32 v76, v76, v77\n"
    "v_cvt_pk_bf16_f32 v77, v78, v79\n"
    "v_mfma_f32_16x16x32_bf16 v[4:7], v[216:219], a[36:39], v[4:7]\n"
    "s_mov_b64 s[84:85], s[14:15]\n"
    "ds_write_b64 v250, v[76:77] offset:37376\n"
    "v_cvt_pk_bf16_f32 v80, v80, v81\n"
    "v_mfma_f32_16x16x32_bf16 v[4:7], v[220:223], a[52:55], v[4:7]\n"
    "s_mov_b32 s86, 0\n"
    "ds_read_b128 v[236:239], v252 offset:704\n"
    "v_cvt_pk_bf16_f32 v81, v82, v83\n"
    "v_mfma_f32_16x16x32_bf16 v[0:3], v[220:223], a[48:51], v[0:3]\n"
    "s_mov_b32 s87, 0x20000\n"
    "ds_write_b64 v250, v[80:81] offset:40960\n"
    "v_cvt_pk_bf16_f32 v84, v84, v85\n"
    "v_mfma_f32_16x16x32_bf16 v[0:3], v[216:219], a[32:35], v[0:3]\n"
    "s_waitcnt lgkmcnt(7)\n"
    "v_cvt_pk_bf16_f32 v85, v86, v87\n"
    "ds_write_b64 v250, v[84:85] offset:45056\n"
    "v_mfma_f32_16x16x32_bf16 v[16:19], v[224:227], a[0:3], 0\n"
    "s_and_b32 s88, %1, 3\n"
    "v_cvt_pk_bf16_f32 v88, v88, v89\n"
    "v_cvt_pk_bf16_f32 v89, v90, v91\n"
    "v_mfma_f32_16x16x32_bf16 v[16:19], v[228:231], a[16:19], v[16:19]\n"
    "s_mul_i32 s88, s88, 28\n"
    "ds_read_b128 v[208:211], v252 offset:33792\n"
    "ds_write_b64 v250, v[88:89] offset:41472\n"
    "v_mfma_f32_16x16x32_bf16 v[20:23], v[228:231], a[20:23], 0\n"
    "s_lshr_b32 s88, s88, 3\n"
    "v_cvt_pk_bf16_f32 v92, v92, v93\n"
    "v_cvt_pk_bf16_f32 v93, v94, v95\n"
    "v_mfma_f32_16x16x32_bf16 v[20:23], v[224:227], a[4:7], v[20:23]\n"
    "s_lshl_b32 s88, s88, 8\n"
    "ds_write_b64 v250, v[92:93] offset:45568\n"
    "v_cvt_pk_bf16_f32 v96, v96, v97\n"
    "v_mfma_f32_16x16x32_bf16 v[24:27], v[224:227], a[8:11], 0\n"
    "s_lshr_b32 s89, %1, 3\n"
    "v_cvt_pk_bf16_f32 v97, v98, v99\n"
    "ds_write_b64 v250, v[96:97] offset:49152\n"
    "v_mfma_f32_16x16x32_bf16 v[24:27], v[228:231], a[24:27], v[24:27]\n"
    "s_lshl_b32 s89, s89, 2\n"
    "ds_read_b128 v[212:215], v252 offset:33856\n"
    "v_cvt_pk_bf16_f32 v100, v100, v101\n"
    "v_mfma_f32_16x16x32_bf16 v[28:31], v[228:231], a[28:31], 0\n"
    "s_add_u32 s89, s89, s72\n"
    "v_cvt_pk_bf16_f32 v101, v102, v103\n"
    "ds_write_b64 v250, v[100:101] offset:53248\n"
    "v_mfma_f32_16x16x32_bf16 v[28:31], v[224:227], a[12:15], v[28:31]\n"
    "s_waitcnt lgkmcnt(8)\n"
    "v_cvt_pk_bf16_f32 v104, v104, v105\n"
    "v_cvt_pk_bf16_f32 v105, v106, v107\n"
    "v_mfma_f32_16x16x32_bf16 v[28:31], v[232:235], a[44:47], v[28:31]\n"
    "s_mul_i32 s89, s89, 14336\n"
    "ds_write_b64 v250, v[104:105] offset:49664\n"
    "v_cvt_pk_bf16_f32 v108, v108, v109\n"
    "v_mfma_f32_16x16x32_bf16 v[28:31], v[236:239], a[60:63], v[28:31]\n"
    "s_add_u32 s88, s88, s89\n"
    "ds_read_b128 v[216:219], v252 offset:33920\n"
    "v_cvt_pk_bf16_f32 v109, v110, v111\n"
    "v_mfma_f32_16x16x32_bf16 v[24:27], v[236:239], a[56:59], v[24:27]\n"
    "s_mov_b32 s89, 0\n"
    "ds_write_b64 v250, v[108:109] offset:53760\n"
    "v_cvt_pk_bf16_f32 v112, v112, v113\n"
    "v_mfma_f32_16x16x32_bf16 v[24:27], v[232:235], a[40:43], v[24:27]\n"
    "s_lshl_b32 s70, s72, 10\n"
    "v_cvt_pk_bf16_f32 v113, v114, v115\n"
    "ds_write_b64 v250, v[112:113] offset:57344\n"
    "v_mfma_f32_16x16x32_bf16 v[20:23], v[232:235], a[36:39], v[20:23]\n"
    "s_add_u32 s70, s70, 101376\n"
    "v_cvt_pk_bf16_f32 v116, v116, v117\n"
    "v_cvt_pk_bf16_f32 v117, v118, v119\n"
    "v_mfma_f32_16x16x32_bf16 v[20:23], v[236:239], a[52:55], v[20:23]\n"
    "s_xor_b32 s71, s72, 2\n"
    "ds_read_b128 v[220:223], v252 offset:33984\n"
    "ds_write_b64 v250, v[116:117] offset:61440\n"
    "v_mfma_f32_16x16x32_bf16 v[16:19], v[236:239], a[48:51], v[16:19]\n"
    "s_lshl_b32 s71, s71, 10\n"
    "v_cvt_pk_bf16_f32 v120, v120, v121\n"
    "v_cvt_pk_bf16_f32 v121, v122, v123\n"
    "v_mfma_f32_16x16x32_bf16 v[16:19], v[232:235], a[32:35], v[16:19]\n"
    "s_waitcnt lgkmcnt(7)\n"
    "ds_write_b64 v250, v[120:121] offset:57856\n"
    "v_cvt_pk_bf16_f32 v124, v124, v125\n"
    "v_mfma_f32_16x16x32_bf16 v[128:131], v[208:211], a[0:3], 0\n"
    "s_add_u32 s71, s71, 101376\n"
    "v_cvt_pk_bf16_f32 v125, v126, v127\n"
    "ds_write_b64 v250, v[124:125] offset:61952\n"
    "v_mfma_f32_16x16x32_bf16 v[128:131], v[212:215], a[16:19], v[128:131]\n"
    "ds_read_b128 v[224:227], v252 offset:34304\n"
    "v_mfma_f32_16x16x32_bf16 v[132:135], v[212:215], a[20:23], 0\n"
    "v_mfma_f32_16x16x32_bf16 v[132:135], v[208:211], a[4:7], v[132:135]\n"
    "v_mfma_f32_16x16x32_bf16 v[136:139], v[208:211], a[8:11], 0\n"
    "v_mfma_f32_16x16x32_bf16 v[136:139], v[212:215], a[24:27], v[136:139]\n"
    "ds_read_b128 v[228:231], v252 offset:34368\n"
    "v_mfma_f32_16x16x32_bf16 v[140:143], v[212:215], a[28:31], 0\n"
    "v_mfma_f32_16x16x32_bf16 v[140:143], v[208:211], a[12:15], v[140:143]\n"
    "s_waitcnt lgkmcnt(5)\n"
    "v_mfma_f32_16x16x32_bf16 v[140:143], v[216:219], a[44:47], v[140:143]\n"
    "v_mfma_f32_16x16x32_bf16 v[140:143], v[220:223], a[60:63], v[140:143]\n"
    "ds_read_b128 v[232:235], v252 offset:34432\n"
    "v_mfma_f32_16x16x32_bf16 v[136:139], v[220:223], a[56:59], v[136:139]\n"
    "s_waitcnt lgkmcnt(3)\n"
    "s_barrier\n"
    "v_mfma_f32_16x16x32_bf16 v[136:139], v[216:219], a[40:43], v[136:139]\n"
    "ds_read_b128 a[64:67], v248 offset:32768\n"
    "v_mfma_f32_16x16x32_bf16 v[132:135], v[216:219], a[36:39], v[132:135]\n"
    "ds_read_b128 a[68:71], v248 offset:36864\n"
    "v_mfma_f32_16x16x32_bf16 v[132:135], v[220:223], a[52:55], v[132:135]\n"
    "ds_read_b128 v[236:239], v252 offset:34496\n"
    "ds_read_b128 a[72:75], v249 offset:32768\n"
    "v_mfma_f32_16x16x32_bf16 v[128:131], v[220:223], a[48:51], v[128:131]\n"
    "ds_read_b128 a[76:79], v249 offset:36864\n"
    "v_mfma_f32_16x16x32_bf16 v[128:131], v[216:219], a[32:35], v[128:131]\n"
    "s_waitcnt lgkmcnt(6)\n"
    "ds_read_b128 a[80:83], v248 offset:40960\n"
    "v_mfma_f32_16x16x32_bf16 v[144:147], v[224:227], a[0:3], 0\n"
    "ds_read_b128 a[84:87], v248 offset:45056\n"
    "v_mfma_f32_16x16x32_bf16 v[144:147], v[228:231], a[16:19], v[144:147]\n"
    "ds_read_b128 v[208:211], v252 offset:256\n"
    "ds_read_b128 a[88:91], v249 offset:40960\n"
    "v_mfma_f32_16x16x32_bf16 v[148:151], v[228:231], a[20:23], 0\n"
    "ds_read_b128 a[92:95], v249 offset:45056\n"
    "v_mfma_f32_16x16x32_bf16 v[148:151], v[224:227], a[4:7], v[148:151]\n"
    "ds_read_b128 a[96:99], v248 offset:49152\n"
    "v_mfma_f32_16x16x32_bf16 v[152:155], v[224:227], a[8:11], 0\n"
    "ds_read_b128 a[100:103], v248 offset:53248\n"
    "v_mfma_f32_16x16x32_bf16 v[152:155], v[228:231], a[24:27], v[152:155]\n"
    "ds_read_b128 v[212:215], v252 offset:320\n"
    "ds_read_b128 a[104:107], v249 offset:49152\n"
    "v_mfma_f32_16x16x32_bf16 v[156:159], v[228:231], a[28:31], 0\n"
    "ds_read_b128 a[108:111], v249 offset:53248\n"
    "v_mfma_f32_16x16x32_bf16 v[156:159], v[224:227], a[12:15], v[156:159]\n"
    "s_waitcnt lgkmcnt(12)\n"
    "ds_read_b128 a[112:115], v248 offset:57344\n"
    "v_mfma_f32_16x16x32_bf16 v[156:159], v[232:235], a[44:47], v[156:159]\n"
    "ds_read_b128 a[116:119], v248 offset:61440\n"
    "v_mfma_f32_16x16x32_bf16 v[156:159], v[236:239], a[60:63], v[156:159]\n"
    "ds_read_b128 v[216:219], v252 offset:384\n"
    "ds_read_b128 a[120:123], v249 offset:57344\n"
    "v_mfma_f32_16x16x32_bf16 v[152:155], v[236:239], a[56:59], v[152:155]\n"
    "ds_read_b128 a[124:127], v249 offset:61440\n"
    "v_mfma_f32_16x16x32_bf16 v[152:155], v[232:235], a[40:43], v[152:155]\n"
    "v_mfma_f32_16x16x32_bf16 v[148:151], v[232:235], a[36:39], v[148:151]\n"
    "v_mfma_f32_16x16x32_bf16 v[148:151], v[236:239], a[52:55], v[148:151]\n"
    "ds_read_b128 v[220:223], v252 offset:448\n"
    "v_mfma_f32_16x16x32_bf16 v[144:147], v[236:239], a[48:51], v[144:147]\n"
    "v_lshrrev_b32 v240, 9, v254\n"
    "v_mul_u32_u24 v240, 3584, v240\n"
    "v_mfma_f32_16x16x32_bf16 v[144:147], v[232:235], a[32:35], v[144:147]\n"
    "s_waitcnt lgkmcnt(8)\n"
    "v_bfe_u32 v241, v254, 4, 5\n"
    "v_mfma_f32_16x16x32_bf16 v[0:3], v[208:211], a[64:67], v[0:3]\n"
    "v_lshl_add_u32 v255, v241, 5, v240\n"
    "v_mfma_f32_16x16x32_bf16 v[0:3], v[212:215], a[80:83], v[0:3]\n"
    "ds_read_b128 v[224:227], v252 offset:768\n"
    "v_mfma_f32_16x16x32_bf16 v[4:7], v[212:215], a[84:87], v[4:7]\n"
    "v_mfma_f32_16x16x32_bf16 v[4:7], v[208:211], a[68:71], v[4:7]\n"
    "v_mfma_f32_16x16x32_bf16 v[8:11], v[208:211], a[72:75], v[8:11]\n"
    "v_mfma_f32_16x16x32_bf16 v[8:11], v[212:215], a[88:91], v[8:11]\n"
    "ds_read_b128 v[228:231], v252 offset:832\n"
    "v_mfma_f32_16x16x32_bf16 v[12:15], v[212:215], a[92:95], v[12:15]\n"
    "v_mfma_f32_16x16x32_bf16 v[12:15], v[208:211], a[76:79], v[12:15]\n"
    "s_waitcnt lgkmcnt(2)\n"
    "v_mfma_f32_16x16x32_bf16 v[12:15], v[216:219], a[108:111], v[12:15]\n"
    "s_nop 0\n"
    "s_barrier\n"
    "v_mfma_f32_16x16x32_bf16 v[12:15], v[220:223], a[124:127], v[12:15]\n"
    "s_add_u32 m0, s48, 67584\n"
    "ds_read_b128 v[232:235], v252 offset:896\n"
    "v_mfma_f32_16x16x32_bf16 v[8:11], v[220:223], a[120:123], v[8:11]\n"
    "buffer_load_dwordx4 v254, s[44:47], s51 offen offset:0 lds\n"
    "v_add_u32 v248, s70, v254\n"
    "v_mfma_f32_16x16x32_bf16 v[8:11], v[216:219], a[104:107], v[8:11]\n"
    "s_add_u32 m0, s48, 67616\n"
    "v_add_u32 v249, s71, v254\n"
    "v_mfma_f32_16x16x32_bf16 v[4:7], v[216:219], a[100:103], v[4:7]\n"
    "buffer_load_dwordx4 v254, s[44:47], s51 offen offset:1024 lds\n"
    "v_mfma_f32_16x16x32_bf16 v[4:7], v[220:223], a[116:119], v[4:7]\n"
    "s_add_u32 m0, s48, 67648\n"
    "ds_read_b128 v[236:239], v252 offset:960\n"
    "v_mfma_f32_16x16x32_bf16 v[0:3], v[220:223], a[112:115], v[0:3]\n"
    "buffer_load_dwordx4 v254, s[44:47], s51 offen offset:2048 lds\n"
    "v_mfma_f32_16x16x32_bf16 v[0:3], v[216:219], a[96:99], v[0:3]\n"
    "s_waitcnt lgkmcnt(2)\n"
    "v_mfma_f32_16x16x32_bf16 v[16:19], v[224:227], a[64:67], v[16:19]\n"
    "s_add_u32 m0, s48, 67680\n"
    "v_mfma_f32_16x16x32_bf16 v[16:19], v[228:231], a[80:83], v[16:19]\n"
    "ds_read_b128 v[208:211], v252 offset:34048\n"
    "buffer_load_dwordx4 v254, s[44:47], s51 offen offset:3072 lds\n"
    "v_mfma_f32_16x16x32_bf16 v[20:23], v[228:231], a[84:87], v[20:23]\n"
    "s_add_u32 m0, s48, 71808\n"
    "v_mfma_f32_16x16x32_bf16 v[20:23], v[224:227], a[68:71], v[20:23]\n"
    "buffer_load_dwordx4 v254, s[44:47], s52 offen offset:0 lds\n"
    "v_mfma_f32_16x16x32_bf16 v[24:27], v[224:227], a[72:75], v[24:27]\n"
    "s_add_u32 m0, s48, 71840\n"
    "v_mfma_f32_16x16x32_bf16 v[24:27], v[228:231], a[88:91], v[24:27]\n"
    "ds_read_b128 v[212:215], v252 offset:34112\n"
    "buffer_load_dwordx4 v254, s[44:47], s52 offen offset:1024 lds\n"
    "v_mfma_f32_16x16x32_bf16 v[28:31], v[228:231], a[92:95], v[28:31]\n"
    "s_add_u32 m0, s48, 71872\n"
    "v_max3_f32 v247, v12, v13, v14\n"
    "v_max_f32 v247, v247, v15\n"
    "v_mfma_f32_16x16x32_bf16 v[28:31], v[224:227], a[76:79], v[28:31]\n"
    "s_waitcnt lgkmcnt(2)\n"
    "buffer_load_dwordx4 v254, s[44:47], s52 offen offset:2048 lds\n"
    "v_max3_f32 v246, v8, v9, v10\n"
    "v_mfma_f32_16x16x32_bf16 v[28:31], v[232:235], a[108:111], v[28:31]\n"
    "v_max_f32 v246, v246, v11\n"
    "v_max3_f32 v245, v4, v5, v6\n"
    "v_mfma_f32_16x16x32_bf16 v[28:31], v[236:239], a[124:127], v[28:31]\n"
    "s_nop 0\n"
    "s_barrier\n"
    "v_mfma_f32_16x16x32_bf16 v[24:27], v[236:239], a[120:123], v[24:27]\n"
    "s_add_u32 m0, s48, 71904\n"
    "ds_read_b128 v[216:219], v252 offset:34176\n"
    "v_max_f32 v245, v245, v7\n"
    "v_mfma_f32_16x16x32_bf16 v[24:27], v[232:235], a[104:107], v[24:27]\n"
    "buffer_load_dwordx4 v254, s[44:47], s52 offen offset:3072 lds\n"
    "v_max3_f32 v244, v0, v1, v2\n"
    "v_mfma_f32_16x16x32_bf16 v[20:23], v[232:235], a[100:103], v[20:23]\n"
    "s_add_u32 s51, s51, 32768\n"
    "v_max_f32 v244, v244, v3\n"
    "v_mfma_f32_16x16x32_bf16 v[20:23], v[236:239], a[116:119], v[20:23]\n"
    "s_add_u32 s52, s52, 32768\n"
    "ds_read_b128 v[220:223], v252 offset:34240\n"
    "v_mfma_f32_16x16x32_bf16 v[16:19], v[236:239], a[112:115], v[16:19]\n"
    "s_add_u32 m0, s48, 0\n"
    "v_mfma_f32_16x16x32_bf16 v[16:19], v[232:235], a[96:99], v[16:19]\n"
    "s_waitcnt lgkmcnt(2)\n"
    "buffer_load_dwordx4 v254, s[40:43], s49 offen offset:0 lds\n"
    "v_mfma_f32_16x16x32_bf16 v[128:131], v[208:211], a[64:67], v[128:131]\n"
    "s_add_u32 m0, s48, 32\n"
    "v_mfma_f32_16x16x32_bf16 v[128:131], v[212:215], a[80:83], v[128:131]\n"
    "ds_read_b128 v[224:227], v252 offset:34560\n"
    "buffer_load_dwordx4 v254, s[40:43], s49 offen offset:1024 lds\n"
    "v_mfma_f32_16x16x32_bf16 v[132:135], v[212:215], a[84:87], v[132:135]\n"
    "s_add_u32 m0, s48, 64\n"
    "v_mfma_f32_16x16x32_bf16 v[132:135], v[208:211], a[68:71], v[132:135]\n"
    "buffer_load_dwordx4 v254, s[40:43], s49 offen offset:2048 lds\n"
    "v_mfma_f32_16x16x32_bf16 v[136:139], v[208:211], a[72:75], v[136:139]\n"
    "s_add_u32 m0, s48, 96\n"
    "v_mfma_f32_16x16x32_bf16 v[136:139], v[212:215], a[88:91], v[136:139]\n"
    "ds_read_b128 v[228:231], v252 offset:34624\n"
    "buffer_load_dwordx4 v254, s[40:43], s49 offen offset:3072 lds\n"
    "v_mfma_f32_16x16x32_bf16 v[140:143], v[212:215], a[92:95], v[140:143]\n"
    "s_add_u32 m0, s48, 4224\n"
    "v_max3_f32 v247, v247, v28, v29\n"
    "v_max3_f32 v247, v247, v30, v31\n"
    "v_mfma_f32_16x16x32_bf16 v[140:143], v[208:211], a[76:79], v[140:143]\n"
    "s_waitcnt lgkmcnt(2)\n"
    "buffer_load_dwordx4 v254, s[40:43], s50 offen offset:0 lds\n"
    "v_max3_f32 v246, v246, v24, v25\n"
    "v_mfma_f32_16x16x32_bf16 v[140:143], v[216:219], a[108:111], v[140:143]\n"
    "s_add_u32 m0, s48, 4256\n"
    "v_max3_f32 v246, v246, v26, v27\n"
    "v_max3_f32 v245, v245, v20, v21\n"
    "v_mfma_f32_16x16x32_bf16 v[140:143], v[220:223], a[124:127], v[140:143]\n"
    "ds_read_b128 v[232:235], v252 offset:34688\n"
    "buffer_load_dwordx4 v254, s[40:43], s50 offen offset:1024 lds\n"
    "v_mfma_f32_16x16x32_bf16 v[136:139], v[220:223], a[120:123], v[136:139]\n"
    "s_add_u32 m0, s48, 4288\n"
    "v_max3_f32 v245, v245, v22, v23\n"
    "v_max3_f32 v244, v244, v16, v17\n"
    "v_mfma_f32_16x16x32_bf16 v[136:139], v[216:219], a[104:107], v[136:139]\n"
    "buffer_load_dwordx4 v254, s[40:43], s50 offen offset:2048 lds\n"
    "v_max3_f32 v244, v244, v18, v19\n"
    "v_mfma_f32_16x16x32_bf16 v[132:135], v[216:219], a[100:103], v[132:135]\n"
    "s_add_u32 m0, s48, 4320\n"
    "v_mfma_f32_16x16x32_bf16 v[132:135], v[220:223], a[116:119], v[132:135]\n"
    "ds_read_b128 v[236:239], v252 offset:34752\n"
    "buffer_load_dwordx4 v254, s[40:43], s50 offen offset:3072 lds\n"
    "v_mfma_f32_16x16x32_bf16 v[128:131], v[220:223], a[112:115], v[128:131]\n"
    "s_add_u32 s49, s49, 32768\n"
    "v_mfma_f32_16x16x32_bf16 v[128:131], v[216:219], a[96:99], v[128:131]\n"
    "s_waitcnt lgkmcnt(2)\n"
    "v_mfma_f32_16x16x32_bf16 v[144:147], v[224:227], a[64:67], v[144:147]\n"
    "s_add_u32 s50, s50, 32768\n"
    "v_mfma_f32_16x16x32_bf16 v[144:147], v[228:231], a[80:83], v[144:147]\n"
    "v_mfma_f32_16x16x32_bf16 v[148:151], v[228:231], a[84:87], v[148:151]\n"
    "v_mfma_f32_16x16x32_bf16 v[148:151], v[224:227], a[68:71], v[148:151]\n"
    "v_mfma_f32_16x16x32_bf16 v[152:155], v[224:227], a[72:75], v[152:155]\n"
    "v_mfma_f32_16x16x32_bf16 v[152:155], v[228:231], a[88:91], v[152:155]\n"
    "v_mfma_f32_16x16x32_bf16 v[156:159], v[228:231], a[92:95], v[156:159]\n"
    "v_max3_f32 v247, v247, v140, v141\n"
    "v_max3_f32 v247, v247, v142, v143\n"
    "v_mfma_f32_16x16x32_bf16 v[156:159], v[224:227], a[76:79], v[156:159]\n"
    "s_waitcnt lgkmcnt(0)\n"
    "v_max3_f32 v246, v246, v136, v137\n"
    "v_max3_f32 v246, v246, v138, v139\n"
    "v_mfma_f32_16x16x32_bf16 v[156:159], v[232:235], a[108:111], v[156:159]\n"
    "v_max3_f32 v245, v245, v132, v133\n"
    "v_max3_f32 v245, v245, v134, v135\n"
    "v_mfma_f32_16x16x32_bf16 v[156:159], v[236:239], a[124:127], v[156:159]\n"
    "v_max3_f32 v244, v244, v128, v129\n"
    "v_max3_f32 v244, v244, v130, v131\n"
    "v_mfma_f32_16x16x32_bf16 v[152:155], v[236:239], a[120:123], v[152:155]\n"
    "v_mfma_f32_16x16x32_bf16 v[152:155], v[232:235], a[104:107], v[152:155]\n"
    "v_mfma_f32_16x16x32_bf16 v[148:151], v[232:235], a[100:103], v[148:151]\n"
    "v_mfma_f32_16x16x32_bf16 v[148:151], v[236:239], a[116:119], v[148:151]\n"
    "v_mfma_f32_16x16x32_bf16 v[144:147], v[236:239], a[112:115], v[144:147]\n"
    "v_mfma_f32_16x16x32_bf16 v[144:147], v[232:235], a[96:99], v[144:147]\n"
    "s_nop 3\n"
    "v_max3_f32 v247, v247, v156, v157\n"
    "v_max3_f32 v247, v247, v158, v159\n"
    "v_max3_f32 v246, v246, v152, v153\n"
    "v_max3_f32 v246, v246, v154, v155\n"
    "v_max3_f32 v245, v245, v148, v149\n"
    "v_max3_f32 v245, v245, v150, v151\n"
    "v_max3_f32 v244, v244, v144, v145\n"
    "v_max3_f32 v244, v244, v146, v147\n"
    "v_mov_b32 v240, v244\n"
    "v_mov_b32 v241, v245\n"
    "v_mov_b32 v242, v246\n"
    "v_mov_b32 v243, v247\n"
    "v_permlane16_swap_b32 v244, v240\n"
    "v_permlane16_swap_b32 v245, v241\n"
    "v_permlane16_swap_b32 v246, v242\n"
    "v_permlane16_swap_b32 v247, v243\n"
    "v_max_f32 v244, v244, v240\n"
    "v_max_f32 v245, v245, v241\n"
    "v_max_f32 v246, v246, v242\n"
    "v_max_f32 v247, v247, v243\n"
    "v_mov_b32 v240, v244\n"
    "v_mov_b32 v241, v245\n"
    "v_mov_b32 v242, v246\n"
    "v_mov_b32 v243, v247\n"
    "v_permlane32_swap_b32 v244, v240\n"
    "v_permlane32_swap_b32 v245, v241\n"
    "v_permlane32_swap_b32 v246, v242\n"
    "v_permlane32_swap_b32 v247, v243\n"
    "v_max_f32 v244, v244, v240\n"
    "v_max_f32 v245, v245, v241\n"
    "v_max_f32 v246, v246, v242\n"
    "v_max_f32 v247, v247, v243\n"
    "ds_write_b128 v248, v[244:247]\n"
    "s_waitcnt vmcnt(8)\n"
    "s_waitcnt lgkmcnt(0)\n"
    "s_barrier\n"
    "ds_read_b128 v[240:243], v249\n"
    "ds_read_b64_tr_b16 v[208:209], v253 offset:0\n"
    "ds_read_b64_tr_b16 v[210:211], v253 offset:512\n"
    "ds_read_b64_tr_b16 v[212:213], v253 offset:32\n"
    "ds_read_b64_tr_b16 v[214:215], v253 offset:544\n"
    "ds_read_b64_tr_b16 v[216:217], v253 offset:64\n"
    "ds_read_b64_tr_b16 v[218:219], v253 offset:576\n"
    "ds_read_b64_tr_b16 v[220:221], v253 offset:96\n"
    "ds_read_b64_tr_b16 v[222:223], v253 offset:608\n"
    "ds_read_b64_tr_b16 v[224:225], v253 offset:128\n"
    "ds_read_b64_tr_b16 v[226:227], v253 offset:640\n"
    "s_waitcnt lgkmcnt(10)\n"
    "v_max_f32 v244, v244, v242\n"
    "v_max_f32 v245, v245, v243\n"
    "v_max_f32 v246, v246, v240\n"
    "v_max_f32 v247, v247, v241\n"
    "v_sub_f32 v192, -102.0, v244\n"
    "v_sub_f32 v196, -102.0, v245\n"
    "v_sub_f32 v200, -102.0, v246\n"
    "v_sub_f32 v204, -102.0, v247\n"
    "v_add_f32 v12, v12, v204\n"
    "v_add_f32 v13, v13, v204\n"
    "v_add_f32 v14, v14, v204\n"
    "v_add_f32 v15, v15, v204\n"
    "s_add_u32 m0, s48, 130048\n"
    "v_exp_f32 v240, v12\n"
    "v_exp_f32 v241, v13\n"
    "buffer_load_dwordx4 v254, s[44:47], s51 offen offset:0 lds\n"
    "v_add_f32 v8, v8, v200\n"
    "v_add_f32 v9, v9, v200\n"
    "v_exp_f32 v242, v14\n"
    "v_exp_f32 v243, v15\n"
    "v_add_f32 v251, v240, v241\n"
    "v_cvt_pk_bf16_f32 v172, v240, v241\n"
    "v_add_f32 v10, v10, v200\n"
    "v_add_f32 v11, v11, v200\n"
    "s_add_u32 m0, s48, 130080\n"
    "v_exp_f32 v240, v8\n"
    "v_exp_f32 v241, v9\n"
    "v_add_f32 v251, v251, v242\n"
    "v_add_f32 v251, v251, v243\n"
    "v_cvt_pk_bf16_f32 v173, v242, v243\n"
    "buffer_load_dwordx4 v254, s[44:47], s51 offen offset:1024 lds\n"
    "v_add_f32 v4, v4, v196\n"
    "v_add_f32 v5, v5, v196\n"
    "v_exp_f32 v242, v10\n"
    "v_exp_f32 v243, v11\n"
    "v_add_f32 v250, v240, v241\n"
    "v_cvt_pk_bf16_f32 v168, v240, v241\n"
    "v_add_f32 v6, v6, v196\n"
    "v_add_f32 v7, v7, v196\n"
    "s_add_u32 m0, s48, 130112\n"
    "v_exp_f32 v240, v4\n"
    "v_exp_f32 v241, v5\n"
    "v_add_f32 v250, v250, v242\n"
    "v_add_f32 v250, v250, v243\n"
    "v_cvt_pk_bf16_f32 v169, v242, v243\n"
    "buffer_load_dwordx4 v254, s[44:47], s51 offen offset:2048 lds\n"
    "v_add_f32 v0, v0, v192\n"
    "v_add_f32 v1, v1, v192\n"
    "v_exp_f32 v242, v6\n"
    "v_exp_f32 v243, v7\n"
    "v_add_f32 v249, v240, v241\n"
    "v_cvt_pk_bf16_f32 v164, v240, v241\n"
    "v_add_f32 v2, v2, v192\n"
    "v_add_f32 v3, v3, v192\n"
    "s_add_u32 m0, s48, 130144\n"
    "v_exp_f32 v240, v0\n"
    "v_exp_f32 v241, v1\n"
    "v_add_f32 v249, v249, v242\n"
    "v_add_f32 v249, v249, v243\n"
    "v_cvt_pk_bf16_f32 v165, v242, v243\n"
    "buffer_load_dwordx4 v254, s[44:47], s51 offen offset:3072 lds\n"
    "v_add_f32 v28, v28, v204\n"
    "v_add_f32 v29, v29, v204\n"
    "v_exp_f32 v242, v2\n"
    "v_exp_f32 v243, v3\n"
    "v_add_f32 v248, v240, v241\n"
    "v_cvt_pk_bf16_f32 v160, v240, v241\n"
    "v_add_f32 v30, v30, v204\n"
    "v_add_f32 v31, v31, v204\n"
    "s_add_u32 m0, s48, 134272\n"
    "v_exp_f32 v240, v28\n"
    "v_exp_f32 v241, v29\n"
    "v_add_f32 v248, v248, v242\n"
    "v_add_f32 v248, v248, v243\n"
    "v_cvt_pk_bf16_f32 v161, v242, v243\n"
    "buffer_load_dwordx4 v254, s[44:47], s52 offen offset:0 lds\n"
    "v_add_f32 v24, v24, v200\n"
    "v_add_f32 v25, v25, v200\n"
    "v_exp_f32 v242, v30\n"
    "v_exp_f32 v243, v31\n"
    "v_add_f32 v251, v251, v240\n"
    "v_add_f32 v251, v251, v241\n"
    "v_cvt_pk_bf16_f32 v174, v240, v241\n"
    "v_add_f32 v26, v26, v200\n"
    "v_add_f32 v27, v27, v200\n"
    "s_add_u32 m0, s48, 134304\n"
    "v_exp_f32 v240, v24\n"
    "v_exp_f32 v241, v25\n"
    "v_add_f32 v251, v251, v242\n"
    "v_add_f32 v251, v251, v243\n"
    "v_cvt_pk_bf16_f32 v175, v242, v243\n"
    "buffer_load_dwordx4 v254, s[44:47], s52 offen offset:1024 lds\n"
    "v_add_f32 v20, v20, v196\n"
    "v_add_f32 v21, v21, v196\n"
    "v_exp_f32 v242, v26\n"
    "v_exp_f32 v243, v27\n"
    "v_add_f32 v250, v250, v240\n"
    "v_add_f32 v250, v250, v241\n"
    "v_cvt_pk_bf16_f32 v170, v240, v241\n"
    "v_add_f32 v22, v22, v196\n"
    "v_add_f32 v23, v23, v196\n"
    "s_add_u32 m0, s48, 134336\n"
    "v_exp_f32 v240, v20\n"
    "v_exp_f32 v241, v21\n"
    "v_add_f32 v250, v250, v242\n"
    "v_add_f32 v250, v250, v243\n"
    "v_cvt_pk_bf16_f32 v171, v242, v243\n"
    "buffer_load_dwordx4 v254, s[44:47], s52 offen offset:2048 lds\n"
    "v_add_f32 v16, v16, v192\n"
    "v_add_f32 v17, v17, v192\n"
    "v_exp_f32 v242, v22\n"
    "v_exp_f32 v243, v23\n"
    "v_add_f32 v249, v249, v240\n"
    "v_add_f32 v249, v249, v241\n"
    "v_cvt_pk_bf16_f32 v166, v240, v241\n"
    "v_add_f32 v18, v18, v192\n"
    "v_add_f32 v19, v19, v192\n"
    "s_add_u32 m0, s48, 134368\n"
    "v_exp_f32 v240, v16\n"
    "v_exp_f32 v241, v17\n"
    "v_add_f32 v249, v249, v242\n"
    "v_add_f32 v249, v249, v243\n"
    "v_cvt_pk_bf16_f32 v167, v242, v243\n"
    "buffer_load_dwordx4 v254, s[44:47], s52 offen offset:3072 lds\n"
    "v_exp_f32 v242, v18\n"
    "v_exp_f32 v243, v19\n"
    "v_add_f32 v248, v248, v240\n"
    "v_add_f32 v248, v248, v241\n"
    "v_cvt_pk_bf16_f32 v162, v240, v241\n"
    "s_add_u32 s51, s51, 32768\n"
    "s_add_u32 s52, s52, 32768\n"
    "v_add_f32 v248, v248, v242\n"
    "v_add_f32 v248, v248, v243\n"
    "v_cvt_pk_bf16_f32 v163, v242, v243\n"
    "s_waitcnt lgkmcnt(6)\n"
    "s_nop 0\n"
    "v_mfma_f32_16x16x32_bf16 v[0:3], v[208:211], v[160:163], 0\n"
    "s_mov_b32 m0, 114688\n"
    "ds_read_b64_tr_b16 v[228:229], v253 offset:160\n"
    "ds_read_b64_tr_b16 v[230:231], v253 offset:672\n"
    "v_mfma_f32_16x16x32_bf16 v[8:11], v[212:215], v[160:163], 0\n"
    "buffer_load_dword v254, s[44:47], s53 offen lds\n"
    "v_add_f32 v140, v140, v204\n"
    "v_add_f32 v141, v141, v204\n"
    "v_add_f32 v142, v142, v204\n"
    "v_mfma_f32_16x16x32_bf16 v[12:15], v[212:215], v[164:167], 0\n"
    "v_add_f32 v143, v143, v204\n"
    "v_exp_f32 v240, v140\n"
    "v_mfma_f32_16x16x32_bf16 v[4:7], v[208:211], v[164:167], 0\n"
    "v_exp_f32 v241, v141\n"
    "v_add_f32 v136, v136, v200\n"
    "v_add_f32 v137, v137, v200\n"
    "v_mfma_f32_16x16x32_bf16 a[128:131], v[208:211], v[168:171], 0\n"
    "s_add_u32 s53, s53, 65536\n"
    "ds_read_b64_tr_b16 v[232:233], v253 offset:192\n"
    "ds_read_b64_tr_b16 v[234:235], v253 offset:704\n"
    "v_mfma_f32_16x16x32_bf16 a[136:139], v[212:215], v[168:171], 0\n"
    "v_exp_f32 v242, v142\n"
    "v_exp_f32 v243, v143\n"
    "v_mfma_f32_16x16x32_bf16 a[140:143], v[212:215], v[172:175], 0\n"
    "v_add_f32 v247, v240, v241\n"
    "v_cvt_pk_bf16_f32 v188, v240, v241\n"
    "v_add_f32 v138, v138, v200\n"
    "v_add_f32 v139, v139, v200\n"
    "v_mfma_f32_16x16x32_bf16 a[132:135], v[208:211], v[172:175], 0\n"
    "s_waitcnt lgkmcnt(6)\n"
    "v_exp_f32 v240, v136\n"
    "v_exp_f32 v241, v137\n"
    "v_mfma_f32_16x16x32_bf16 a[148:151], v[216:219], v[172:175], 0\n"
    "ds_read_b64_tr_b16 v[236:237], v253 offset:224\n"
    "ds_read_b64_tr_b16 v[238:239], v253 offset:736\n"
    "v_mfma_f32_16x16x32_bf16 a[156:159], v[220:223], v[172:175], 0\n"
    "v_add_f32 v247, v247, v242\n"
    "v_add_f32 v247, v247, v243\n"
    "v_cvt_pk_bf16_f32 v189, v242, v243\n"
    "v_add_f32 v132, v132, v196\n"
    "v_mfma_f32_16x16x32_bf16 a[152:155], v[220:223], v[168:171], 0\n"
    "v_add_f32 v133, v133, v196\n"
    "v_exp_f32 v242, v138\n"
    "v_mfma_f32_16x16x32_bf16 a[144:147], v[216:219], v[168:171], 0\n"
    "v_exp_f32 v243, v139\n"
    "v_add_f32 v246, v240, v241\n"
    "v_cvt_pk_bf16_f32 v184, v240, v241\n"
    "v_mfma_f32_16x16x32_bf16 v[20:23], v[216:219], v[164:167], 0\n"
    "ds_read_b64_tr_b16 v[208:209], v253 offset:256\n"
    "ds_read_b64_tr_b16 v[210:211], v253 offset:768\n"
    "v_mfma_f32_16x16x32_bf16 v[28:31], v[220:223], v[164:167], 0\n"
    "v_add_f32 v134, v134, v196\n"
    "v_add_f32 v135, v135, v196\n"
    "v_exp_f32 v240, v132\n"
    "v_mfma_f32_16x16x32_bf16 v[24:27], v[220:223], v[160:163], 0\n"
    "v_exp_f32 v241, v133\n"
    "v_add_f32 v246, v246, v242\n"
    "v_add_f32 v246, v246, v243\n"
    "v_mfma_f32_16x16x32_bf16 v[16:19], v[216:219], v[160:163], 0\n"
    "s_waitcnt lgkmcnt(6)\n"
    "v_cvt_pk_bf16_f32 v185, v242, v243\n"
    "v_add_f32 v128, v128, v192\n"
    "v_add_f32 v129, v129, v192\n"
    "v_mfma_f32_16x16x32_bf16 v[32:35], v[224:227], v[160:163], 0\n"
    "ds_read_b64_tr_b16 v[212:213], v253 offset:288\n"
    "ds_read_b64_tr_b16 v[214:215], v253 offset:800\n"
    "v_mfma_f32_16x16x32_bf16 v[40:43], v[228:231], v[160:163], 0\n"
    "v_exp_f32 v242, v134\n"
    "v_exp_f32 v243, v135\n"
    "v_mfma_f32_16x16x32_bf16 v[44:47], v[228:231], v[164:167], 0\n"
    "v_add_f32 v245, v240, v241\n"
    "v_cvt_pk_bf16_f32 v180, v240, v241\n"
    "v_add_f32 v130, v130, v192\n"
    "v_add_f32 v131, v131, v192\n"
    "v_mfma_f32_16x16x32_bf16 v[36:39], v[224:227], v[164:167], 0\n"
    "v_exp_f32 v240, v128\n"
    "v_exp_f32 v241, v129\n"
    "v_mfma_f32_16x16x32_bf16 a[160:163], v[224:227], v[168:171], 0\n"
    "ds_read_b64_tr_b16 v[216:217], v253 offset:320\n"
    "ds_read_b64_tr_b16 v[218:219], v253 offset:832\n"
    "v_mfma_f32_16x16x32_bf16 a[168:171], v[228:231], v[168:171], 0\n"
    "v_add_f32 v245, v245, v242\n"
    "v_add_f32 v245, v245, v243\n"
    "v_cvt_pk_bf16_f32 v181, v242, v243\n"
    "v_add_f32 v156, v156, v204\n"
    "v_mfma_f32_16x16x32_bf16 a[172:175], v[228:231], v[172:175], 0\n"
    "v_add_f32 v157, v157, v204\n"
    "v_exp_f32 v242, v130\n"
    "v_mfma_f32_16x16x32_bf16 a[164:167], v[224:227], v[172:175], 0\n"
    "s_waitcnt lgkmcnt(6)\n"
    "v_exp_f32 v243, v131\n"
    "v_add_f32 v244, v240, v241\n"
    "v_cvt_pk_bf16_f32 v176, v240, v241\n"
    "v_mfma_f32_16x16x32_bf16 a[180:183], v[232:235], v[172:175], 0\n"
    "ds_read_b64_tr_b16 v[220:221], v253 offset:352\n"
    "ds_read_b64_tr_b16 v[222:223], v253 offset:864\n"
    "v_mfma_f32_16x16x32_bf16 a[188:191], v[236:239], v[172:175], 0\n"
    "ds_read_b64_tr_b16 v[224:225], v253 offset:384\n"
    "ds_read_b64_tr_b16 v[226:227], v253 offset:896\n"
    "v_mfma_f32_16x16x32_bf16 a[184:187], v[236:239], v[168:171], 0\n"
    "v_add_f32 v158, v158, v204\n"
    "v_add_f32 v159, v159, v204\n"
    "v_exp_f32 v240, v156\n"
    "v_mfma_f32_16x16x32_bf16 a[176:179], v[232:235], v[168:171], 0\n"
    "v_exp_f32 v241, v157\n"
    "v_add_f32 v244, v244, v242\n"
    "v_add_f32 v244, v244, v243\n"
    "v_mfma_f32_16x16x32_bf16 v[52:55], v[232:235], v[164:167], 0\n"
    "ds_read_b64_tr_b16 v[228:229], v253 offset:416\n"
    "ds_read_b64_tr_b16 v[230:231], v253 offset:928\n"
    "v_mfma_f32_16x16x32_bf16 v[60:63], v[236:239], v[164:167], 0\n"
    "v_cvt_pk_bf16_f32 v177, v242, v243\n"
    "v_add_f32 v152, v152, v200\n"
    "v_add_f32 v153, v153, v200\n"
    "v_mfma_f32_16x16x32_bf16 v[56:59], v[236:239], v[160:163], 0\n"
    "v_exp_f32 v242, v158\n"
    "v_exp_f32 v243, v159\n"
    "v_mfma_f32_16x16x32_bf16 v[48:51], v[232:235], v[160:163], 0\n"
    "s_waitcnt lgkmcnt(8)\n"
    "v_add_f32 v247, v247, v240\n"
    "v_add_f32 v247, v247, v241\n"
    "v_cvt_pk_bf16_f32 v190, v240, v241\n"
    "v_add_f32 v154, v154, v200\n"
    "v_mfma_f32_16x16x32_bf16 v[64:67], v[208:211], v[160:163], 0\n"
    "ds_read_b64_tr_b16 v[232:233], v253 offset:448\n"
    "ds_read_b64_tr_b16 v[234:235], v253 offset:960\n"
    "v_mfma_f32_16x16x32_bf16 v[72:75], v[212:215], v[160:163], 0\n"
    "ds_read_b64_tr_b16 v[236:237], v253 offset:480\n"
    "ds_read_b64_tr_b16 v[238:239], v253 offset:992\n"
    "v_mfma_f32_16x16x32_bf16 v[76:79], v[212:215], v[164:167], 0\n"
    "v_add_f32 v155, v155, v200\n"
    "v_exp_f32 v240, v152\n"
    "v_mfma_f32_16x16x32_bf16 v[68:71], v[208:211], v[164:167], 0\n"
    "v_exp_f32 v241, v153\n"
    "v_add_f32 v247, v247, v242\n"
    "v_add_f32 v247, v247, v243\n"
    "v_mfma_f32_16x16x32_bf16 a[192:195], v[208:211], v[168:171], 0\n"
    "v_cvt_pk_bf16_f32 v191, v242, v243\n"
    "v_add_f32 v148, v148, v196\n"
    "v_add_f32 v149, v149, v196\n"
    "v_mfma_f32_16x16x32_bf16 a[200:203], v[212:215], v[168:171], 0\n"
    "v_exp_f32 v242, v154\n"
    "v_exp_f32 v243, v155\n"
    "v_mfma_f32_16x16x32_bf16 a[204:207], v[212:215], v[172:175], 0\n"
    "v_add_f32 v246, v246, v240\n"
    "v_add_f32 v246, v246, v241\n"
    "v_cvt_pk_bf16_f32 v186, v240, v241\n"
    "v_add_f32 v150, v150, v196\n"
    "v_mfma_f32_16x16x32_bf16 a[196:199], v[208:211], v[172:175], 0\n"
    "s_waitcnt lgkmcnt(8)\n"
    "v_add_f32 v151, v151, v196\n"
    "v_exp_f32 v240, v148\n"
    "v_mfma_f32_16x16x32_bf16 a[212:215], v[216:219], v[172:175], 0\n"
    "v_exp_f32 v241, v149\n"
    "v_add_f32 v246, v246, v242\n"
    "v_add_f32 v246, v246, v243\n"
    "v_mfma_f32_16x16x32_bf16 a[220:223], v[220:223], v[172:175], 0\n"
    "v_cvt_pk_bf16_f32 v187, v242, v243\n"
    "v_add_f32 v144, v144, v192\n"
    "v_add_f32 v145, v145, v192\n"
    "v_mfma_f32_16x16x32_bf16 a[216:219], v[220:223], v[168:171], 0\n"
    "v_exp_f32 v242, v150\n"
    "v_exp_f32 v243, v151\n"
    "v_mfma_f32_16x16x32_bf16 a[208:211], v[216:219], v[168:171], 0\n"
    "v_add_f32 v245, v245, v240\n"
    "v_add_f32 v245, v245, v241\n"
    "v_cvt_pk_bf16_f32 v182, v240, v241\n"
    "v_add_f32 v146, v146, v192\n"
    "v_mfma_f32_16x16x32_bf16 v[84:87], v[216:219], v[164:167], 0\n"
    "s_waitcnt vmcnt(1) lgkmcnt(0)\n"
    "s_barrier\n"
    "v_mfma_f32_16x16x32_bf16 v[92:95], v[220:223], v[164:167], 0\n"
    "ds_read_b128 v[208:211], v252 offset:0\n"
    "v_add_f32 v147, v147, v192\n"
    "v_mfma_f32_16x16x32_bf16 v[88:91], v[220:223], v[160:163], 0\n"
    "v_exp_f32 v240, v144\n"
    "v_exp_f32 v241, v145\n"
    "v_mfma_f32_16x16x32_bf16 v[80:83], v[216:219], v[160:163], 0\n"
    "v_add_f32 v245, v245, v242\n"
    "v_add_f32 v245, v245, v243\n"
    "v_cvt_pk_bf16_f32 v183, v242, v243\n"
    "v_mfma_f32_16x16x32_bf16 v[96:99], v[224:227], v[160:163], 0\n"
    "v_exp_f32 v242, v146\n"
    "v_exp_f32 v243, v147\n"
    "v_mfma_f32_16x16x32_bf16 v[104:107], v[228:231], v[160:163], 0\n"
    "ds_read_b128 v[212:215], v252 offset:64\n"
    "v_add_f32 v244, v244, v240\n"
    "v_mfma_f32_16x16x32_bf16 v[108:111], v[228:231], v[164:167], 0\n"
    "v_add_f32 v244, v244, v241\n"
    "v_cvt_pk_bf16_f32 v178, v240, v241\n"
    "v_add_f32 v244, v244, v242\n"
    "v_add_f32 v244, v244, v243\n"
    "v_mfma_f32_16x16x32_bf16 v[100:103], v[224:227], v[164:167], 0\n"
    "v_cvt_pk_bf16_f32 v179, v242, v243\n"
    "v_mov_b32 v193, v192\n"
    "v_mov_b32 v194, v192\n"
    "v_mov_b32 v195, v192\n"
    "v_mfma_f32_16x16x32_bf16 a[224:227], v[224:227], v[168:171], 0\n"
    "v_mov_b32 v197, v196\n"
    "v_mov_b32 v198, v196\n"
    "v_mov_b32 v199, v196\n"
    "v_mov_b32 v201, v200\n"
    "v_mfma_f32_16x16x32_bf16 a[232:235], v[228:231], v[168:171], 0\n"
    "ds_read_b128 v[216:219], v252 offset:128\n"
    "v_mov_b32 v202, v200\n"
    "v_mfma_f32_16x16x32_bf16 a[236:239], v[228:231], v[172:175], 0\n"
    "v_mov_b32 v203, v200\n"
    "v_mov_b32 v205, v204\n"
    "v_mov_b32 v206, v204\n"
    "v_mov_b32 v207, v204\n"
    "v_mfma_f32_16x16x32_bf16 a[228:231], v[224:227], v[172:175], 0\n"
    "v_mfma_f32_16x16x32_bf16 a[244:247], v[232:235], v[172:175], 0\n"
    "v_mfma_f32_16x16x32_bf16 a[252:255], v[236:239], v[172:175], 0\n"
    "ds_read_b128 v[220:223], v252 offset:192\n"
    "v_mfma_f32_16x16x32_bf16 a[248:251], v[236:239], v[168:171], 0\n"
    "v_mfma_f32_16x16x32_bf16 a[240:243], v[232:235], v[168:171], 0\n"
    "v_mfma_f32_16x16x32_bf16 v[116:119], v[232:235], v[164:167], 0\n"
    "v_mfma_f32_16x16x32_bf16 v[124:127], v[236:239], v[164:167], 0\n"
    "ds_read_b128 v[224:227], v252 offset:256\n"
    "v_mfma_f32_16x16x32_bf16 v[120:123], v[236:239], v[160:163], 0\n"
    "v_mfma_f32_16x16x32_bf16 v[112:115], v[232:235], v[160:163], 0\n"
    "s_waitcnt lgkmcnt(3)\n"
    "s_mov_b32 s55, 15\n"
    ".p2align 6\n"
    "LOOP_%=:\n"
    "v_mfma_f32_16x16x32_bf16 v[128:131], v[208:211], a[0:3], v[192:195]\n"
    "s_add_u32 m0, s48, 33792\n"
    "ds_read_b128 v[228:231], v252 offset:320\n"
    "v_mfma_f32_16x16x32_bf16 v[128:131], v[212:215], a[16:19], v[128:131]\n"
    "buffer_load_dwordx4 v254, s[40:43], s49 offen offset:0 lds\n"
    "v_mfma_f32_16x16x32_bf16 v[132:135], v[212:215], a[20:23], v[196:199]\n"
    "v_add_f32 v248, v248, v244\n"
    "v_add_f32 v249, v249, v245\n"
    "v_mfma_f32_16x16x32_bf16 v[132:135], v[208:211], a[4:7], v[132:135]\n"
    "v_add_f32 v250, v250, v246\n"
    "v_add_f32 v251, v251, v247\n"
    "v_mfma_f32_16x16x32_bf16 v[136:139], v[208:211], a[8:11], v[200:203]\n"
    "s_add_u32 m0, s48, 33824\n"
    "ds_read_b128 v[232:235], v252 offset:384\n"
    "v_mfma_f32_16x16x32_bf16 v[136:139], v[212:215], a[24:27], v[136:139]\n"
    "buffer_load_dwordx4 v254, s[40:43], s49 offen offset:1024 lds\n"
    "v_mfma_f32_16x16x32_bf16 v[140:143], v[212:215], a[28:31], v[204:207]\n"
    "v_mfma_f32_16x16x32_bf16 v[140:143], v[208:211], a[12:15], v[140:143]\n"
    "s_waitcnt lgkmcnt(3)\n"
    "v_mfma_f32_16x16x32_bf16 v[140:143], v[216:219], a[44:47], v[140:143]\n"
    "s_add_u32 m0, s48, 33856\n"
    "ds_read_b128 v[236:239], v252 offset:448\n"
    "v_mfma_f32_16x16x32_bf16 v[140:143], v[220:223], a[60:63], v[140:143]\n"
    "buffer_load_dwordx4 v254, s[40:43], s49 offen offset:2048 lds\n"
    "v_mfma_f32_16x16x32_bf16 v[136:139], v[220:223], a[56:59], v[136:139]\n"
    "v_mfma_f32_16x16x32_bf16 v[136:139], v[216:219], a[40:43], v[136:139]\n"
    "v_mfma_f32_16x16x32_bf16 v[132:135], v[216:219], a[36:39], v[132:135]\n"
    "s_add_u32 m0, s48, 33888\n"
    "ds_read_b128 v[208:211], v252 offset:512\n"
    "v_mfma_f32_16x16x32_bf16 v[132:135], v[220:223], a[52:55], v[132:135]\n"
    "buffer_load_dwordx4 v254, s[40:43], s49 offen offset:3072 lds\n"
    "v_mfma_f32_16x16x32_bf16 v[128:131], v[220:223], a[48:51], v[128:131]\n"
    "v_mfma_f32_16x16x32_bf16 v[128:131], v[216:219], a[32:35], v[128:131]\n"
    "s_waitcnt lgkmcnt(3)\n"
    "v_mfma_f32_16x16x32_bf16 v[128:131], v[224:227], a[64:67], v[128:131]\n"
    "s_add_u32 m0, s48, 38016\n"
    "ds_read_b128 v[212:215], v252 offset:576\n"
    "v_mfma_f32_16x16x32_bf16 v[128:131], v[228:231], a[80:83], v[128:131]\n"
    "buffer_load_dwordx4 v254, s[40:43], s50 offen offset:0 lds\n"
    "v_mfma_f32_16x16x32_bf16 v[132:135], v[228:231], a[84:87], v[132:135]\n"
    "v_mfma_f32_16x16x32_bf16 v[132:135], v[224:227], a[68:71], v[132:135]\n"
    "v_mfma_f32_16x16x32_bf16 v[136:139], v[224:227], a[72:75], v[136:139]\n"
    "s_add_u32 m0, s48, 38048\n"
    "ds_read_b128 v[216:219], v252 offset:640\n"
    "v_mfma_f32_16x16x32_bf16 v[136:139], v[228:231], a[88:91], v[136:139]\n"
    "buffer_load_dwordx4 v254, s[40:43], s50 offen offset:1024 lds\n"
    "v_mfma_f32_16x16x32_bf16 v[140:143], v[228:231], a[92:95], v[140:143]\n"
    "v_mfma_f32_16x16x32_bf16 v[140:143], v[224:227], a[76:79], v[140:143]\n"
    "s_waitcnt lgkmcnt(3)\n"
    "v_mfma_f32_16x16x32_bf16 v[140:143], v[232:235], a[108:111], v[140:143]\n"
    "s_add_u32 m0, s48, 38080\n"
    "ds_read_b128 v[220:223], v252 offset:704\n"
    "v_mfma_f32_16x16x32_bf16 v[140:143], v[236:239], a[124:127], v[140:143]\n"
    "buffer_load_dwordx4 v254, s[40:43], s50 offen offset:2048 lds\n"
    "v_mfma_f32_16x16x32_bf16 v[136:139], v[236:239], a[120:123], v[136:139]\n"
    "v_mfma_f32_16x16x32_bf16 v[136:139], v[232:235], a[104:107], v[136:139]\n"
    "v_mfma_f32_16x16x32_bf16 v[132:135], v[232:235], a[100:103], v[132:135]\n"
    "s_add_u32 m0, s48, 38112\n"
    "ds_read_b128 v[224:227], v252 offset:768\n"
    "v_mfma_f32_16x16x32_bf16 v[132:135], v[236:239], a[116:119], v[132:135]\n"
    "buffer_load_dwordx4 v254, s[40:43], s50 offen offset:3072 lds\n"
    "v_mfma_f32_16x16x32_bf16 v[128:131], v[236:239], a[112:115], v[128:131]\n"
    "v_mfma_f32_16x16x32_bf16 v[128:131], v[232:235], a[96:99], v[128:131]\n"
    "s_waitcnt lgkmcnt(3)\n"
    "v_mfma_f32_16x16x32_bf16 v[144:147], v[208:211], a[0:3], v[192:195]\n"
    "s_add_u32 m0, s48, 67584\n"
    "ds_read_b128 v[228:231], v252 offset:832\n"
    "v_mfma_f32_16x16x32_bf16 v[144:147], v[212:215], a[16:19], v[144:147]\n"
    "buffer_load_dwordx4 v254, s[44:47], s51 offen offset:0 lds\n"
    "v_mfma_f32_16x16x32_bf16 v[148:151], v[212:215], a[20:23], v[196:199]\n"
    "v_exp_f32 v240, v140\n"
    "v_mfma_f32_16x16x32_bf16 v[148:151], v[208:211], a[4:7], v[148:151]\n"
    "v_exp_f32 v241, v141\n"
    "v_mfma_f32_16x16x32_bf16 v[152:155], v[208:211], a[8:11], v[200:203]\n"
    "s_add_u32 m0, s48, 67616\n"
    "ds_read_b128 v[232:235], v252 offset:896\n"
    "v_add_f32 v247, v240, v241\n"
    "v_mfma_f32_16x16x32_bf16 v[152:155], v[212:215], a[24:27], v[152:155]\n"
    "buffer_load_dwordx4 v254, s[44:47], s51 offen offset:1024 lds\n"
    "v_cvt_pk_bf16_f32 v172, v240, v241\n"
    "v_mfma_f32_16x16x32_bf16 v[156:159], v[212:215], a[28:31], v[204:207]\n"
    "v_exp_f32 v242, v142\n"
    "v_mfma_f32_16x16x32_bf16 v[156:159], v[208:211], a[12:15], v[156:159]\n"
    "s_waitcnt lgkmcnt(3)\n"
    "v_exp_f32 v243, v143\n"
    "v_mfma_f32_16x16x32_bf16 v[156:159], v[216:219], a[44:47], v[156:159]\n"
    "s_add_u32 m0, s48, 67648\n"
    "ds_read_b128 v[236:239], v252 offset:960\n"
    "v_add_f32 v247, v247, v242\n"
    "v_mfma_f32_16x16x32_bf16 v[156:159], v[220:223], a[60:63], v[156:159]\n"
    "buffer_load_dwordx4 v254, s[44:47], s51 offen offset:2048 lds\n"
    "v_add_f32 v247, v247, v243\n"
    "v_mfma_f32_16x16x32_bf16 v[152:155], v[220:223], a[56:59], v[152:155]\n"
    "v_cvt_pk_bf16_f32 v173, v242, v243\n"
    "v_mfma_f32_16x16x32_bf16 v[152:155], v[216:219], a[40:43], v[152:155]\n"
    "v_exp_f32 v240, v136\n"
    "v_mfma_f32_16x16x32_bf16 v[148:151], v[216:219], a[36:39], v[148:151]\n"
    "s_add_u32 m0, s48, 67680\n"
    "ds_read_b64_tr_b16 v[208:209], v253 offset:62464\n"
    "ds_read_b64_tr_b16 v[210:211], v253 offset:62976\n"
    "v_mfma_f32_16x16x32_bf16 v[148:151], v[220:223], a[52:55], v[148:151]\n"
    "buffer_load_dwordx4 v254, s[44:47], s51 offen offset:3072 lds\n"
    "v_mfma_f32_16x16x32_bf16 v[144:147], v[220:223], a[48:51], v[144:147]\n"
    "v_exp_f32 v241, v137\n"
    "v_mfma_f32_16x16x32_bf16 v[144:147], v[216:219], a[32:35], v[144:147]\n"
    "s_waitcnt lgkmcnt(4)\n"
    "v_add_f32 v246, v240, v241\n"
    "v_cvt_pk_bf16_f32 v168, v240, v241\n"
    "v_mfma_f32_16x16x32_bf16 v[144:147], v[224:227], a[64:67], v[144:147]\n"
    "s_add_u32 m0, s48, 71808\n"
    "ds_read_b64_tr_b16 v[212:213], v253 offset:62496\n"
    "ds_read_b64_tr_b16 v[214:215], v253 offset:63008\n"
    "v_mfma_f32_16x16x32_bf16 v[144:147], v[228:231], a[80:83], v[144:147]\n"
    "buffer_load_dwordx4 v254, s[44:47], s52 offen offset:0 lds\n"
    "v_mfma_f32_16x16x32_bf16 v[148:151], v[228:231], a[84:87], v[148:151]\n"
    "v_exp_f32 v242, v138\n"
    "v_mfma_f32_16x16x32_bf16 v[148:151], v[224:227], a[68:71], v[148:151]\n"
    "v_exp_f32 v243, v139\n"
    "v_mfma_f32_16x16x32_bf16 v[152:155], v[224:227], a[72:75], v[152:155]\n"
    "s_add_u32 m0, s48, 71840\n"
    "ds_read_b64_tr_b16 v[216:217], v253 offset:62528\n"
    "ds_read_b64_tr_b16 v[218:219], v253 offset:63040\n"
    "v_mfma_f32_16x16x32_bf16 v[152:155], v[228:231], a[88:91], v[152:155]\n"
    "buffer_load_dwordx4 v254, s[44:47], s52 offen offset:1024 lds\n"
    "v_add_f32 v246, v246, v242\n"
    "v_mfma_f32_16x16x32_bf16 v[156:159], v[228:231], a[92:95], v[156:159]\n"
    "v_add_f32 v246, v246, v243\n"
    "v_cvt_pk_bf16_f32 v169, v242, v243\n"
    "v_mfma_f32_16x16x32_bf16 v[156:159], v[224:227], a[76:79], v[156:159]\n"
    "s_waitcnt lgkmcnt(6)\n"
    "v_exp_f32 v240, v132\n"
    "v_mfma_f32_16x16x32_bf16 v[156:159], v[232:235], a[108:111], v[156:159]\n"
    "s_add_u32 m0, s48, 71872\n"
    "ds_read_b64_tr_b16 v[220:221], v253 offset:62560\n"
    "ds_read_b64_tr_b16 v[222:223], v253 offset:63072\n"
    "v_mfma_f32_16x16x32_bf16 v[156:159], v[236:239], a[124:127], v[156:159]\n"
    "buffer_load_dwordx4 v254, s[44:47], s52 offen offset:2048 lds\n"
    "v_mfma_f32_16x16x32_bf16 v[152:155], v[236:239], a[120:123], v[152:155]\n"
    "v_exp_f32 v241, v133\n"
    "v_mfma_f32_16x16x32_bf16 v[152:155], v[232:235], a[104:107], v[152:155]\n"
    "v_add_f32 v245, v240, v241\n"
    "v_cvt_pk_bf16_f32 v164, v240, v241\n"
    "v_mfma_f32_16x16x32_bf16 v[148:151], v[232:235], a[100:103], v[148:151]\n"
    "s_add_u32 m0, s48, 71904\n"
    "ds_read_b64_tr_b16 v[224:225], v253 offset:62592\n"
    "ds_read_b64_tr_b16 v[226:227], v253 offset:63104\n"
    "v_mfma_f32_16x16x32_bf16 v[148:151], v[236:239], a[116:119], v[148:151]\n"
    "buffer_load_dwordx4 v254, s[44:47], s52 offen offset:3072 lds\n"
    "v_mfma_f32_16x16x32_bf16 v[144:147], v[236:239], a[112:115], v[144:147]\n"
    "v_exp_f32 v242, v134\n"
    "v_mfma_f32_16x16x32_bf16 v[144:147], v[232:235], a[96:99], v[144:147]\n"
    "s_waitcnt lgkmcnt(6)\n"
    "v_exp_f32 v243, v135\n"
    "v_mfma_f32_16x16x32_bf16 v[0:3], v[208:211], v[176:179], v[0:3]\n"
    "s_mov_b32 m0, 114688\n"
    "ds_read_b64_tr_b16 v[228:229], v253 offset:62624\n"
    "ds_read_b64_tr_b16 v[230:231], v253 offset:63136\n"
    "v_mfma_f32_16x16x32_bf16 v[8:11], v[212:215], v[176:179], v[8:11]\n"
    "buffer_load_dword v254, s[40:43], s53 offen lds\n"
    "v_add_f32 v245, v245, v242\n"
    "v_mfma_f32_16x16x32_bf16 v[12:15], v[212:215], v[180:183], v[12:15]\n"
    "s_add_u32 s49, s49, 32768\n"
    "v_add_f32 v245, v245, v243\n"
    "v_cvt_pk_bf16_f32 v165, v242, v243\n"
    "v_mfma_f32_16x16x32_bf16 v[4:7], v[208:211], v[180:183], v[4:7]\n"
    "s_add_u32 s50, s50, 32768\n"
    "buffer_load_dword v254, s[76:79], s80 offen sc1 lds\n"
    "v_mfma_f32_16x16x32_bf16 a[128:131], v[208:211], v[184:187], a[128:131]\n"
    "s_add_u32 s51, s51, 32768\n"
    "ds_read_b64_tr_b16 v[232:233], v253 offset:62656\n"
    "ds_read_b64_tr_b16 v[234:235], v253 offset:63168\n"
    "v_mfma_f32_16x16x32_bf16 a[136:139], v[212:215], v[184:187], a[136:139]\n"
    "s_add_u32 s52, s52, 32768\n"
    "v_exp_f32 v240, v128\n"
    "v_mfma_f32_16x16x32_bf16 a[140:143], v[212:215], v[188:191], a[140:143]\n"
    "s_add_u32 s89, s89, 7168\n"
    "v_exp_f32 v241, v129\n"
    "v_mfma_f32_16x16x32_bf16 a[132:135], v[208:211], v[188:191], a[132:135]\n"
    "s_waitcnt lgkmcnt(6)\n"
    "v_add_f32 v244, v240, v241\n"
    "v_cvt_pk_bf16_f32 v160, v240, v241\n"
    "v_mfma_f32_16x16x32_bf16 a[148:151], v[216:219], v[188:191], a[148:151]\n"
    "s_cmp_eq_u32 s55, 5\n"
    "ds_read_b64_tr_b16 v[236:237], v253 offset:62688\n"
    "ds_read_b64_tr_b16 v[238:239], v253 offset:63200\n"
    "v_mfma_f32_16x16x32_bf16 a[156:159], v[220:223], v[188:191], a[156:159]\n"
    "s_cselect_b64 s[84:85], s[14:15], s[84:85]\n"
    "v_exp_f32 v242, v130\n"
    "v_mfma_f32_16x16x32_bf16 a[152:155], v[220:223], v[184:187], a[152:155]\n"
    "s_cselect_b32 s89, s88, s89\n"
    "v_exp_f32 v243, v131\n"
    "v_mfma_f32_16x16x32_bf16 a[144:147], v[216:219], v[184:187], a[144:147]\n"
    "s_cselect_b32 s86, 1605632, s86\n"
    "v_add_f32 v244, v244, v242\n"
    "v_add_f32 v244, v244, v243\n"
    "v_mfma_f32_16x16x32_bf16 v[20:23], v[216:219], v[180:183], v[20:23]\n"
    "ds_read_b64_tr_b16 v[208:209], v253 offset:62720\n"
    "ds_read_b64_tr_b16 v[210:211], v253 offset:63232\n"
    "v_mfma_f32_16x16x32_bf16 v[28:31], v[220:223], v[180:183], v[28:31]\n"
    "v_cvt_pk_bf16_f32 v161, v242, v243\n"
    "v_mfma_f32_16x16x32_bf16 v[24:27], v[220:223], v[176:179], v[24:27]\n"
    "v_exp_f32 v240, v156\n"
    "v_mfma_f32_16x16x32_bf16 v[16:19], v[216:219], v[176:179], v[16:19]\n"
    "s_waitcnt lgkmcnt(6)\n"
    "v_exp_f32 v241, v157\n"
    "v_mfma_f32_16x16x32_bf16 v[32:35], v[224:227], v[176:179], v[32:35]\n"
    "s_cmp_eq_u32 s55, 3\n"
    "ds_read_b64_tr_b16 v[212:213], v253 offset:62752\n"
    "ds_read_b64_tr_b16 v[214:215], v253 offset:63264\n"
    "v_mfma_f32_16x16x32_bf16 v[40:43], v[228:231], v[176:179], v[40:43]\n"
    "s_cselect_b64 s[84:85], s[16:17], s[84:85]\n"
    "v_add_f32 v247, v247, v240\n"
    "v_add_f32 v247, v247, v241\n"
    "v_mfma_f32_16x16x32_bf16 v[44:47], v[228:231], v[180:183], v[44:47]\n"
    "s_cselect_b32 s89, s88, s89\n"
    "v_cvt_pk_bf16_f32 v174, v240, v241\n"
    "v_mfma_f32_16x16x32_bf16 v[36:39], v[224:227], v[180:183], v[36:39]\n"
    "s_cselect_b32 s86, 1605632, s86\n"
    "v_exp_f32 v242, v158\n"
    "v_mfma_f32_16x16x32_bf16 a[160:163], v[224:227], v[184:187], a[160:163]\n"
    "ds_read_b64_tr_b16 v[216:217], v253 offset:62784\n"
    "ds_read_b64_tr_b16 v[218:219], v253 offset:63296\n"
    "v_mfma_f32_16x16x32_bf16 a[168:171], v[228:231], v[184:187], a[168:171]\n"
    "v_exp_f32 v243, v159\n"
    "v_mfma_f32_16x16x32_bf16 a[172:175], v[228:231], v[188:191], a[172:175]\n"
    "v_add_f32 v247, v247, v242\n"
    "v_add_f32 v247, v247, v243\n"
    "v_mfma_f32_16x16x32_bf16 a[164:167], v[224:227], v[188:191], a[164:167]\n"
    "s_waitcnt lgkmcnt(6)\n"
    "v_cvt_pk_bf16_f32 v175, v242, v243\n"
    "v_mfma_f32_16x16x32_bf16 a[180:183], v[232:235], v[188:191], a[180:183]\n"
    "s_cmp_eq_u32 s55, 1\n"
    "ds_read_b64_tr_b16 v[220:221], v253 offset:62816\n"
    "ds_read_b64_tr_b16 v[222:223], v253 offset:63328\n"
    "v_mfma_f32_16x16x32_bf16 a[188:191], v[236:239], v[188:191], a[188:191]\n"
    "s_cselect_b64 s[84:85], s[16:17], s[84:85]\n"
    "ds_read_b64_tr_b16 v[224:225], v253 offset:62848\n"
    "ds_read_b64_tr_b16 v[226:227], v253 offset:63360\n"
    "v_mfma_f32_16x16x32_bf16 a[184:187], v[236:239], v[184:187], a[184:187]\n"
    "s_cselect_b32 s89, s88, s89\n"
    "v_exp_f32 v240, v152\n"
    "v_mfma_f32_16x16x32_bf16 a[176:179], v[232:235], v[184:187], a[176:179]\n"
    "s_cselect_b32 s86, 0, s86\n"
    "v_exp_f32 v241, v153\n"
    "v_mfma_f32_16x16x32_bf16 v[52:55], v[232:235], v[180:183], v[52:55]\n"
    "ds_read_b64_tr_b16 v[228:229], v253 offset:62880\n"
    "ds_read_b64_tr_b16 v[230:231], v253 offset:63392\n"
    "v_mfma_f32_16x16x32_bf16 v[60:63], v[236:239], v[180:183], v[60:63]\n"
    "v_add_f32 v246, v246, v240\n"
    "v_add_f32 v246, v246, v241\n"
    "v_mfma_f32_16x16x32_bf16 v[56:59], v[236:239], v[176:179], v[56:59]\n"
    "v_cvt_pk_bf16_f32 v170, v240, v241\n"
    "v_mfma_f32_16x16x32_bf16 v[48:51], v[232:235], v[176:179], v[48:51]\n"
    "s_waitcnt lgkmcnt(8)\n"
    "v_exp_f32 v242, v154\n"
    "v_mfma_f32_16x16x32_bf16 v[64:67], v[208:211], v[176:179], v[64:67]\n"
    "ds_read_b64_tr_b16 v[232:233], v253 offset:62912\n"
    "ds_read_b64_tr_b16 v[234:235], v253 offset:63424\n"
    "v_mfma_f32_16x16x32_bf16 v[72:75], v[212:215], v[176:179], v[72:75]\n"
    "ds_read_b64_tr_b16 v[236:237], v253 offset:62944\n"
    "ds_read_b64_tr_b16 v[238:239], v253 offset:63456\n"
    "v_mfma_f32_16x16x32_bf16 v[76:79], v[212:215], v[180:183], v[76:79]\n"
    "v_exp_f32 v243, v155\n"
    "v_mfma_f32_16x16x32_bf16 v[68:71], v[208:211], v[180:183], v[68:71]\n"
    "v_add_f32 v246, v246, v242\n"
    "v_add_f32 v246, v246, v243\n"
    "v_mfma_f32_16x16x32_bf16 a[192:195], v[208:211], v[184:187], a[192:195]\n"
    "v_cvt_pk_bf16_f32 v171, v242, v243\n"
    "v_mfma_f32_16x16x32_bf16 a[200:203], v[212:215], v[184:187], a[200:203]\n"
    "v_exp_f32 v240, v148\n"
    "v_mfma_f32_16x16x32_bf16 a[204:207], v[212:215], v[188:191], a[204:207]\n"
    "v_exp_f32 v241, v149\n"
    "v_mfma_f32_16x16x32_bf16 a[196:199], v[208:211], v[188:191], a[196:199]\n"
    "s_waitcnt lgkmcnt(8)\n"
    "v_add_f32 v245, v245, v240\n"
    "v_add_f32 v245, v245, v241\n"
    "v_mfma_f32_16x16x32_bf16 a[212:215], v[216:219], v[188:191], a[212:215]\n"
    "v_cvt_pk_bf16_f32 v166, v240, v241\n"
    "v_mfma_f32_16x16x32_bf16 a[220:223], v[220:223], v[188:191], a[220:223]\n"
    "v_exp_f32 v242, v150\n"
    "v_mfma_f32_16x16x32_bf16 a[216:219], v[220:223], v[184:187], a[216:219]\n"
    "v_exp_f32 v243, v151\n"
    "v_mfma_f32_16x16x32_bf16 a[208:211], v[216:219], v[184:187], a[208:211]\n"
    "v_add_f32 v245, v245, v242\n"
    "v_add_f32 v245, v245, v243\n"
    "v_mfma_f32_16x16x32_bf16 v[84:87], v[216:219], v[180:183], v[84:87]\n"
    "s_waitcnt vmcnt(2) lgkmcnt(0)\n"
    "s_barrier\n"
    "v_mfma_f32_16x16x32_bf16 v[92:95], v[220:223], v[180:183], v[92:95]\n"
    "ds_read_b128 v[208:211], v252 offset:33792\n"
    "v_cvt_pk_bf16_f32 v167, v242, v243\n"
    "v_mfma_f32_16x16x32_bf16 v[88:91], v[220:223], v[176:179], v[88:91]\n"
    "v_exp_f32 v240, v144\n"
    "v_mfma_f32_16x16x32_bf16 v[80:83], v[216:219], v[176:179], v[80:83]\n"
    "v_exp_f32 v241, v145\n"
    "v_mfma_f32_16x16x32_bf16 v[96:99], v[224:227], v[176:179], v[96:99]\n"
    "v_add_f32 v244, v244, v240\n"
    "v_add_f32 v244, v244, v241\n"
    "v_mfma_f32_16x16x32_bf16 v[104:107], v[228:231], v[176:179], v[104:107]\n"
    "ds_read_b128 v[212:215], v252 offset:33856\n"
    "v_cvt_pk_bf16_f32 v162, v240, v241\n"
    "v_mfma_f32_16x16x32_bf16 v[108:111], v[228:231], v[180:183], v[108:111]\n"
    "v_exp_f32 v242, v146\n"
    "v_mfma_f32_16x16x32_bf16 v[100:103], v[224:227], v[180:183], v[100:103]\n"
    "v_exp_f32 v243, v147\n"
    "v_mfma_f32_16x16x32_bf16 a[224:227], v[224:227], v[184:187], a[224:227]\n"
    "v_add_f32 v244, v244, v242\n"
    "v_add_f32 v244, v244, v243\n"
    "v_mfma_f32_16x16x32_bf16 a[232:235], v[228:231], v[184:187], a[232:235]\n"
    "ds_read_b128 v[216:219], v252 offset:33920\n"
    "v_cvt_pk_bf16_f32 v163, v242, v243\n"
    "v_mfma_f32_16x16x32_bf16 a[236:239], v[228:231], v[188:191], a[236:239]\n"
    "v_max3_f32 v240, v244, v245, v246\n"
    "v_max_f32 v240, v240, v247\n"
    "v_mfma_f32_16x16x32_bf16 a[228:231], v[224:227], v[188:191], a[228:231]\n"
    "v_cmp_lt_f32 vcc, s54, v240\n"
    "v_mfma_f32_16x16x32_bf16 a[244:247], v[232:235], v[188:191], a[244:247]\n"
    "v_mfma_f32_16x16x32_bf16 a[252:255], v[236:239], v[188:191], a[252:255]\n"
    "ds_read_b128 v[220:223], v252 offset:33984\n"
    "v_mfma_f32_16x16x32_bf16 a[248:251], v[236:239], v[184:187], a[248:251]\n"
    "v_mfma_f32_16x16x32_bf16 a[240:243], v[232:235], v[184:187], a[240:243]\n"
    "v_mfma_f32_16x16x32_bf16 v[116:119], v[232:235], v[180:183], v[116:119]\n"
    "v_mfma_f32_16x16x32_bf16 v[124:127], v[236:239], v[180:183], v[124:127]\n"
    "ds_read_b128 v[224:227], v252 offset:34048\n"
    "v_mfma_f32_16x16x32_bf16 v[120:123], v[236:239], v[176:179], v[120:123]\n"
    "v_mfma_f32_16x16x32_bf16 v[112:115], v[232:235], v[176:179], v[112:115]\n"
    "s_waitcnt lgkmcnt(3)\n"
    "s_cbranch_vccnz RARE0_%=\n"
    "RET0_%=:\n"
    "v_mfma_f32_16x16x32_bf16 v[128:131], v[208:211], a[0:3], v[192:195]\n"
    "s_add_u32 m0, s48, 0\n"
    "ds_read_b128 v[228:231], v252 offset:34112\n"
    "v_mfma_f32_16x16x32_bf16 v[128:131], v[212:215], a[16:19], v[128:131]\n"
    "buffer_load_dwordx4 v254, s[40:43], s49 offen offset:0 lds\n"
    "v_mfma_f32_16x16x32_bf16 v[132:135], v[212:215], a[20:23], v[196:199]\n"
    "v_add_f32 v248, v248, v244\n"
    "v_add_f32 v249, v249, v245\n"
    "v_mfma_f32_16x16x32_bf16 v[132:135], v[208:211], a[4:7], v[132:135]\n"
    "v_add_f32 v250, v250, v246\n"
    "v_add_f32 v251, v251, v247\n"
    "v_mfma_f32_16x16x32_bf16 v[136:139], v[208:211], a[8:11], v[200:203]\n"
    "s_add_u32 m0, s48, 32\n"
    "ds_read_b128 v[232:235], v252 offset:34176\n"
    "v_mfma_f32_16x16x32_bf16 v[136:139], v[212:215], a[24:27], v[136:139]\n"
    "buffer_load_dwordx4 v254, s[40:43], s49 offen offset:1024 lds\n"
    "v_mfma_f32_16x16x32_bf16 v[140:143], v[212:215], a[28:31], v[204:207]\n"
    "v_mfma_f32_16x16x32_bf16 v[140:143], v[208:211], a[12:15], v[140:143]\n"
    "s_waitcnt lgkmcnt(3)\n"
    "v_mfma_f32_16x16x32_bf16 v[140:143], v[216:219], a[44:47], v[140:143]\n"
    "s_add_u32 m0, s48, 64\n"
    "ds_read_b128 v[236:239], v252 offset:34240\n"
    "v_mfma_f32_16x16x32_bf16 v[140:143], v[220:223], a[60:63], v[140:143]\n"
    "buffer_load_dwordx4 v254, s[40:43], s49 offen offset:2048 lds\n"
    "v_mfma_f32_16x16x32_bf16 v[136:139], v[220:223], a[56:59], v[136:139]\n"
    "v_mfma_f32_16x16x32_bf16 v[136:139], v[216:219], a[40:43], v[136:139]\n"
    "v_mfma_f32_16x16x32_bf16 v[132:135], v[216:219], a[36:39], v[132:135]\n"
    "s_add_u32 m0, s48, 96\n"
    "ds_read_b128 v[208:211], v252 offset:34304\n"
    "v_mfma_f32_16x16x32_bf16 v[132:135], v[220:223], a[52:55], v[132:135]\n"
    "buffer_load_dwordx4 v254, s[40:43], s49 offen offset:3072 lds\n"
    "v_mfma_f32_16x16x32_bf16 v[128:131], v[220:223], a[48:51], v[128:131]\n"
    "v_mfma_f32_16x16x32_bf16 v[128:131], v[216:219], a[32:35], v[128:131]\n"
    "s_waitcnt lgkmcnt(3)\n"
    "v_mfma_f32_16x16x32_bf16 v[128:131], v[224:227], a[64:67], v[128:131]\n"
    "s_add_u32 m0, s48, 4224\n"
    "ds_read_b128 v[212:215], v252 offset:34368\n"
    "v_mfma_f32_16x16x32_bf16 v[128:131], v[228:231], a[80:83], v[128:131]\n"
    "buffer_load_dwordx4 v254, s[40:43], s50 offen offset:0 lds\n"
    "v_mfma_f32_16x16x32_bf16 v[132:135], v[228:231], a[84:87], v[132:135]\n"
    "v_mfma_f32_16x16x32_bf16 v[132:135], v[224:227], a[68:71], v[132:135]\n"
    "v_mfma_f32_16x16x32_bf16 v[136:139], v[224:227], a[72:75], v[136:139]\n"
    "s_add_u32 m0, s48, 4256\n"
    "ds_read_b128 v[216:219], v252 offset:34432\n"
    "v_mfma_f32_16x16x32_bf16 v[136:139], v[228:231], a[88:91], v[136:139]\n"
    "buffer_load_dwordx4 v254, s[40:43], s50 offen offset:1024 lds\n"
    "v_mfma_f32_16x16x32_bf16 v[140:143], v[228:231], a[92:95], v[140:143]\n"
    "v_mfma_f32_16x16x32_bf16 v[140:143], v[224:227], a[76:79], v[140:143]\n"
    "s_waitcnt lgkmcnt(3)\n"
    "v_mfma_f32_16x16x32_bf16 v[140:143], v[232:235], a[108:111], v[140:143]\n"
    "s_add_u32 m0, s48, 4288\n"
    "ds_read_b128 v[220:223], v252 offset:34496\n"
    "v_mfma_f32_16x16x32_bf16 v[140:143], v[236:239], a[124:127], v[140:143]\n"
    "buffer_load_dwordx4 v254, s[40:43], s50 offen offset:2048 lds\n"
    "v_mfma_f32_16x16x32_bf16 v[136:139], v[236:239], a[120:123], v[136:139]\n"
    "v_mfma_f32_16x16x32_bf16 v[136:139], v[232:235], a[104:107], v[136:139]\n"
    "v_mfma_f32_16x16x32_bf16 v[132:135], v[232:235], a[100:103], v[132:135]\n"
    "s_add_u32 m0, s48, 4320\n"
    "ds_read_b128 v[224:227], v252 offset:34560\n"
    "v_mfma_f32_16x16x32_bf16 v[132:135], v[236:239], a[116:119], v[132:135]\n"
    "buffer_load_dwordx4 v254, s[40:43], s50 offen offset:3072 lds\n"
    "v_mfma_f32_16x16x32_bf16 v[128:131], v[236:239], a[112:115], v[128:131]\n"
    "v_mfma_f32_16x16x32_bf16 v[128:131], v[232:235], a[96:99], v[128:131]\n"
    "s_waitcnt lgkmcnt(3)\n"
    "v_mfma_f32_16x16x32_bf16 v[144:147], v[208:211], a[0:3], v[192:195]\n"
    "s_add_u32 m0, s48, 130048\n"
    "ds_read_b128 v[228:231], v252 offset:34624\n"
    "v_mfma_f32_16x16x32_bf16 v[144:147], v[212:215], a[16:19], v[144:147]\n"
    "buffer_load_dwordx4 v254, s[44:47], s51 offen offset:0 lds\n"
    "v_mfma_f32_16x16x32_bf16 v[148:151], v[212:215], a[20:23], v[196:199]\n"
    "v_exp_f32 v240, v140\n"
    "v_mfma_f32_16x16x32_bf16 v[148:151], v[208:211], a[4:7], v[148:151]\n"
    "v_exp_f32 v241, v141\n"
    "v_mfma_f32_16x16x32_bf16 v[152:155], v[208:211], a[8:11], v[200:203]\n"
    "s_add_u32 m0, s48, 130080\n"
    "ds_read_b128 v[232:235], v252 offset:34688\n"
    "v_add_f32 v247, v240, v241\n"
    "v_mfma_f32_16x16x32_bf16 v[152:155], v[212:215], a[24:27], v[152:155]\n"
    "buffer_load_dwordx4 v254, s[44:47], s51 offen offset:1024 lds\n"
    "v_cvt_pk_bf16_f32 v188, v240, v241\n"
    "v_mfma_f32_16x16x32_bf16 v[156:159], v[212:215], a[28:31], v[204:207]\n"
    "v_exp_f32 v242, v142\n"
    "v_mfma_f32_16x16x32_bf16 v[156:159], v[208:211], a[12:15], v[156:159]\n"
    "s_waitcnt lgkmcnt(3)\n"
    "v_exp_f32 v243, v143\n"
    "v_mfma_f32_16x16x32_bf16 v[156:159], v[216:219], a[44:47], v[156:159]\n"
    "s_add_u32 m0, s48, 130112\n"
    "ds_read_b128 v[236:239], v252 offset:34752\n"
    "v_add_f32 v247, v247, v242\n"
    "v_mfma_f32_16x16x32_bf16 v[156:159], v[220:223], a[60:63], v[156:159]\n"
    "buffer_load_dwordx4 v254, s[44:47], s51 offen offset:2048 lds\n"
    "v_add_f32 v247, v247, v243\n"
    "v_mfma_f32_16x16x32_bf16 v[152:155], v[220:223], a[56:59], v[152:155]\n"
    "v_cvt_pk_bf16_f32 v189, v242, v243\n"
    "v_mfma_f32_16x16x32_bf16 v[152:155], v[216:219], a[40:43], v[152:155]\n"
    "v_exp_f32 v240, v136\n"
    "v_mfma_f32_16x16x32_bf16 v[148:151], v[216:219], a[36:39], v[148:151]\n"
    "s_add_u32 m0, s48, 130144\n"
    "ds_read_b64_tr_b16 v[208:209], v253 offset:0\n"
    "ds_read_b64_tr_b16 v[210:211], v253 offset:512\n"
    "v_mfma_f32_16x16x32_bf16 v[148:151], v[220:223], a[52:55], v[148:151]\n"
    "buffer_load_dwordx4 v254, s[44:47], s51 offen offset:3072 lds\n"
    "v_mfma_f32_16x16x32_bf16 v[144:147], v[220:223], a[48:51], v[144:147]\n"
    "v_exp_f32 v241, v137\n"
    "v_mfma_f32_16x16x32_bf16 v[144:147], v[216:219], a[32:35], v[144:147]\n"
    "s_waitcnt lgkmcnt(4)\n"
    "v_add_f32 v246, v240, v241\n"
    "v_cvt_pk_bf16_f32 v184, v240, v241\n"
    "v_mfma_f32_16x16x32_bf16 v[144:147], v[224:227], a[64:67], v[144:147]\n"
    "s_add_u32 m0, s48, 134272\n"
    "ds_read_b64_tr_b16 v[212:213], v253 offset:32\n"
    "ds_read_b64_tr_b16 v[214:215], v253 offset:544\n"
    "v_mfma_f32_16x16x32_bf16 v[144:147], v[228:231], a[80:83], v[144:147]\n"
    "buffer_load_dwordx4 v254, s[44:47], s52 offen offset:0 lds\n"
    "v_mfma_f32_16x16x32_bf16 v[148:151], v[228:231], a[84:87], v[148:151]\n"
    "v_exp_f32 v242, v138\n"
    "v_mfma_f32_16x16x32_bf16 v[148:151], v[224:227], a[68:71], v[148:151]\n"
    "v_exp_f32 v243, v139\n"
    "v_mfma_f32_16x16x32_bf16 v[152:155], v[224:227], a[72:75], v[152:155]\n"
    "s_add_u32 m0, s48, 134304\n"
    "ds_read_b64_tr_b16 v[216:217], v253 offset:64\n"
    "ds_read_b64_tr_b16 v[218:219], v253 offset:576\n"
    "v_mfma_f32_16x16x32_bf16 v[152:155], v[228:231], a[88:91], v[152:155]\n"
    "buffer_load_dwordx4 v254, s[44:47], s52 offen offset:1024 lds\n"
    "v_add_f32 v246, v246, v242\n"
    "v_mfma_f32_16x16x32_bf16 v[156:159], v[228:231], a[92:95], v[156:159]\n"
    "v_add_f32 v246, v246, v243\n"
    "v_cvt_pk_bf16_f32 v185, v242, v243\n"
    "v_mfma_f32_16x16x32_bf16 v[156:159], v[224:227], a[76:79], v[156:159]\n"
    "s_waitcnt lgkmcnt(6)\n"
    "v_exp_f32 v240, v132\n"
    "v_mfma_f32_16x16x32_bf16 v[156:159], v[232:235], a[108:111], v[156:159]\n"
    "s_add_u32 m0, s48, 134336\n"
    "ds_read_b64_tr_b16 v[220:221], v253 offset:96\n"
    "ds_read_b64_tr_b16 v[222:223], v253 offset:608\n"
    "v_mfma_f32_16x16x32_bf16 v[156:159], v[236:239], a[124:127], v[156:159]\n"
    "buffer_load_dwordx4 v254, s[44:47], s52 offen offset:2048 lds\n"
    "v_mfma_f32_16x16x32_bf16 v[152:155], v[236:239], a[120:123], v[152:155]\n"
    "v_exp_f32 v241, v133\n"
    "v_mfma_f32_16x16x32_bf16 v[152:155], v[232:235], a[104:107], v[152:155]\n"
    "v_add_f32 v245, v240, v241\n"
    "v_cvt_pk_bf16_f32 v180, v240, v241\n"
    "v_mfma_f32_16x16x32_bf16 v[148:151], v[232:235], a[100:103], v[148:151]\n"
    "s_add_u32 m0, s48, 134368\n"
    "ds_read_b64_tr_b16 v[224:225], v253 offset:128\n"
    "ds_read_b64_tr_b16 v[226:227], v253 offset:640\n"
    "v_mfma_f32_16x16x32_bf16 v[148:151], v[236:239], a[116:119], v[148:151]\n"
    "buffer_load_dwordx4 v254, s[44:47], s52 offen offset:3072 lds\n"
    "v_mfma_f32_16x16x32_bf16 v[144:147], v[236:239], a[112:115], v[144:147]\n"
    "v_exp_f32 v242, v134\n"
    "v_mfma_f32_16x16x32_bf16 v[144:147], v[232:235], a[96:99], v[144:147]\n"
    "s_waitcnt lgkmcnt(6)\n"
    "v_exp_f32 v243, v135\n"
    "v_mfma_f32_16x16x32_bf16 v[0:3], v[208:211], v[160:163], v[0:3]\n"
    "s_mov_b32 m0, 114688\n"
    "ds_read_b64_tr_b16 v[228:229], v253 offset:160\n"
    "ds_read_b64_tr_b16 v[230:231], v253 offset:672\n"
    "v_mfma_f32_16x16x32_bf16 v[8:11], v[212:215], v[160:163], v[8:11]\n"
    "buffer_load_dword v254, s[44:47], s53 offen lds\n"
    "v_add_f32 v245, v245, v242\n"
    "v_mfma_f32_16x16x32_bf16 v[12:15], v[212:215], v[164:167], v[12:15]\n"
    "s_add_u32 s49, s49, 32768\n"
    "v_add_f32 v245, v245, v243\n"
    "v_cvt_pk_bf16_f32 v181, v242, v243\n"
    "v_mfma_f32_16x16x32_bf16 v[4:7], v[208:211], v[164:167], v[4:7]\n"
    "s_add_u32 s50, s50, 32768\n"
    "buffer_load_dword v255, s[84:87], s89 offen lds\n"
    "v_mfma_f32_16x16x32_bf16 a[128:131], v[208:211], v[168:171], a[128:131]\n"
    "s_add_u32 s51, s51, 32768\n"
    "ds_read_b64_tr_b16 v[232:233], v253 offset:192\n"
    "ds_read_b64_tr_b16 v[234:235], v253 offset:704\n"
    "v_mfma_f32_16x16x32_bf16 a[136:139], v[212:215], v[168:171], a[136:139]\n"
    "s_add_u32 s52, s52, 32768\n"
    "v_exp_f32 v240, v128\n"
    "v_mfma_f32_16x16x32_bf16 a[140:143], v[212:215], v[172:175], a[140:143]\n"
    "s_add_u32 s53, s53, 65536\n"
    "v_exp_f32 v241, v129\n"
    "v_mfma_f32_16x16x32_bf16 a[132:135], v[208:211], v[172:175], a[132:135]\n"
    "s_waitcnt lgkmcnt(6)\n"
    "v_add_f32 v244, v240, v241\n"
    "v_cvt_pk_bf16_f32 v176, v240, v241\n"
    "v_mfma_f32_16x16x32_bf16 a[148:151], v[216:219], v[172:175], a[148:151]\n"
    "s_sub_u32 s55, s55, 1\n"
    "ds_read_b64_tr_b16 v[236:237], v253 offset:224\n"
    "ds_read_b64_tr_b16 v[238:239], v253 offset:736\n"
    "v_mfma_f32_16x16x32_bf16 a[156:159], v[220:223], v[172:175], a[156:159]\n"
    "s_add_u32 s80, s80, s81\n"
    "v_exp_f32 v242, v130\n"
    "v_mfma_f32_16x16x32_bf16 a[152:155], v[220:223], v[168:171], a[152:155]\n"
    "s_cmp_eq_u32 s55, 7\n"
    "v_exp_f32 v243, v131\n"
    "v_mfma_f32_16x16x32_bf16 a[144:147], v[216:219], v[168:171], a[144:147]\n"
    "s_cselect_b64 s[76:77], s[18:19], s[76:77]\n"
    "v_add_f32 v244, v244, v242\n"
    "v_add_f32 v244, v244, v243\n"
    "v_mfma_f32_16x16x32_bf16 v[20:23], v[216:219], v[164:167], v[20:23]\n"
    "s_cselect_b32 s80, s69, s80\n"
    "ds_read_b64_tr_b16 v[208:209], v253 offset:256\n"
    "ds_read_b64_tr_b16 v[210:211], v253 offset:768\n"
    "v_mfma_f32_16x16x32_bf16 v[28:31], v[220:223], v[164:167], v[28:31]\n"
    "s_cselect_b32 s78, 1605632, s78\n"
    "v_cvt_pk_bf16_f32 v177, v242, v243\n"
    "v_mfma_f32_16x16x32_bf16 v[24:27], v[220:223], v[160:163], v[24:27]\n"
    "v_exp_f32 v240, v156\n"
    "v_mfma_f32_16x16x32_bf16 v[16:19], v[216:219], v[160:163], v[16:19]\n"
    "s_waitcnt lgkmcnt(6)\n"
    "v_exp_f32 v241, v157\n"
    "v_mfma_f32_16x16x32_bf16 v[32:35], v[224:227], v[160:163], v[32:35]\n"
    "s_cmp_eq_u32 s55, 5\n"
    "ds_read_b64_tr_b16 v[212:213], v253 offset:288\n"
    "ds_read_b64_tr_b16 v[214:215], v253 offset:800\n"
    "v_mfma_f32_16x16x32_bf16 v[40:43], v[228:231], v[160:163], v[40:43]\n"
    "s_cselect_b64 s[76:77], s[12:13], s[76:77]\n"
    "v_add_f32 v247, v247, v240\n"
    "v_add_f32 v247, v247, v241\n"
    "v_mfma_f32_16x16x32_bf16 v[44:47], v[228:231], v[164:167], v[44:47]\n"
    "s_cselect_b32 s80, s68, s80\n"
    "v_cvt_pk_bf16_f32 v190, v240, v241\n"
    "v_mfma_f32_16x16x32_bf16 v[36:39], v[224:227], v[164:167], v[36:39]\n"
    "s_cselect_b32 s78, 229376, s78\n"
    "v_exp_f32 v242, v158\n"
    "v_mfma_f32_16x16x32_bf16 a[160:163], v[224:227], v[168:171], a[160:163]\n"
    "s_cselect_b32 s81, 0x10000, s81\n"
    "ds_read_b64_tr_b16 v[216:217], v253 offset:320\n"
    "ds_read_b64_tr_b16 v[218:219], v253 offset:832\n"
    "v_mfma_f32_16x16x32_bf16 a[168:171], v[228:231], v[168:171], a[168:171]\n"
    "v_exp_f32 v243, v159\n"
    "v_mfma_f32_16x16x32_bf16 a[172:175], v[228:231], v[172:175], a[172:175]\n"
    "v_add_f32 v247, v247, v242\n"
    "v_add_f32 v247, v247, v243\n"
    "v_mfma_f32_16x16x32_bf16 a[164:167], v[224:227], v[172:175], a[164:167]\n"
    "s_waitcnt lgkmcnt(6)\n"
    "v_cvt_pk_bf16_f32 v191, v242, v243\n"
    "v_mfma_f32_16x16x32_bf16 a[180:183], v[232:235], v[172:175], a[180:183]\n"
    "ds_read_b64_tr_b16 v[220:221], v253 offset:352\n"
    "ds_read_b64_tr_b16 v[222:223], v253 offset:864\n"
    "v_mfma_f32_16x16x32_bf16 a[188:191], v[236:239], v[172:175], a[188:191]\n"
    "ds_read_b64_tr_b16 v[224:225], v253 offset:384\n"
    "ds_read_b64_tr_b16 v[226:227], v253 offset:896\n"
    "v_mfma_f32_16x16x32_bf16 a[184:187], v[236:239], v[168:171], a[184:187]\n"
    "v_exp_f32 v240, v152\n"
    "v_mfma_f32_16x16x32_bf16 a[176:179], v[232:235], v[168:171], a[176:179]\n"
    "v_exp_f32 v241, v153\n"
    "v_mfma_f32_16x16x32_bf16 v[52:55], v[232:235], v[164:167], v[52:55]\n"
    "ds_read_b64_tr_b16 v[228:229], v253 offset:416\n"
    "ds_read_b64_tr_b16 v[230:231], v253 offset:928\n"
    "v_mfma_f32_16x16x32_bf16 v[60:63], v[236:239], v[164:167], v[60:63]\n"
    "v_add_f32 v246, v246, v240\n"
    "v_add_f32 v246, v246, v241\n"
    "v_mfma_f32_16x16x32_bf16 v[56:59], v[236:239], v[160:163], v[56:59]\n"
    "v_cvt_pk_bf16_f32 v186, v240, v241\n"
    "v_mfma_f32_16x16x32_bf16 v[48:51], v[232:235], v[160:163], v[48:51]\n"
    "s_waitcnt lgkmcnt(8)\n"
    "v_exp_f32 v242, v154\n"
    "v_mfma_f32_16x16x32_bf16 v[64:67], v[208:211], v[160:163], v[64:67]\n"
    "ds_read_b64_tr_b16 v[232:233], v253 offset:448\n"
    "ds_read_b64_tr_b16 v[234:235], v253 offset:960\n"
    "v_mfma_f32_16x16x32_bf16 v[72:75], v[212:215], v[160:163], v[72:75]\n"
    "ds_read_b64_tr_b16 v[236:237], v253 offset:480\n"
    "ds_read_b64_tr_b16 v[238:239], v253 offset:992\n"
    "v_mfma_f32_16x16x32_bf16 v[76:79], v[212:215], v[164:167], v[76:79]\n"
    "v_exp_f32 v243, v155\n"
    "v_mfma_f32_16x16x32_bf16 v[68:71], v[208:211], v[164:167], v[68:71]\n"
    "v_add_f32 v246, v246, v242\n"
    "v_add_f32 v246, v246, v243\n"
    "v_mfma_f32_16x16x32_bf16 a[192:195], v[208:211], v[168:171], a[192:195]\n"
    "v_cvt_pk_bf16_f32 v187, v242, v243\n"
    "v_mfma_f32_16x16x32_bf16 a[200:203], v[212:215], v[168:171], a[200:203]\n"
    "v_exp_f32 v240, v148\n"
    "v_mfma_f32_16x16x32_bf16 a[204:207], v[212:215], v[172:175], a[204:207]\n"
    "v_exp_f32 v241, v149\n"
    "v_mfma_f32_16x16x32_bf16 a[196:199], v[208:211], v[172:175], a[196:199]\n"
    "s_waitcnt lgkmcnt(8)\n"
    "v_add_f32 v245, v245, v240\n"
    "v_add_f32 v245, v245, v241\n"
    "v_mfma_f32_16x16x32_bf16 a[212:215], v[216:219], v[172:175], a[212:215]\n"
    "v_cvt_pk_bf16_f32 v182, v240, v241\n"
    "v_mfma_f32_16x16x32_bf16 a[220:223], v[220:223], v[172:175], a[220:223]\n"
    "v_exp_f32 v242, v150\n"
    "v_mfma_f32_16x16x32_bf16 a[216:219], v[220:223], v[168:171], a[216:219]\n"
    "v_exp_f32 v243, v151\n"
    "v_mfma_f32_16x16x32_bf16 a[208:211], v[216:219], v[168:171], a[208:211]\n"
    "v_add_f32 v245, v245, v242\n"
    "v_add_f32 v245, v245, v243\n"
    "v_mfma_f32_16x16x32_bf16 v[84:87], v[216:219], v[164:167], v[84:87]\n"
    "s_waitcnt vmcnt(2) lgkmcnt(0)\n"
    "s_barrier\n"
    "v_mfma_f32_16x16x32_bf16 v[92:95], v[220:223], v[164:167], v[92:95]\n"
    "ds_read_b128 v[208:211], v252 offset:0\n"
    "v_cvt_pk_bf16_f32 v183, v242, v243\n"
    "v_mfma_f32_16x16x32_bf16 v[88:91], v[220:223], v[160:163], v[88:91]\n"
    "v_exp_f32 v240, v144\n"
    "v_mfma_f32_16x16x32_bf16 v[80:83], v[216:219], v[160:163], v[80:83]\n"
    "v_exp_f32 v241, v145\n"
    "v_mfma_f32_16x16x32_bf16 v[96:99], v[224:227], v[160:163], v[96:99]\n"
    "v_add_f32 v244, v244, v240\n"
    "v_add_f32 v244, v244, v241\n"
    "v_mfma_f32_16x16x32_bf16 v[104:107], v[228:231], v[160:163], v[104:107]\n"
    "ds_read_b128 v[212:215], v252 offset:64\n"
    "v_cvt_pk_bf16_f32 v178, v240, v241\n"
    "v_mfma_f32_16x16x32_bf16 v[108:111], v[228:231], v[164:167], v[108:111]\n"
    "v_exp_f32 v242, v146\n"
    "v_mfma_f32_16x16x32_bf16 v[100:103], v[224:227], v[164:167], v[100:103]\n"
    "v_exp_f32 v243, v147\n"
    "v_mfma_f32_16x16x32_bf16 a[224:227], v[224:227], v[168:171], a[224:227]\n"
    "v_add_f32 v244, v244, v242\n"
    "v_add_f32 v244, v244, v243\n"
    "v_mfma_f32_16x16x32_bf16 a[232:235], v[228:231], v[168:171], a[232:235]\n"
    "ds_read_b128 v[216:219], v252 offset:128\n"
    "v_cvt_pk_bf16_f32 v179, v242, v243\n"
    "v_mfma_f32_16x16x32_bf16 a[236:239], v[228:231], v[172:175], a[236:239]\n"
    "v_max3_f32 v240, v244, v245, v246\n"
    "v_max_f32 v240, v240, v247\n"
    "v_mfma_f32_16x16x32_bf16 a[228:231], v[224:227], v[172:175], a[228:231]\n"
    "v_cmp_lt_f32 vcc, s54, v240\n"
    "v_mfma_f32_16x16x32_bf16 a[244:247], v[232:235], v[172:175], a[244:247]\n"
    "v_mfma_f32_16x16x32_bf16 a[252:255], v[236:239], v[172:175], a[252:255]\n"
    "ds_read_b128 v[220:223], v252 offset:192\n"
    "v_mfma_f32_16x16x32_bf16 a[248:251], v[236:239], v[168:171], a[248:251]\n"
    "v_mfma_f32_16x16x32_bf16 a[240:243], v[232:235], v[168:171], a[240:243]\n"
    "v_mfma_f32_16x16x32_bf16 v[116:119], v[232:235], v[164:167], v[116:119]\n"
    "s_cmp_eq_u64 vcc, 0\n"
    "v_mfma_f32_16x16x32_bf16 v[124:127], v[236:239], v[164:167], v[124:127]\n"
    "s_cselect_b32 s70, s55, 0\n"
    "ds_read_b128 v[224:227], v252 offset:256\n"
    "v_mfma_f32_16x16x32_bf16 v[120:123], v[236:239], v[160:163], v[120:123]\n"
    "s_cmp_lg_u32 s70, 0\n"
    "v_mfma_f32_16x16x32_bf16 v[112:115], v[232:235], v[160:163], v[112:115]\n"
    "s_waitcnt lgkmcnt(3)\n"
    "s_cbranch_scc1 LOOP_%=\n"
    "s_cbranch_vccnz RARE1_%=\n"
    "RET1_%=:\n"
    "ds_read_b64_tr_b16 v[128:129], v253 offset:62464\n"
    "ds_read_b64_tr_b16 v[130:131], v253 offset:62976\n"
    "ds_read_b64_tr_b16 v[132:133], v253 offset:62496\n"
    "ds_read_b64_tr_b16 v[134:135], v253 offset:63008\n"
    "ds_read_b64_tr_b16 v[136:137], v253 offset:62528\n"
    "ds_read_b64_tr_b16 v[138:139], v253 offset:63040\n"
    "ds_read_b64_tr_b16 v[140:141], v253 offset:62560\n"
    "ds_read_b64_tr_b16 v[142:143], v253 offset:63072\n"
    "ds_read_b64_tr_b16 v[144:145], v253 offset:62592\n"
    "ds_read_b64_tr_b16 v[146:147], v253 offset:63104\n"
    "v_add_f32 v248, v248, v244\n"
    "v_add_f32 v249, v249, v245\n"
    "v_add_f32 v250, v250, v246\n"
    "v_add_f32 v251, v251, v247\n"
    "s_lshl_b32 s70, s72, 10\n"
    "s_xor_b32 s71, s70, 2048\n"
    "v_add_u32 v160, s70, v254\n"
    "v_add_u32 v162, s71, v254\n"
    "v_add_u32 v161, 0x10000, v160\n"
    "v_add_u32 v163, 0x10000, v162\n"
    "v_mov_b32 v240, v248\n"
    "v_mov_b32 v241, v249\n"
    "v_mov_b32 v242, v250\n"
    "v_mov_b32 v243, v251\n"
    "v_permlane16_swap_b32 v248, v240\n"
    "v_permlane16_swap_b32 v249, v241\n"
    "v_permlane16_swap_b32 v250, v242\n"
    "v_permlane16_swap_b32 v251, v243\n"
    "v_add_f32 v248, v248, v240\n"
    "v_add_f32 v249, v249, v241\n"
    "v_add_f32 v250, v250, v242\n"
    "v_add_f32 v251, v251, v243\n"
    "s_waitcnt lgkmcnt(6)\n"
    "v_mfma_f32_16x16x32_bf16 a[128:131], v[128:131], v[184:187], a[128:131]\n"
    "s_mov_b64 s[56:57], s[12:13]\n"
    "ds_read_b64_tr_b16 v[148:149], v253 offset:62624\n"
    "ds_read_b64_tr_b16 v[150:151], v253 offset:63136\n"
    "v_mfma_f32_16x16x32_bf16 a[136:139], v[132:135], v[184:187], a[136:139]\n"
    "s_mov_b32 s58, 229376\n"
    "ds_read_b64_tr_b16 v[152:153], v253 offset:62656\n"
    "ds_read_b64_tr_b16 v[154:155], v253 offset:63168\n"
    "v_mfma_f32_16x16x32_bf16 a[140:143], v[132:135], v[188:191], a[140:143]\n"
    "s_mov_b32 s59, 0x20000\n"
    "ds_read_b64_tr_b16 v[156:157], v253 offset:62688\n"
    "ds_read_b64_tr_b16 v[158:159], v253 offset:63200\n"
    "v_mfma_f32_16x16x32_bf16 a[132:135], v[128:131], v[188:191], a[132:135]\n"
    "s_waitcnt lgkmcnt(4)\n"
    "ds_read_b64_tr_b16 v[208:209], v253 offset:62720\n"
    "ds_read_b64_tr_b16 v[210:211], v253 offset:63232\n"
    "v_mfma_f32_16x16x32_bf16 a[148:151], v[136:139], v[188:191], a[148:151]\n"
    "s_lshl_b32 s48, s72, 13\n"
    "ds_read_b64_tr_b16 v[212:213], v253 offset:62752\n"
    "ds_read_b64_tr_b16 v[214:215], v253 offset:63264\n"
    "v_mfma_f32_16x16x32_bf16 a[156:159], v[140:143], v[188:191], a[156:159]\n"
    "s_mul_i32 s68, s72, 57344\n"
    "ds_read_b64_tr_b16 v[216:217], v253 offset:62784\n"
    "ds_read_b64_tr_b16 v[218:219], v253 offset:63296\n"
    "v_mfma_f32_16x16x32_bf16 a[152:155], v[140:143], v[184:187], a[152:155]\n"
    "s_mov_b64 s[64:65], s[28:29]\n"
    "ds_read_b64_tr_b16 v[220:221], v253 offset:62816\n"
    "ds_read_b64_tr_b16 v[222:223], v253 offset:63328\n"
    "v_mfma_f32_16x16x32_bf16 a[144:147], v[136:139], v[184:187], a[144:147]\n"
    "s_waitcnt lgkmcnt(4)\n"
    "ds_read_b64_tr_b16 v[224:225], v253 offset:62848\n"
    "ds_read_b64_tr_b16 v[226:227], v253 offset:63360\n"
    "v_mfma_f32_16x16x32_bf16 a[160:163], v[144:147], v[184:187], a[160:163]\n"
    "s_mov_b32 s66, 351232\n"
    "ds_read_b64_tr_b16 v[228:229], v253 offset:62880\n"
    "ds_read_b64_tr_b16 v[230:231], v253 offset:63392\n"
    "v_mfma_f32_16x16x32_bf16 a[168:171], v[148:151], v[184:187], a[168:171]\n"
    "s_mov_b32 s67, 0x20000\n"
    "ds_read_b64_tr_b16 v[232:233], v253 offset:62912\n"
    "ds_read_b64_tr_b16 v[234:235], v253 offset:63424\n"
    "v_mfma_f32_16x16x32_bf16 a[172:175], v[148:151], v[188:191], a[172:175]\n"
    "ds_read_b64_tr_b16 v[236:237], v253 offset:62944\n"
    "ds_read_b64_tr_b16 v[238:239], v253 offset:63456\n"
    "v_mfma_f32_16x16x32_bf16 a[164:167], v[144:147], v[188:191], a[164:167]\n"
    "ds_write_b128 v160, a[128:131] offset:0\n"
    "v_mov_b32 v240, v248\n"
    "v_mfma_f32_16x16x32_bf16 a[180:183], v[152:155], v[188:191], a[180:183]\n"
    "v_mov_b32 v241, v249\n"
    "v_mov_b32 v242, v250\n"
    "v_mfma_f32_16x16x32_bf16 a[188:191], v[156:159], v[188:191], a[188:191]\n"
    "v_mov_b32 v243, v251\n"
    "v_permlane32_swap_b32 v248, v240\n"
    "v_mfma_f32_16x16x32_bf16 a[184:187], v[156:159], v[184:187], a[184:187]\n"
    "ds_write_b128 v160, a[132:135] offset:4096\n"
    "v_permlane32_swap_b32 v249, v241\n"
    "v_mfma_f32_16x16x32_bf16 a[176:179], v[152:155], v[184:187], a[176:179]\n"
    "v_permlane32_swap_b32 v250, v242\n"
    "v_permlane32_swap_b32 v251, v243\n"
    "v_mfma_f32_16x16x32_bf16 a[192:195], v[208:211], v[184:187], a[192:195]\n"
    "v_add_f32 v248, v248, v240\n"
    "v_add_f32 v249, v249, v241\n"
    "v_mfma_f32_16x16x32_bf16 a[200:203], v[212:215], v[184:187], a[200:203]\n"
    "ds_write_b128 v160, a[136:139] offset:8192\n"
    "v_add_f32 v250, v250, v242\n"
    "v_mfma_f32_16x16x32_bf16 a[204:207], v[212:215], v[188:191], a[204:207]\n"
    "v_add_f32 v251, v251, v243\n"
    "v_mov_b32 v240, v200\n"
    "v_mfma_f32_16x16x32_bf16 a[196:199], v[208:211], v[188:191], a[196:199]\n"
    "s_waitcnt lgkmcnt(11)\n"
    "v_mov_b32 v241, v204\n"
    "v_mov_b32 v242, v250\n"
    "v_mfma_f32_16x16x32_bf16 a[212:215], v[216:219], v[188:191], a[212:215]\n"
    "ds_write_b128 v160, a[140:143] offset:12288\n"
    "v_mov_b32 v243, v251\n"
    "v_mfma_f32_16x16x32_bf16 a[220:223], v[220:223], v[188:191], a[220:223]\n"
    "v_mfma_f32_16x16x32_bf16 a[216:219], v[220:223], v[184:187], a[216:219]\n"
    "ds_write_b128 v161, v[240:243] offset:40960\n"
    "v_mfma_f32_16x16x32_bf16 a[208:211], v[216:219], v[184:187], a[208:211]\n"
    "s_waitcnt lgkmcnt(9)\n"
    "v_lshrrev_b32 v164, 4, v254\n"
    "v_lshrrev_b32 v165, 3, v164\n"
    "v_mfma_f32_16x16x32_bf16 a[224:227], v[224:227], v[184:187], a[224:227]\n"
    "v_mul_u32_u24 v166, 896, v165\n"
    "v_bfe_u32 v167, v164, 1, 2\n"
    "v_mfma_f32_16x16x32_bf16 a[232:235], v[228:231], v[184:187], a[232:235]\n"
    "ds_write_b128 v160, a[144:147] offset:16384\n"
    "v_lshrrev_b32 v165, 1, v165\n"
    "v_mfma_f32_16x16x32_bf16 a[236:239], v[228:231], v[188:191], a[236:239]\n"
    "v_xor_b32 v167, v167, v165\n"
    "v_lshl_add_u32 v166, v167, 5, v166\n"
    "v_mfma_f32_16x16x32_bf16 a[228:231], v[224:227], v[188:191], a[228:231]\n"
    "s_waitcnt lgkmcnt(6)\n"
    "v_and_b32 v167, 1, v164\n"
    "v_lshl_add_u32 v255, v167, 4, v166\n"
    "v_mfma_f32_16x16x32_bf16 a[244:247], v[232:235], v[188:191], a[244:247]\n"
    "s_waitcnt vmcnt(0)\n"
    "s_waitcnt lgkmcnt(1)\n"
    "s_barrier\n"
    "v_mfma_f32_16x16x32_bf16 a[252:255], v[236:239], v[188:191], a[252:255]\n"
    "s_mov_b64 s[76:77], s[26:27]\n"
    "ds_read_b128 v[244:247], v163 offset:40960\n"
    "ds_write_b128 v160, a[148:151] offset:20480\n"
    "v_mfma_f32_16x16x32_bf16 a[248:251], v[236:239], v[184:187], a[248:251]\n"
    "s_mov_b32 s78, 41103360\n"
    "v_mfma_f32_16x16x32_bf16 a[240:243], v[232:235], v[184:187], a[240:243]\n"
    "s_mov_b32 s79, 0x20000\n"
    "v_mfma_f32_16x16x32_bf16 v[0:3], v[128:131], v[176:179], v[0:3]\n"
    "s_mov_b64 s[88:89], s[4:5]\n"
    "ds_write_b128 v160, a[152:155] offset:24576\n"
    "v_mfma_f32_16x16x32_bf16 v[8:11], v[132:135], v[176:179], v[8:11]\n"
    "s_mov_b32 s90, 1835008\n"
    "v_mfma_f32_16x16x32_bf16 v[12:15], v[132:135], v[180:183], v[12:15]\n"
    "s_mov_b32 s91, 0x20000\n"
    "v_mfma_f32_16x16x32_bf16 v[4:7], v[128:131], v[180:183], v[4:7]\n"
    "s_lshl_b32 s53, s72, 10\n"
    "ds_write_b128 v160, a[156:159] offset:28672\n"
    "v_mfma_f32_16x16x32_bf16 v[20:23], v[136:139], v[180:183], v[20:23]\n"
    "s_add_u32 s49, s53, 4096\n"
    "v_mfma_f32_16x16x32_bf16 v[28:31], v[140:143], v[180:183], v[28:31]\n"
    "s_min_u32 s52, s53, 2048\n"
    "v_mfma_f32_16x16x32_bf16 v[24:27], v[140:143], v[176:179], v[24:27]\n"
    "s_waitcnt lgkmcnt(3)\n"
    "ds_write_b128 v160, a[160:163] offset:32768\n"
    "v_mfma_f32_16x16x32_bf16 v[16:19], v[136:139], v[176:179], v[16:19]\n"
    "s_add_u32 s51, s52, 4096\n"
    "v_min_f32 v240, v192, v244\n"
    "v_min_f32 v241, v196, v245\n"
    "v_mfma_f32_16x16x32_bf16 v[32:35], v[144:147], v[176:179], v[32:35]\n"
    "s_add_u32 s52, s51, 4096\n"
    "v_sub_f32 v168, v240, v192\n"
    "v_sub_f32 v169, v241, v196\n"
    "v_mfma_f32_16x16x32_bf16 v[40:43], v[148:151], v[176:179], v[40:43]\n"
    "s_mul_i32 s50, %1, 7168\n"
    "ds_write_b128 v160, a[164:167] offset:36864\n"
    "v_sub_f32 v170, v240, v244\n"
    "v_mfma_f32_16x16x32_bf16 v[44:47], v[148:151], v[180:183], v[44:47]\n"
    "s_add_u32 s73, s50, s51\n"
    "v_sub_f32 v171, v241, v245\n"
    "v_mfma_f32_16x16x32_bf16 v[36:39], v[144:147], v[180:183], v[36:39]\n"
    "s_add_u32 s50, s50, s53\n"
    "v_exp_f32 v168, v168\n"
    "v_mfma_f32_16x16x32_bf16 v[52:55], v[152:155], v[180:183], v[52:55]\n"
    "ds_write_b128 v160, a[168:171] offset:40960\n"
    "v_mfma_f32_16x16x32_bf16 v[60:63], v[156:159], v[180:183], v[60:63]\n"
    "v_exp_f32 v169, v169\n"
    "v_mfma_f32_16x16x32_bf16 v[56:59], v[156:159], v[176:179], v[56:59]\n"
    "v_exp_f32 v170, v170\n"
    "v_mfma_f32_16x16x32_bf16 v[48:51], v[152:155], v[176:179], v[48:51]\n"
    "ds_write_b128 v160, a[172:175] offset:45056\n"
    "v_mfma_f32_16x16x32_bf16 v[64:67], v[208:211], v[176:179], v[64:67]\n"
    "v_exp_f32 v171, v171\n"
    "v_mfma_f32_16x16x32_bf16 v[72:75], v[212:215], v[176:179], v[72:75]\n"
    "v_mul_f32 v240, v168, v248\n"
    "v_mul_f32 v241, v169, v249\n"
    "v_mfma_f32_16x16x32_bf16 v[76:79], v[212:215], v[180:183], v[76:79]\n"
    "ds_write_b128 v160, a[176:179] offset:49152\n"
    "v_fmac_f32 v240, v170, v246\n"
    "v_mfma_f32_16x16x32_bf16 v[68:71], v[208:211], v[180:183], v[68:71]\n"
    "v_fmac_f32 v241, v171, v247\n"
    "v_mfma_f32_16x16x32_bf16 v[84:87], v[216:219], v[180:183], v[84:87]\n"
    "v_rcp_f32 v240, v240\n"
    "v_mfma_f32_16x16x32_bf16 v[92:95], v[220:223], v[180:183], v[92:95]\n"
    "ds_write_b128 v160, a[180:183] offset:53248\n"
    "v_mfma_f32_16x16x32_bf16 v[88:91], v[220:223], v[176:179], v[88:91]\n"
    "v_rcp_f32 v241, v241\n"
    "v_mfma_f32_16x16x32_bf16 v[80:83], v[216:219], v[176:179], v[80:83]\n"
    "v_mul_f32 v168, v168, v240\n"
    "v_mul_f32 v169, v169, v241\n"
    "v_mfma_f32_16x16x32_bf16 v[96:99], v[224:227], v[176:179], v[96:99]\n"
    "ds_write_b128 v160, a[184:187] offset:57344\n"
    "v_mul_f32 v170, v170, v240\n"
    "v_mfma_f32_16x16x32_bf16 v[104:107], v[228:231], v[176:179], v[104:107]\n"
    "v_mul_f32 v171, v171, v241\n"
    "v_mfma_f32_16x16x32_bf16 v[108:111], v[228:231], v[180:183], v[108:111]\n"
    "v_mov_b32 v128, v160\n"
    "v_mov_b32 v130, v161\n"
    "v_mfma_f32_16x16x32_bf16 v[100:103], v[224:227], v[180:183], v[100:103]\n"
    "ds_write_b128 v160, a[188:191] offset:61440\n"
    "v_mov_b32 v129, v162\n"
    "v_mfma_f32_16x16x32_bf16 v[116:119], v[232:235], v[180:183], v[116:119]\n"
    "v_mov_b32 v131, v163\n"
    "v_mov_b32 v136, v168\n"
    "v_mfma_f32_16x16x32_bf16 v[124:127], v[236:239], v[180:183], v[124:127]\n"
    "v_mov_b32 v137, v169\n"
    "v_mov_b32 v140, v170\n"
    "v_mfma_f32_16x16x32_bf16 v[120:123], v[236:239], v[176:179], v[120:123]\n"
    "ds_write_b128 v161, a[192:195] offset:0\n"
    "v_mov_b32 v141, v171\n"
    "v_mfma_f32_16x16x32_bf16 v[112:115], v[232:235], v[176:179], v[112:115]\n"
    "v_lshrrev_b32 v153, 2, v164\n"
    "v_lshlrev_b32 v154, 7, v153\n"
    "v_and_b32 v155, 3, v164\n"
    "v_lshl_add_u32 v154, v155, 3, v154\n"
    "v_bfe_u32 v153, v164, 3, 2\n"
    "v_xor_b32 v155, 0, v153\n"
    "v_lshl_add_u32 v144, v155, 5, v154\n"
    "v_add_u32 v148, 0x10000, v144\n"
    "v_add_u32 v156, 0x20000, v144\n"
    "v_xor_b32 v155, 1, v153\n"
    "v_lshl_add_u32 v145, v155, 5, v154\n"
    "v_add_u32 v149, 0x10000, v145\n"
    "v_add_u32 v157, 0x20000, v145\n"
    "v_xor_b32 v155, 2, v153\n"
    "v_lshl_add_u32 v146, v155, 5, v154\n"
    "v_add_u32 v150, 0x10000, v146\n"
    "v_add_u32 v158, 0x20000, v146\n"
    "v_xor_b32 v155, 3, v153\n"
    "v_lshl_add_u32 v147, v155, 5, v154\n"
    "v_add_u32 v151, 0x10000, v147\n"
    "v_add_u32 v159, 0x20000, v147\n"
    "s_waitcnt lgkmcnt(5)\n"
    "s_barrier\n"
    "ds_read_b128 v[160:163], v129 offset:0\n"
    "ds_read_b128 v[164:167], v129 offset:4096\n"
    "ds_read_b128 v[168:171], v129 offset:8192\n"
    "ds_read_b128 v[172:175], v129 offset:12288\n"
    "ds_read_b128 v[176:179], v129 offset:16384\n"
    "ds_read_b128 v[180:183], v129 offset:20480\n"
    "ds_read_b128 v[184:187], v129 offset:24576\n"
    "ds_read_b128 v[188:191], v129 offset:28672\n"
    "ds_read_b128 v[192:195], v129 offset:32768\n"
    "ds_read_b128 v[196:199], v129 offset:36864\n"
    "ds_read_b128 v[200:203], v129 offset:40960\n"
    "ds_read_b128 v[204:207], v129 offset:45056\n"
    "ds_write_b128 v130, a[196:199] offset:4096\n"
    "v_pk_mul_f32 v[0:1], v[0:1], v[136:137] op_sel:[0,0] op_sel_hi:[1,0]\n"
    "v_pk_mul_f32 v[2:3], v[2:3], v[136:137] op_sel:[0,0] op_sel_hi:[1,0]\n"
    "s_waitcnt lgkmcnt(11)\n"
    "v_pk_fma_f32 v[0:1], v[160:161], v[140:141], v[0:1] op_sel:[0,0,0] op_sel_hi:[1,0,1]\n"
    "v_pk_fma_f32 v[2:3], v[162:163], v[140:141], v[2:3] op_sel:[0,0,0] op_sel_hi:[1,0,1]\n"
    "v_cvt_pk_bf16_f32 v0, v0, v1\n"
    "v_cvt_pk_bf16_f32 v1, v2, v3\n"
    "ds_write_b128 v130, a[200:203] offset:8192\n"
    "v_pk_mul_f32 v[4:5], v[4:5], v[136:137] op_sel:[0,1] op_sel_hi:[1,1]\n"
    "v_pk_mul_f32 v[6:7], v[6:7], v[136:137] op_sel:[0,1] op_sel_hi:[1,1]\n"
    "v_pk_fma_f32 v[4:5], v[164:165], v[140:141], v[4:5] op_sel:[0,1,0] op_sel_hi:[1,1,1]\n"
    "v_pk_fma_f32 v[6:7], v[166:167], v[140:141], v[6:7] op_sel:[0,1,0] op_sel_hi:[1,1,1]\n"
    "v_cvt_pk_bf16_f32 v4, v4, v5\n"
    "v_cvt_pk_bf16_f32 v5, v6, v7\n"
    "v_pk_mul_f32 v[8:9], v[8:9], v[136:137] op_sel:[0,0] op_sel_hi:[1,0]\n"
    "ds_write_b128 v130, a[204:207] offset:12288\n"
    "v_pk_mul_f32 v[10:11], v[10:11], v[136:137] op_sel:[0,0] op_sel_hi:[1,0]\n"
    "s_waitcnt lgkmcnt(11)\n"
    "v_pk_fma_f32 v[8:9], v[168:169], v[140:141], v[8:9] op_sel:[0,0,0] op_sel_hi:[1,0,1]\n"
    "v_pk_fma_f32 v[10:11], v[170:171], v[140:141], v[10:11] op_sel:[0,0,0] op_sel_hi:[1,0,1]\n"
    "v_cvt_pk_bf16_f32 v2, v8, v9\n"
    "v_cvt_pk_bf16_f32 v3, v10, v11\n"
    "v_pk_mul_f32 v[12:13], v[12:13], v[136:137] op_sel:[0,1] op_sel_hi:[1,1]\n"
    "ds_write_b128 v130, a[208:211] offset:16384\n"
    "v_pk_mul_f32 v[14:15], v[14:15], v[136:137] op_sel:[0,1] op_sel_hi:[1,1]\n"
    "v_pk_fma_f32 v[12:13], v[172:173], v[140:141], v[12:13] op_sel:[0,1,0] op_sel_hi:[1,1,1]\n"
    "v_pk_fma_f32 v[14:15], v[174:175], v[140:141], v[14:15] op_sel:[0,1,0] op_sel_hi:[1,1,1]\n"
    "v_cvt_pk_bf16_f32 v6, v12, v13\n"
    "v_cvt_pk_bf16_f32 v7, v14, v15\n"
    "v_pk_mul_f32 v[16:17], v[16:17], v[136:137] op_sel:[0,0] op_sel_hi:[1,0]\n"
    "v_pk_mul_f32 v[18:19], v[18:19], v[136:137] op_sel:[0,0] op_sel_hi:[1,0]\n"
    "ds_write_b128 v130, a[212:215] offset:20480\n"
    "s_waitcnt lgkmcnt(11)\n"
    "v_pk_fma_f32 v[16:17], v[176:177], v[140:141], v[16:17] op_sel:[0,0,0] op_sel_hi:[1,0,1]\n"
    "v_pk_fma_f32 v[18:19], v[178:179], v[140:141], v[18:19] op_sel:[0,0,0] op_sel_hi:[1,0,1]\n"
    "v_cvt_pk_bf16_f32 v16, v16, v17\n"
    "v_cvt_pk_bf16_f32 v17, v18, v19\n"
    "v_pk_mul_f32 v[20:21], v[20:21], v[136:137] op_sel:[0,1] op_sel_hi:[1,1]\n"
    "v_pk_mul_f32 v[22:23], v[22:23], v[136:137] op_sel:[0,1] op_sel_hi:[1,1]\n"
    "ds_write_b128 v130, a[216:219] offset:24576\n"
    "v_pk_fma_f32 v[20:21], v[180:181], v[140:141], v[20:21] op_sel:[0,1,0] op_sel_hi:[1,1,1]\n"
    "v_pk_fma_f32 v[22:23], v[182:183], v[140:141], v[22:23] op_sel:[0,1,0] op_sel_hi:[1,1,1]\n"
    "v_cvt_pk_bf16_f32 v20, v20, v21\n"
    "v_cvt_pk_bf16_f32 v21, v22, v23\n"
    "v_pk_mul_f32 v[24:25], v[24:25], v[136:137] op_sel:[0,0] op_sel_hi:[1,0]\n"
    "v_pk_mul_f32 v[26:27], v[26:27], v[136:137] op_sel:[0,0] op_sel_hi:[1,0]\n"
    "s_waitcnt lgkmcnt(10)\n"
    "ds_write_b128 v130, a[220:223] offset:28672\n"
    "v_pk_fma_f32 v[24:25], v[184:185], v[140:141], v[24:25] op_sel:[0,0,0] op_sel_hi:[1,0,1]\n"
    "v_pk_fma_f32 v[26:27], v[186:187], v[140:141], v[26:27] op_sel:[0,0,0] op_sel_hi:[1,0,1]\n"
    "v_cvt_pk_bf16_f32 v18, v24, v25\n"
    "v_cvt_pk_bf16_f32 v19, v26, v27\n"
    "v_pk_mul_f32 v[28:29], v[28:29], v[136:137] op_sel:[0,1] op_sel_hi:[1,1]\n"
    "v_pk_mul_f32 v[30:31], v[30:31], v[136:137] op_sel:[0,1] op_sel_hi:[1,1]\n"
    "v_pk_fma_f32 v[28:29], v[188:189], v[140:141], v[28:29] op_sel:[0,1,0] op_sel_hi:[1,1,1]\n"
    "ds_write_b128 v130, a[224:227] offset:32768\n"
    "v_pk_fma_f32 v[30:31], v[190:191], v[140:141], v[30:31] op_sel:[0,1,0] op_sel_hi:[1,1,1]\n"
    "v_cvt_pk_bf16_f32 v22, v28, v29\n"
    "v_cvt_pk_bf16_f32 v23, v30, v31\n"
    "buffer_load_dwordx4 v[8:11], v254, s[76:79], s49 offen sc1\n"
    "v_pk_mul_f32 v[32:33], v[32:33], v[136:137] op_sel:[0,0] op_sel_hi:[1,0]\n"
    "v_pk_mul_f32 v[34:35], v[34:35], v[136:137] op_sel:[0,0] op_sel_hi:[1,0]\n"
    "s_waitcnt lgkmcnt(10)\n"
    "ds_write_b128 v130, a[228:231] offset:36864\n"
    "v_pk_fma_f32 v[32:33], v[192:193], v[140:141], v[32:33] op_sel:[0,0,0] op_sel_hi:[1,0,1]\n"
    "v_pk_fma_f32 v[34:35], v[194:195], v[140:141], v[34:35] op_sel:[0,0,0] op_sel_hi:[1,0,1]\n"
    "v_cvt_pk_bf16_f32 v32, v32, v33\n"
    "v_cvt_pk_bf16_f32 v33, v34, v35\n"
    "buffer_load_dwordx4 v[12:15], v254, s[76:79], s52 offen sc1\n"
    "v_pk_mul_f32 v[36:37], v[36:37], v[136:137] op_sel:[0,1] op_sel_hi:[1,1]\n"
    "v_pk_mul_f32 v[38:39], v[38:39], v[136:137] op_sel:[0,1] op_sel_hi:[1,1]\n"
    "ds_write_b128 v130, a[232:235] offset:40960\n"
    "v_pk_fma_f32 v[36:37], v[196:197], v[140:141], v[36:37] op_sel:[0,1,0] op_sel_hi:[1,1,1]\n"
    "v_pk_fma_f32 v[38:39], v[198:199], v[140:141], v[38:39] op_sel:[0,1,0] op_sel_hi:[1,1,1]\n"
    "v_cvt_pk_bf16_f32 v36, v36, v37\n"
    "v_cvt_pk_bf16_f32 v37, v38, v39\n"
    "buffer_load_dwordx4 v[24:27], v254, s[88:91], s50 offen\n"
    "v_pk_mul_f32 v[40:41], v[40:41], v[136:137] op_sel:[0,0] op_sel_hi:[1,0]\n"
    "v_pk_mul_f32 v[42:43], v[42:43], v[136:137] op_sel:[0,0] op_sel_hi:[1,0]\n"
    "ds_write_b128 v130, a[236:239] offset:45056\n"
    "s_waitcnt lgkmcnt(11)\n"
    "v_pk_fma_f32 v[40:41], v[200:201], v[140:141], v[40:41] op_sel:[0,0,0] op_sel_hi:[1,0,1]\n"
    "v_pk_fma_f32 v[42:43], v[202:203], v[140:141], v[42:43] op_sel:[0,0,0] op_sel_hi:[1,0,1]\n"
    "v_cvt_pk_bf16_f32 v34, v40, v41\n"
    "v_cvt_pk_bf16_f32 v35, v42, v43\n"
    "buffer_load_dwordx4 v[28:31], v254, s[88:91], s73 offen\n"
    "v_pk_mul_f32 v[44:45], v[44:45], v[136:137] op_sel:[0,1] op_sel_hi:[1,1]\n"
    "ds_write_b128 v130, a[240:243] offset:49152\n"
    "v_pk_mul_f32 v[46:47], v[46:47], v[136:137] op_sel:[0,1] op_sel_hi:[1,1]\n"
    "v_pk_fma_f32 v[44:45], v[204:205], v[140:141], v[44:45] op_sel:[0,1,0] op_sel_hi:[1,1,1]\n"
    "v_pk_fma_f32 v[46:47], v[206:207], v[140:141], v[46:47] op_sel:[0,1,0] op_sel_hi:[1,1,1]\n"
    "v_cvt_pk_bf16_f32 v38, v44, v45\n"
    "v_cvt_pk_bf16_f32 v39, v46, v47\n"
    "v_pk_mul_f32 v[48:49], v[48:49], v[136:137] op_sel:[0,0] op_sel_hi:[1,0]\n"
    "v_pk_mul_f32 v[50:51], v[50:51], v[136:137] op_sel:[0,0] op_sel_hi:[1,0]\n"
    "ds_write_b128 v130, a[244:247] offset:53248\n"
    "v_pk_mul_f32 v[52:53], v[52:53], v[136:137] op_sel:[0,1] op_sel_hi:[1,1]\n"
    "v_pk_mul_f32 v[54:55], v[54:55], v[136:137] op_sel:[0,1] op_sel_hi:[1,1]\n"
    "v_pk_mul_f32 v[56:57], v[56:57], v[136:137] op_sel:[0,0] op_sel_hi:[1,0]\n"
    "v_pk_mul_f32 v[58:59], v[58:59], v[136:137] op_sel:[0,0] op_sel_hi:[1,0]\n"
    "v_pk_mul_f32 v[60:61], v[60:61], v[136:137] op_sel:[0,1] op_sel_hi:[1,1]\n"
    "v_pk_mul_f32 v[62:63], v[62:63], v[136:137] op_sel:[0,1] op_sel_hi:[1,1]\n"
    "v_pk_mul_f32 v[64:65], v[64:65], v[136:137] op_sel:[0,0] op_sel_hi:[1,0]\n"
    "ds_write_b128 v130, a[248:251] offset:57344\n"
    "v_pk_mul_f32 v[66:67], v[66:67], v[136:137] op_sel:[0,0] op_sel_hi:[1,0]\n"
    "v_pk_mul_f32 v[68:69], v[68:69], v[136:137] op_sel:[0,1] op_sel_hi:[1,1]\n"
    "v_pk_mul_f32 v[70:71], v[70:71], v[136:137] op_sel:[0,1] op_sel_hi:[1,1]\n"
    "v_pk_mul_f32 v[72:73], v[72:73], v[136:137] op_sel:[0,0] op_sel_hi:[1,0]\n"
    "v_pk_mul_f32 v[74:75], v[74:75], v[136:137] op_sel:[0,0] op_sel_hi:[1,0]\n"
    "v_pk_mul_f32 v[76:77], v[76:77], v[136:137] op_sel:[0,1] op_sel_hi:[1,1]\n"
    "v_pk_mul_f32 v[78:79], v[78:79], v[136:137] op_sel:[0,1] op_sel_hi:[1,1]\n"
    "ds_write_b128 v130, a[252:255] offset:61440\n"
    "v_pk_mul_f32 v[80:81], v[80:81], v[136:137] op_sel:[0,0] op_sel_hi:[1,0]\n"
    "v_pk_mul_f32 v[82:83], v[82:83], v[136:137] op_sel:[0,0] op_sel_hi:[1,0]\n"
    "v_pk_mul_f32 v[84:85], v[84:85], v[136:137] op_sel:[0,1] op_sel_hi:[1,1]\n"
    "v_pk_mul_f32 v[86:87], v[86:87], v[136:137] op_sel:[0,1] op_sel_hi:[1,1]\n"
    "v_pk_mul_f32 v[88:89], v[88:89], v[136:137] op_sel:[0,0] op_sel_hi:[1,0]\n"
    "v_pk_mul_f32 v[90:91], v[90:91], v[136:137] op_sel:[0,0] op_sel_hi:[1,0]\n"
    "v_pk_mul_f32 v[92:93], v[92:93], v[136:137] op_sel:[0,1] op_sel_hi:[1,1]\n"
    "v_pk_mul_f32 v[94:95], v[94:95], v[136:137] op_sel:[0,1] op_sel_hi:[1,1]\n"
    "v_pk_mul_f32 v[96:97], v[96:97], v[136:137] op_sel:[0,0] op_sel_hi:[1,0]\n"
    "v_pk_mul_f32 v[98:99], v[98:99], v[136:137] op_sel:[0,0] op_sel_hi:[1,0]\n"
    "v_pk_mul_f32 v[100:101], v[100:101], v[136:137] op_sel:[0,1] op_sel_hi:[1,1]\n"
    "v_pk_mul_f32 v[102:103], v[102:103], v[136:137] op_sel:[0,1] op_sel_hi:[1,1]\n"
    "v_pk_mul_f32 v[104:105], v[104:105], v[136:137] op_sel:[0,0] op_sel_hi:[1,0]\n"
    "v_pk_mul_f32 v[106:107], v[106:107], v[136:137] op_sel:[0,0] op_sel_hi:[1,0]\n"
    "v_pk_mul_f32 v[108:109], v[108:109], v[136:137] op_sel:[0,1] op_sel_hi:[1,1]\n"
    "v_pk_mul_f32 v[110:111], v[110:111], v[136:137] op_sel:[0,1] op_sel_hi:[1,1]\n"
    "v_pk_mul_f32 v[112:113], v[112:113], v[136:137] op_sel:[0,0] op_sel_hi:[1,0]\n"
    "v_pk_mul_f32 v[114:115], v[114:115], v[136:137] op_sel:[0,0] op_sel_hi:[1,0]\n"
    "v_pk_mul_f32 v[116:117], v[116:117], v[136:137] op_sel:[0,1] op_sel_hi:[1,1]\n"
    "v_pk_mul_f32 v[118:119], v[118:119], v[136:137] op_sel:[0,1] op_sel_hi:[1,1]\n"
    "v_pk_mul_f32 v[120:121], v[120:121], v[136:137] op_sel:[0,0] op_sel_hi:[1,0]\n"
    "v_pk_mul_f32 v[122:123], v[122:123], v[136:137] op_sel:[0,0] op_sel_hi:[1,0]\n"
    "v_pk_mul_f32 v[124:125], v[124:125], v[136:137] op_sel:[0,1] op_sel_hi:[1,1]\n"
    "v_pk_mul_f32 v[126:127], v[126:127], v[136:137] op_sel:[0,1] op_sel_hi:[1,1]\n"
    "s_add_u32 m0, s48, 131072\n"
    "s_nop 0\n"
    "buffer_load_dwordx4 v255, s[56:59], s68 offen lds\n"
    "s_add_u32 m0, s48, 132096\n"
    "s_add_u32 s68, s68, 7168\n"
    "buffer_load_dwordx4 v255, s[56:59], s68 offen lds\n"
    "s_add_u32 m0, s48, 133120\n"
    "s_add_u32 s68, s68, 7168\n"
    "buffer_load_dwordx4 v255, s[56:59], s68 offen lds\n"
    "s_add_u32 m0, s48, 134144\n"
    "s_add_u32 s68, s68, 7168\n"
    "buffer_load_dwordx4 v255, s[56:59], s68 offen lds\n"
    "s_waitcnt lgkmcnt(0)\n"
    "s_barrier\n"
    "ds_read_b128 v[160:163], v129 offset:49152\n"
    "ds_read_b128 v[164:167], v129 offset:53248\n"
    "ds_read_b128 v[168:171], v129 offset:57344\n"
    "ds_read_b128 v[172:175], v129 offset:61440\n"
    "ds_read_b128 v[176:179], v131 offset:0\n"
    "ds_read_b128 v[180:183], v131 offset:4096\n"
    "ds_read_b128 v[184:187], v131 offset:8192\n"
    "ds_read_b128 v[188:191], v131 offset:12288\n"
    "ds_read_b128 v[192:195], v131 offset:16384\n"
    "ds_read_b128 v[196:199], v131 offset:20480\n"
    "ds_read_b128 v[200:203], v131 offset:24576\n"
    "ds_read_b128 v[204:207], v131 offset:28672\n"
    "s_add_u32 m0, s48, 135168\n"
    "s_add_u32 s68, s68, 7168\n"
    "buffer_load_dwordx4 v255, s[56:59], s68 offen lds\n"
    "s_add_u32 m0, s48, 136192\n"
    "s_add_u32 s68, s68, 7168\n"
    "buffer_load_dwordx4 v255, s[56:59], s68 offen lds\n"
    "s_add_u32 m0, s48, 137216\n"
    "s_add_u32 s68, s68, 7168\n"
    "buffer_load_dwordx4 v255, s[56:59], s68 offen lds\n"
    "s_add_u32 m0, s48, 138240\n"
    "s_add_u32 s68, s68, 7168\n"
    "buffer_load_dwordx4 v255, s[56:59], s68 offen lds\n"
    "s_waitcnt lgkmcnt(10)\n"
    "v_pk_fma_f32 v[48:49], v[160:161], v[140:141], v[48:49] op_sel:[0,0,0] op_sel_hi:[1,0,1]\n"
    "v_pk_fma_f32 v[50:51], v[162:163], v[140:141], v[50:51] op_sel:[0,0,0] op_sel_hi:[1,0,1]\n"
    "ds_read_b128 v[160:163], v131 offset:32768\n"
    "v_cvt_pk_bf16_f32 v48, v48, v49\n"
    "v_cvt_pk_bf16_f32 v49, v50, v51\n"
    "v_pk_fma_f32 v[52:53], v[164:165], v[140:141], v[52:53] op_sel:[0,1,0] op_sel_hi:[1,1,1]\n"
    "v_pk_fma_f32 v[54:55], v[166:167], v[140:141], v[54:55] op_sel:[0,1,0] op_sel_hi:[1,1,1]\n"
    "ds_read_b128 v[164:167], v131 offset:36864\n"
    "v_cvt_pk_bf16_f32 v52, v52, v53\n"
    "v_cvt_pk_bf16_f32 v53, v54, v55\n"
    "s_waitcnt lgkmcnt(10)\n"
    "v_pk_fma_f32 v[56:57], v[168:169], v[140:141], v[56:57] op_sel:[0,0,0] op_sel_hi:[1,0,1]\n"
    "v_pk_fma_f32 v[58:59], v[170:171], v[140:141], v[58:59] op_sel:[0,0,0] op_sel_hi:[1,0,1]\n"
    "ds_read_b128 v[168:171], v131 offset:40960\n"
    "v_cvt_pk_bf16_f32 v50, v56, v57\n"
    "v_cvt_pk_bf16_f32 v51, v58, v59\n"
    "v_pk_fma_f32 v[60:61], v[172:173], v[140:141], v[60:61] op_sel:[0,1,0] op_sel_hi:[1,1,1]\n"
    "v_pk_fma_f32 v[62:63], v[174:175], v[140:141], v[62:63] op_sel:[0,1,0] op_sel_hi:[1,1,1]\n"
    "ds_read_b128 v[172:175], v131 offset:45056\n"
    "v_cvt_pk_bf16_f32 v54, v60, v61\n"
    "v_cvt_pk_bf16_f32 v55, v62, v63\n"
    "s_waitcnt lgkmcnt(10)\n"
    "v_pk_fma_f32 v[64:65], v[176:177], v[140:141], v[64:65] op_sel:[0,0,0] op_sel_hi:[1,0,1]\n"
    "v_pk_fma_f32 v[66:67], v[178:179], v[140:141], v[66:67] op_sel:[0,0,0] op_sel_hi:[1,0,1]\n"
    "ds_read_b128 v[176:179], v131 offset:49152\n"
    "v_cvt_pk_bf16_f32 v64, v64, v65\n"
    "v_cvt_pk_bf16_f32 v65, v66, v67\n"
    "v_pk_fma_f32 v[68:69], v[180:181], v[140:141], v[68:69] op_sel:[0,1,0] op_sel_hi:[1,1,1]\n"
    "v_pk_fma_f32 v[70:71], v[182:183], v[140:141], v[70:71] op_sel:[0,1,0] op_sel_hi:[1,1,1]\n"
    "ds_read_b128 v[180:183], v131 offset:53248\n"
    "v_cvt_pk_bf16_f32 v68, v68, v69\n"
    "v_cvt_pk_bf16_f32 v69, v70, v71\n"
    "s_waitcnt lgkmcnt(10)\n"
    "v_pk_fma_f32 v[72:73], v[184:185], v[140:141], v[72:73] op_sel:[0,0,0] op_sel_hi:[1,0,1]\n"
    "v_pk_fma_f32 v[74:75], v[186:187], v[140:141], v[74:75] op_sel:[0,0,0] op_sel_hi:[1,0,1]\n"
    "ds_read_b128 v[184:187], v131 offset:57344\n"
    "v_cvt_pk_bf16_f32 v66, v72, v73\n"
    "v_cvt_pk_bf16_f32 v67, v74, v75\n"
    "v_pk_fma_f32 v[76:77], v[188:189], v[140:141], v[76:77] op_sel:[0,1,0] op_sel_hi:[1,1,1]\n"
    "v_pk_fma_f32 v[78:79], v[190:191], v[140:141], v[78:79] op_sel:[0,1,0] op_sel_hi:[1,1,1]\n"
    "ds_read_b128 v[188:191], v131 offset:61440\n"
    "v_cvt_pk_bf16_f32 v70, v76, v77\n"
    "v_cvt_pk_bf16_f32 v71, v78, v79\n"
    "s_waitcnt lgkmcnt(10)\n"
    "v_pk_fma_f32 v[80:81], v[192:193], v[140:141], v[80:81] op_sel:[0,0,0] op_sel_hi:[1,0,1]\n"
    "v_pk_fma_f32 v[82:83], v[194:195], v[140:141], v[82:83] op_sel:[0,0,0] op_sel_hi:[1,0,1]\n"
    "v_cvt_pk_bf16_f32 v80, v80, v81\n"
    "v_cvt_pk_bf16_f32 v81, v82, v83\n"
    "v_pk_fma_f32 v[84:85], v[196:197], v[140:141], v[84:85] op_sel:[0,1,0] op_sel_hi:[1,1,1]\n"
    "v_pk_fma_f32 v[86:87], v[198:199], v[140:141], v[86:87] op_sel:[0,1,0] op_sel_hi:[1,1,1]\n"
    "s_waitcnt lgkmcnt(0)\n"
    "s_barrier\n"
    "s_add_u32 m0, s48, 0\n"
    "s_sub_u32 s68, s68, 50048\n"
    "v_cvt_pk_bf16_f32 v84, v84, v85\n"
    "v_cvt_pk_bf16_f32 v85, v86, v87\n"
    "buffer_load_dwordx4 v255, s[56:59], s68 offen lds\n"
    "v_pk_fma_f32 v[88:89], v[200:201], v[140:141], v[88:89] op_sel:[0,0,0] op_sel_hi:[1,0,1]\n"
    "v_pk_fma_f32 v[90:91], v[202:203], v[140:141], v[90:91] op_sel:[0,0,0] op_sel_hi:[1,0,1]\n"
    "s_add_u32 m0, s48, 1024\n"
    "s_add_u32 s68, s68, 7168\n"
    "v_cvt_pk_bf16_f32 v82, v88, v89\n"
    "v_cvt_pk_bf16_f32 v83, v90, v91\n"
    "buffer_load_dwordx4 v255, s[56:59], s68 offen lds\n"
    "v_pk_fma_f32 v[92:93], v[204:205], v[140:141], v[92:93] op_sel:[0,1,0] op_sel_hi:[1,1,1]\n"
    "v_pk_fma_f32 v[94:95], v[206:207], v[140:141], v[94:95] op_sel:[0,1,0] op_sel_hi:[1,1,1]\n"
    "s_add_u32 m0, s48, 2048\n"
    "s_add_u32 s68, s68, 7168\n"
    "v_cvt_pk_bf16_f32 v86, v92, v93\n"
    "v_cvt_pk_bf16_f32 v87, v94, v95\n"
    "buffer_load_dwordx4 v255, s[56:59], s68 offen lds\n"
    "v_pk_fma_f32 v[96:97], v[160:161], v[140:141], v[96:97] op_sel:[0,0,0] op_sel_hi:[1,0,1]\n"
    "v_pk_fma_f32 v[98:99], v[162:163], v[140:141], v[98:99] op_sel:[0,0,0] op_sel_hi:[1,0,1]\n"
    "s_add_u32 m0, s48, 3072\n"
    "s_add_u32 s68, s68, 7168\n"
    "v_cvt_pk_bf16_f32 v96, v96, v97\n"
    "v_cvt_pk_bf16_f32 v97, v98, v99\n"
    "buffer_load_dwordx4 v255, s[56:59], s68 offen lds\n"
    "v_pk_fma_f32 v[100:101], v[164:165], v[140:141], v[100:101] op_sel:[0,1,0] op_sel_hi:[1,1,1]\n"
    "v_pk_fma_f32 v[102:103], v[166:167], v[140:141], v[102:103] op_sel:[0,1,0] op_sel_hi:[1,1,1]\n"
    "s_add_u32 m0, s48, 4096\n"
    "s_add_u32 s68, s68, 7168\n"
    "v_cvt_pk_bf16_f32 v100, v100, v101\n"
    "v_cvt_pk_bf16_f32 v101, v102, v103\n"
    "buffer_load_dwordx4 v255, s[56:59], s68 offen lds\n"
    "v_pk_fma_f32 v[104:105], v[168:169], v[140:141], v[104:105] op_sel:[0,0,0] op_sel_hi:[1,0,1]\n"
    "v_pk_fma_f32 v[106:107], v[170:171], v[140:141], v[106:107] op_sel:[0,0,0] op_sel_hi:[1,0,1]\n"
    "s_add_u32 m0, s48, 5120\n"
    "s_add_u32 s68, s68, 7168\n"
    "v_cvt_pk_bf16_f32 v98, v104, v105\n"
    "v_cvt_pk_bf16_f32 v99, v106, v107\n"
    "buffer_load_dwordx4 v255, s[56:59], s68 offen lds\n"
    "v_pk_fma_f32 v[108:109], v[172:173], v[140:141], v[108:109] op_sel:[0,1,0] op_sel_hi:[1,1,1]\n"
    "v_pk_fma_f32 v[110:111], v[174:175], v[140:141], v[110:111] op_sel:[0,1,0] op_sel_hi:[1,1,1]\n"
    "s_add_u32 m0, s48, 6144\n"
    "s_add_u32 s68, s68, 7168\n"
    "v_cvt_pk_bf16_f32 v102, v108, v109\n"
    "v_cvt_pk_bf16_f32 v103, v110, v111\n"
    "buffer_load_dwordx4 v255, s[56:59], s68 offen lds\n"
    "v_pk_fma_f32 v[112:113], v[176:177], v[140:141], v[112:113] op_sel:[0,0,0] op_sel_hi:[1,0,1]\n"
    "v_pk_fma_f32 v[114:115], v[178:179], v[140:141], v[114:115] op_sel:[0,0,0] op_sel_hi:[1,0,1]\n"
    "s_add_u32 m0, s48, 7168\n"
    "s_add_u32 s68, s68, 7168\n"
    "v_cvt_pk_bf16_f32 v112, v112, v113\n"
    "v_cvt_pk_bf16_f32 v113, v114, v115\n"
    "buffer_load_dwordx4 v255, s[56:59], s68 offen lds\n"
    "v_pk_fma_f32 v[116:117], v[180:181], v[140:141], v[116:117] op_sel:[0,1,0] op_sel_hi:[1,1,1]\n"
    "v_pk_fma_f32 v[118:119], v[182:183], v[140:141], v[118:119] op_sel:[0,1,0] op_sel_hi:[1,1,1]\n"
    "v_cvt_pk_bf16_f32 v116, v116, v117\n"
    "v_cvt_pk_bf16_f32 v117, v118, v119\n"
    "v_pk_fma_f32 v[120:121], v[184:185], v[140:141], v[120:121] op_sel:[0,0,0] op_sel_hi:[1,0,1]\n"
    "v_pk_fma_f32 v[122:123], v[186:187], v[140:141], v[122:123] op_sel:[0,0,0] op_sel_hi:[1,0,1]\n"
    "v_cvt_pk_bf16_f32 v114, v120, v121\n"
    "v_cvt_pk_bf16_f32 v115, v122, v123\n"
    "v_pk_fma_f32 v[124:125], v[188:189], v[140:141], v[124:125] op_sel:[0,1,0] op_sel_hi:[1,1,1]\n"
    "v_pk_fma_f32 v[126:127], v[190:191], v[140:141], v[126:127] op_sel:[0,1,0] op_sel_hi:[1,1,1]\n"
    "v_cvt_pk_bf16_f32 v118, v124, v125\n"
    "v_cvt_pk_bf16_f32 v119, v126, v127\n"
    "s_waitcnt vmcnt(8)\n"
    "s_barrier\n"
    "ds_read_b64_tr_b16 v[208:209], v156 offset:0\n"
    "ds_read_b64_tr_b16 v[210:211], v156 offset:2048\n"
    "ds_read_b64_tr_b16 v[212:213], v156 offset:4096\n"
    "ds_read_b64_tr_b16 v[214:215], v156 offset:6144\n"
    "ds_read_b64_tr_b16 v[216:217], v156 offset:8192\n"
    "ds_read_b64_tr_b16 v[218:219], v156 offset:10240\n"
    "ds_read_b64_tr_b16 v[220:221], v156 offset:12288\n"
    "ds_read_b64_tr_b16 v[222:223], v156 offset:14336\n"
    "ds_read_b64_tr_b16 v[224:225], v156 offset:16384\n"
    "ds_read_b64_tr_b16 v[226:227], v156 offset:18432\n"
    "ds_read_b64_tr_b16 v[228:229], v156 offset:20480\n"
    "ds_read_b64_tr_b16 v[230:231], v156 offset:22528\n"
    "v_lshrrev_b32 v152, 4, v254\n"
    "v_bfe_u32 v153, v152, 3, 1\n"
    "v_mul_u32_u24 v153, 114688, v153\n"
    "v_lshrrev_b32 v154, 4, v152\n"
    "v_lshl_add_u32 v153, v154, 7, v153\n"
    "v_and_b32 v154, 7, v152\n"
    "v_lshl_add_u32 v132, v154, 4, v153\n"
    "v_add_u32 v133, 229376, v132\n"
    "s_mov_b32 s69, 0\n"
    "s_waitcnt lgkmcnt(8)\n"
    "v_mfma_f32_16x16x32_bf16 v[160:163], v[208:211], v[0:3], 0\n"
    "s_add_u32 m0, s48, 32768\n"
    "ds_read_b64_tr_b16 v[232:233], v156 offset:24576\n"
    "v_add_u32 v40, s53, v254\n"
    "v_mfma_f32_16x16x32_bf16 v[160:163], v[212:215], v[16:19], v[160:163]\n"
    "s_sub_u32 s68, s68, 50048\n"
    "ds_read_b64_tr_b16 v[234:235], v156 offset:26624\n"
    "v_add_u32 v41, s51, v254\n"
    "v_mfma_f32_16x16x32_bf16 v[164:167], v[212:215], v[20:23], 0\n"
    "ds_read_b64_tr_b16 v[236:237], v156 offset:28672\n"
    "buffer_load_dwordx4 v255, s[56:59], s68 offen lds\n"
    "v_mfma_f32_16x16x32_bf16 v[164:167], v[208:211], v[4:7], v[164:167]\n"
    "s_waitcnt lgkmcnt(7)\n"
    "ds_read_b64_tr_b16 v[238:239], v156 offset:30720\n"
    "v_subrev_u32 v244, 3584, v40\n"
    "v_mfma_f32_16x16x32_bf16 v[164:167], v[216:219], v[36:39], v[164:167]\n"
    "s_add_u32 m0, s48, 33792\n"
    "ds_read_b64_tr_b16 v[208:209], v157 offset:0\n"
    "v_cmp_gt_u32 vcc, 1792, v244\n"
    "v_mfma_f32_16x16x32_bf16 v[164:167], v[220:223], v[52:55], v[164:167]\n"
    "s_add_u32 s68, s68, 7168\n"
    "ds_read_b64_tr_b16 v[210:211], v157 offset:2048\n"
    "v_add_u32 v40, 114688, v40\n"
    "v_mfma_f32_16x16x32_bf16 v[160:163], v[220:223], v[48:51], v[160:163]\n"
    "ds_read_b64_tr_b16 v[212:213], v157 offset:4096\n"
    "buffer_load_dwordx4 v255, s[56:59], s68 offen lds\n"
    "v_mfma_f32_16x16x32_bf16 v[160:163], v[216:219], v[32:35], v[160:163]\n"
    "s_waitcnt lgkmcnt(7)\n"
    "ds_read_b64_tr_b16 v[214:215], v157 offset:6144\n"
    "v_cndmask_b32 v242, 0, 1.0, vcc\n"
    "v_mfma_f32_16x16x32_bf16 v[160:163], v[224:227], v[64:67], v[160:163]\n"
    "s_add_u32 m0, s48, 34816\n"
    "ds_read_b64_tr_b16 v[216:217], v157 offset:8192\n"
    "v_subrev_u32 v244, 3584, v41\n"
    "v_mfma_f32_16x16x32_bf16 v[160:163], v[228:231], v[80:83], v[160:163]\n"
    "s_add_u32 s68, s68, 7168\n"
    "ds_read_b64_tr_b16 v[218:219], v157 offset:10240\n"
    "v_cmp_gt_u32 vcc, 1792, v244\n"
    "v_mfma_f32_16x16x32_bf16 v[164:167], v[228:231], v[84:87], v[164:167]\n"
    "ds_read_b64_tr_b16 v[220:221], v157 offset:12288\n"
    "buffer_load_dwordx4 v255, s[56:59], s68 offen lds\n"
    "v_mfma_f32_16x16x32_bf16 v[164:167], v[224:227], v[68:71], v[164:167]\n"
    "s_waitcnt lgkmcnt(7)\n"
    "ds_read_b64_tr_b16 v[222:223], v157 offset:14336\n"
    "v_add_u32 v41, 114688, v41\n"
    "v_mfma_f32_16x16x32_bf16 v[164:167], v[232:235], v[100:103], v[164:167]\n"
    "s_add_u32 m0, s48, 35840\n"
    "ds_read_b64_tr_b16 v[224:225], v157 offset:16384\n"
    "v_cndmask_b32 v243, 0, 1.0, vcc\n"
    "v_mfma_f32_16x16x32_bf16 v[164:167], v[236:239], v[116:119], v[164:167]\n"
    "s_add_u32 s68, s68, 7168\n"
    "ds_read_b64_tr_b16 v[226:227], v157 offset:18432\n"
    "v_and_b32 v153, 7, v152\n"
    "v_mfma_f32_16x16x32_bf16 v[160:163], v[236:239], v[112:115], v[160:163]\n"
    "ds_read_b64_tr_b16 v[228:229], v157 offset:20480\n"
    "buffer_load_dwordx4 v255, s[56:59], s68 offen lds\n"
    "v_mfma_f32_16x16x32_bf16 v[160:163], v[232:235], v[96:99], v[160:163]\n"
    "s_waitcnt lgkmcnt(7)\n"
    "ds_read_b64_tr_b16 v[230:231], v157 offset:22528\n"
    "v_bfe_u32 v154, v152, 3, 2\n"
    "v_mfma_f32_16x16x32_bf16 v[168:171], v[208:211], v[0:3], 0\n"
    "s_add_u32 m0, s48, 36864\n"
    "ds_read_b64_tr_b16 v[232:233], v157 offset:24576\n"
    "v_lshrrev_b32 v155, 5, v152\n"
    "v_mfma_f32_16x16x32_bf16 v[168:171], v[212:215], v[16:19], v[168:171]\n"
    "s_add_u32 s68, s68, 7168\n"
    "ds_read_b64_tr_b16 v[234:235], v157 offset:26624\n"
    "v_lshlrev_b32 v128, 7, v155\n"
    "v_mfma_f32_16x16x32_bf16 v[172:175], v[212:215], v[20:23], 0\n"
    "ds_read_b64_tr_b16 v[236:237], v157 offset:28672\n"
    "buffer_load_dwordx4 v255, s[56:59], s68 offen lds\n"
    "v_mfma_f32_16x16x32_bf16 v[172:175], v[208:211], v[4:7], v[172:175]\n"
    "s_waitcnt lgkmcnt(7)\n"
    "ds_read_b64_tr_b16 v[238:239], v157 offset:30720\n"
    "v_lshl_add_u32 v128, v154, 4, v128\n"
    "v_mfma_f32_16x16x32_bf16 v[172:175], v[216:219], v[36:39], v[172:175]\n"
    "s_add_u32 m0, s48, 37888\n"
    "ds_read_b64_tr_b16 v[208:209], v158 offset:0\n"
    "v_lshl_add_u32 v128, s72, 8, v128\n"
    "v_mfma_f32_16x16x32_bf16 v[172:175], v[220:223], v[52:55], v[172:175]\n"
    "s_add_u32 s68, s68, 7168\n"
    "ds_read_b64_tr_b16 v[210:211], v158 offset:2048\n"
    "v_mul_u32_u24 v129, 896, v153\n"
    "v_mfma_f32_16x16x32_bf16 v[168:171], v[220:223], v[48:51], v[168:171]\n"
    "ds_read_b64_tr_b16 v[212:213], v158 offset:4096\n"
    "buffer_load_dwordx4 v255, s[56:59], s68 offen lds\n"
    "v_mfma_f32_16x16x32_bf16 v[168:171], v[216:219], v[32:35], v[168:171]\n"
    "s_waitcnt lgkmcnt(7)\n"
    "ds_read_b64_tr_b16 v[214:215], v158 offset:6144\n"
    "v_lshrrev_b32 v244, 1, v128\n"
    "v_mfma_f32_16x16x32_bf16 v[168:171], v[224:227], v[64:67], v[168:171]\n"
    "s_add_u32 m0, s48, 38912\n"
    "ds_read_b64_tr_b16 v[216:217], v158 offset:8192\n"
    "v_add_u32 v129, v129, v244\n"
    "v_mfma_f32_16x16x32_bf16 v[168:171], v[228:231], v[80:83], v[168:171]\n"
    "s_add_u32 s68, s68, 7168\n"
    "ds_read_b64_tr_b16 v[218:219], v158 offset:10240\n"
    "v_add_u32 v128, 114688, v128\n"
    "v_mfma_f32_16x16x32_bf16 v[172:175], v[228:231], v[84:87], v[172:175]\n"
    "ds_read_b64_tr_b16 v[220:221], v158 offset:12288\n"
    "buffer_load_dwordx4 v255, s[56:59], s68 offen lds\n"
    "v_mfma_f32_16x16x32_bf16 v[172:175], v[224:227], v[68:71], v[172:175]\n"
    "s_waitcnt lgkmcnt(7)\n"
    "ds_read_b64_tr_b16 v[222:223], v158 offset:14336\n"
    "v_add_u32 v129, 121856, v129\n"
    "v_mfma_f32_16x16x32_bf16 v[172:175], v[232:235], v[100:103], v[172:175]\n"
    "s_add_u32 m0, s48, 39936\n"
    "ds_read_b64_tr_b16 v[224:225], v158 offset:16384\n"
    "v_mfma_f32_16x16x32_bf16 v[172:175], v[236:239], v[116:119], v[172:175]\n"
    "s_add_u32 s68, s68, 7168\n"
    "ds_read_b64_tr_b16 v[226:227], v158 offset:18432\n"
    "v_mfma_f32_16x16x32_bf16 v[168:171], v[236:239], v[112:115], v[168:171]\n"
    "ds_read_b64_tr_b16 v[228:229], v158 offset:20480\n"
    "buffer_load_dwordx4 v255, s[56:59], s68 offen lds\n"
    "v_mfma_f32_16x16x32_bf16 v[168:171], v[232:235], v[96:99], v[168:171]\n"
    "s_waitcnt lgkmcnt(7)\n"
    "ds_read_b64_tr_b16 v[230:231], v158 offset:22528\n"
    "v_mfma_f32_16x16x32_bf16 v[176:179], v[208:211], v[0:3], 0\n"
    "s_add_u32 m0, s48, 65536\n"
    "ds_read_b64_tr_b16 v[232:233], v158 offset:24576\n"
    "v_mfma_f32_16x16x32_bf16 v[176:179], v[212:215], v[16:19], v[176:179]\n"
    "s_sub_u32 s68, s68, 50048\n"
    "ds_read_b64_tr_b16 v[234:235], v158 offset:26624\n"
    "v_mfma_f32_16x16x32_bf16 v[180:183], v[212:215], v[20:23], 0\n"
    "ds_read_b64_tr_b16 v[236:237], v158 offset:28672\n"
    "buffer_load_dwordx4 v255, s[56:59], s68 offen lds\n"
    "v_mfma_f32_16x16x32_bf16 v[180:183], v[208:211], v[4:7], v[180:183]\n"
    "s_waitcnt lgkmcnt(7)\n"
    "ds_read_b64_tr_b16 v[238:239], v158 offset:30720\n"
    "v_mfma_f32_16x16x32_bf16 v[180:183], v[216:219], v[36:39], v[180:183]\n"
    "s_add_u32 m0, s48, 66560\n"
    "ds_read_b64_tr_b16 v[208:209], v159 offset:0\n"
    "v_mfma_f32_16x16x32_bf16 v[180:183], v[220:223], v[52:55], v[180:183]\n"
    "s_add_u32 s68, s68, 7168\n"
    "ds_read_b64_tr_b16 v[210:211], v159 offset:2048\n"
    "v_cvt_pk_bf16_f32 v192, v160, v161\n"
    "v_mfma_f32_16x16x32_bf16 v[176:179], v[220:223], v[48:51], v[176:179]\n"
    "ds_read_b64_tr_b16 v[212:213], v159 offset:4096\n"
    "buffer_load_dwordx4 v255, s[56:59], s68 offen lds\n"
    "v_mfma_f32_16x16x32_bf16 v[176:179], v[216:219], v[32:35], v[176:179]\n"
    "s_waitcnt lgkmcnt(7)\n"
    "ds_read_b64_tr_b16 v[214:215], v159 offset:6144\n"
    "v_cvt_pk_bf16_f32 v193, v162, v163\n"
    "v_mfma_f32_16x16x32_bf16 v[176:179], v[224:227], v[64:67], v[176:179]\n"
    "s_add_u32 m0, s48, 67584\n"
    "ds_read_b64_tr_b16 v[216:217], v159 offset:8192\n"
    "v_cvt_pk_bf16_f32 v194, v168, v169\n"
    "v_mfma_f32_16x16x32_bf16 v[176:179], v[228:231], v[80:83], v[176:179]\n"
    "s_add_u32 s68, s68, 7168\n"
    "ds_read_b64_tr_b16 v[218:219], v159 offset:10240\n"
    "v_cvt_pk_bf16_f32 v195, v170, v171\n"
    "v_mfma_f32_16x16x32_bf16 v[180:183], v[228:231], v[84:87], v[180:183]\n"
    "ds_read_b64_tr_b16 v[220:221], v159 offset:12288\n"
    "buffer_load_dwordx4 v255, s[56:59], s68 offen lds\n"
    "v_mfma_f32_16x16x32_bf16 v[180:183], v[224:227], v[68:71], v[180:183]\n"
    "s_waitcnt lgkmcnt(7)\n"
    "ds_read_b64_tr_b16 v[222:223], v159 offset:14336\n"
    "buffer_store_dwordx4 v[192:195], v132, s[64:67], s69 offen offset:0 sc1\n"
    "v_mfma_f32_16x16x32_bf16 v[180:183], v[232:235], v[100:103], v[180:183]\n"
    "s_add_u32 m0, s48, 68608\n"
    "ds_read_b64_tr_b16 v[224:225], v159 offset:16384\n"
    "v_cvt_pk_bf16_f32 v196, v164, v165\n"
    "v_mfma_f32_16x16x32_bf16 v[180:183], v[236:239], v[116:119], v[180:183]\n"
    "s_add_u32 s68, s68, 7168\n"
    "ds_read_b64_tr_b16 v[226:227], v159 offset:18432\n"
    "v_cvt_pk_bf16_f32 v197, v166, v167\n"
    "v_mfma_f32_16x16x32_bf16 v[176:179], v[236:239], v[112:115], v[176:179]\n"
    "ds_read_b64_tr_b16 v[228:229], v159 offset:20480\n"
    "buffer_load_dwordx4 v255, s[56:59], s68 offen lds\n"
    "v_mfma_f32_16x16x32_bf16 v[176:179], v[232:235], v[96:99], v[176:179]\n"
    "s_waitcnt lgkmcnt(7)\n"
    "ds_read_b64_tr_b16 v[230:231], v159 offset:22528\n"
    "v_cvt_pk_bf16_f32 v198, v172, v173\n"
    "v_mfma_f32_16x16x32_bf16 v[184:187], v[208:211], v[0:3], 0\n"
    "s_add_u32 m0, s48, 69632\n"
    "ds_read_b64_tr_b16 v[232:233], v159 offset:24576\n"
    "v_cvt_pk_bf16_f32 v199, v174, v175\n"
    "v_mfma_f32_16x16x32_bf16 v[184:187], v[212:215], v[16:19], v[184:187]\n"
    "s_add_u32 s68, s68, 7168\n"
    "ds_read_b64_tr_b16 v[234:235], v159 offset:26624\n"
    "buffer_store_dwordx4 v[196:199], v133, s[64:67], s69 offen offset:0 sc1\n"
    "v_mfma_f32_16x16x32_bf16 v[188:191], v[212:215], v[20:23], 0\n"
    "s_waitcnt vmcnt(14)\n"
    "s_barrier\n"
    "v_mfma_f32_16x16x32_bf16 v[188:191], v[208:211], v[4:7], v[188:191]\n"
    "s_waitcnt lgkmcnt(6)\n"
    "ds_read_b64_tr_b16 v[236:237], v159 offset:28672\n"
    "ds_read_b64_tr_b16 v[238:239], v159 offset:30720\n"
    "v_mfma_f32_16x16x32_bf16 v[188:191], v[216:219], v[36:39], v[188:191]\n"
    "ds_read_b64_tr_b16 v[208:209], v144 offset:0\n"
    "buffer_load_dwordx4 v255, s[56:59], s68 offen lds\n"
    "v_mfma_f32_16x16x32_bf16 v[188:191], v[220:223], v[52:55], v[188:191]\n"
    "ds_read_b64_tr_b16 v[210:211], v144 offset:2048\n"
    "v_mfma_f32_16x16x32_bf16 v[184:187], v[220:223], v[48:51], v[184:187]\n"
    "s_add_u32 m0, s48, 70656\n"
    "ds_read_b64_tr_b16 v[212:213], v144 offset:4096\n"
    "v_mfma_f32_16x16x32_bf16 v[184:187], v[216:219], v[32:35], v[184:187]\n"
    "s_waitcnt lgkmcnt(7)\n"
    "ds_read_b64_tr_b16 v[214:215], v144 offset:6144\n"
    "v_mfma_f32_16x16x32_bf16 v[184:187], v[224:227], v[64:67], v[184:187]\n"
    "s_add_u32 s68, s68, 7168\n"
    "ds_read_b64_tr_b16 v[216:217], v144 offset:8192\n"
    "v_mfma_f32_16x16x32_bf16 v[184:187], v[228:231], v[80:83], v[184:187]\n"
    "ds_read_b64_tr_b16 v[218:219], v144 offset:10240\n"
    "buffer_load_dwordx4 v255, s[56:59], s68 offen lds\n"
    "v_mfma_f32_16x16x32_bf16 v[188:191], v[228:231], v[84:87], v[188:191]\n"
    "ds_read_b64_tr_b16 v[220:221], v144 offset:12288\n"
    "v_mfma_f32_16x16x32_bf16 v[188:191], v[224:227], v[68:71], v[188:191]\n"
    "s_waitcnt lgkmcnt(7)\n"
    "ds_read_b64_tr_b16 v[222:223], v144 offset:14336\n"
    "v_mfma_f32_16x16x32_bf16 v[188:191], v[232:235], v[100:103], v[188:191]\n"
    "s_add_u32 m0, s48, 71680\n"
    "ds_read_b64_tr_b16 v[224:225], v144 offset:16384\n"
    "v_mfma_f32_16x16x32_bf16 v[188:191], v[236:239], v[116:119], v[188:191]\n"
    "s_add_u32 s68, s68, 7168\n"
    "ds_read_b64_tr_b16 v[226:227], v144 offset:18432\n"
    "v_mfma_f32_16x16x32_bf16 v[184:187], v[236:239], v[112:115], v[184:187]\n"
    "ds_read_b64_tr_b16 v[228:229], v144 offset:20480\n"
    "buffer_load_dwordx4 v255, s[56:59], s68 offen lds\n"
    "v_mfma_f32_16x16x32_bf16 v[184:187], v[232:235], v[96:99], v[184:187]\n"
    "s_waitcnt lgkmcnt(7)\n"
    "ds_read_b64_tr_b16 v[230:231], v144 offset:22528\n"
    "v_mfma_f32_16x16x32_bf16 v[160:163], v[208:211], v[0:3], 0\n"
    "s_add_u32 m0, s48, 72704\n"
    "ds_read_b64_tr_b16 v[232:233], v144 offset:24576\n"
    "v_mfma_f32_16x16x32_bf16 v[160:163], v[212:215], v[16:19], v[160:163]\n"
    "s_add_u32 s68, s68, 7168\n"
    "ds_read_b64_tr_b16 v[234:235], v144 offset:26624\n"
    "v_mfma_f32_16x16x32_bf16 v[164:167], v[212:215], v[20:23], 0\n"
    "ds_read_b64_tr_b16 v[236:237], v144 offset:28672\n"
    "buffer_load_dwordx4 v255, s[56:59], s68 offen lds\n"
    "v_mfma_f32_16x16x32_bf16 v[164:167], v[208:211], v[4:7], v[164:167]\n"
    "s_waitcnt lgkmcnt(7)\n"
    "ds_read_b64_tr_b16 v[238:239], v144 offset:30720\n"
    "v_mfma_f32_16x16x32_bf16 v[164:167], v[216:219], v[36:39], v[164:167]\n"
    "s_add_u32 m0, s48, 98304\n"
    "ds_read_b64_tr_b16 v[208:209], v145 offset:0\n"
    "v_mfma_f32_16x16x32_bf16 v[164:167], v[220:223], v[52:55], v[164:167]\n"
    "s_sub_u32 s68, s68, 50048\n"
    "ds_read_b64_tr_b16 v[210:211], v145 offset:2048\n"
    "v_cvt_pk_bf16_f32 v200, v176, v177\n"
    "v_mfma_f32_16x16x32_bf16 v[160:163], v[220:223], v[48:51], v[160:163]\n"
    "ds_read_b64_tr_b16 v[212:213], v145 offset:4096\n"
    "buffer_load_dwordx4 v255, s[56:59], s68 offen lds\n"
    "v_mfma_f32_16x16x32_bf16 v[160:163], v[216:219], v[32:35], v[160:163]\n"
    "s_waitcnt lgkmcnt(7)\n"
    "ds_read_b64_tr_b16 v[214:215], v145 offset:6144\n"
    "v_cvt_pk_bf16_f32 v201, v178, v179\n"
    "v_mfma_f32_16x16x32_bf16 v[160:163], v[224:227], v[64:67], v[160:163]\n"
    "s_add_u32 m0, s48, 99328\n"
    "ds_read_b64_tr_b16 v[216:217], v145 offset:8192\n"
    "v_cvt_pk_bf16_f32 v202, v184, v185\n"
    "v_mfma_f32_16x16x32_bf16 v[160:163], v[228:231], v[80:83], v[160:163]\n"
    "s_add_u32 s68, s68, 7168\n"
    "ds_read_b64_tr_b16 v[218:219], v145 offset:10240\n"
    "v_cvt_pk_bf16_f32 v203, v186, v187\n"
    "v_mfma_f32_16x16x32_bf16 v[164:167], v[228:231], v[84:87], v[164:167]\n"
    "ds_read_b64_tr_b16 v[220:221], v145 offset:12288\n"
    "buffer_load_dwordx4 v255, s[56:59], s68 offen lds\n"
    "v_mfma_f32_16x16x32_bf16 v[164:167], v[224:227], v[68:71], v[164:167]\n"
    "s_waitcnt lgkmcnt(7)\n"
    "ds_read_b64_tr_b16 v[222:223], v145 offset:14336\n"
    "buffer_store_dwordx4 v[200:203], v132, s[64:67], s69 offen offset:512 sc1\n"
    "v_mfma_f32_16x16x32_bf16 v[164:167], v[232:235], v[100:103], v[164:167]\n"
    "s_add_u32 m0, s48, 100352\n"
    "ds_read_b64_tr_b16 v[224:225], v145 offset:16384\n"
    "v_cvt_pk_bf16_f32 v204, v180, v181\n"
    "v_mfma_f32_16x16x32_bf16 v[164:167], v[236:239], v[116:119], v[164:167]\n"
    "s_add_u32 s68, s68, 7168\n"
    "ds_read_b64_tr_b16 v[226:227], v145 offset:18432\n"
    "v_cvt_pk_bf16_f32 v205, v182, v183\n"
    "v_mfma_f32_16x16x32_bf16 v[160:163], v[236:239], v[112:115], v[160:163]\n"
    "ds_read_b64_tr_b16 v[228:229], v145 offset:20480\n"
    "buffer_load_dwordx4 v255, s[56:59], s68 offen lds\n"
    "v_mfma_f32_16x16x32_bf16 v[160:163], v[232:235], v[96:99], v[160:163]\n"
    "s_waitcnt lgkmcnt(7)\n"
    "ds_read_b64_tr_b16 v[230:231], v145 offset:22528\n"
    "v_cvt_pk_bf16_f32 v206, v188, v189\n"
    "v_mfma_f32_16x16x32_bf16 v[168:171], v[208:211], v[0:3], 0\n"
    "s_add_u32 m0, s48, 101376\n"
    "ds_read_b64_tr_b16 v[232:233], v145 offset:24576\n"
    "v_cvt_pk_bf16_f32 v207, v190, v191\n"
    "v_mfma_f32_16x16x32_bf16 v[168:171], v[212:215], v[16:19], v[168:171]\n"
    "s_add_u32 s68, s68, 7168\n"
    "ds_read_b64_tr_b16 v[234:235], v145 offset:26624\n"
    "buffer_store_dwordx4 v[204:207], v133, s[64:67], s69 offen offset:512 sc1\n"
    "v_mfma_f32_16x16x32_bf16 v[172:175], v[212:215], v[20:23], 0\n"
    "ds_read_b64_tr_b16 v[236:237], v145 offset:28672\n"
    "buffer_load_dwordx4 v255, s[56:59], s68 offen lds\n"
    "v_mfma_f32_16x16x32_bf16 v[172:175], v[208:211], v[4:7], v[172:175]\n"
    "s_waitcnt lgkmcnt(7)\n"
    "ds_read_b64_tr_b16 v[238:239], v145 offset:30720\n"
    "v_lshlrev_b32 v130, 11, v155\n"
    "v_mfma_f32_16x16x32_bf16 v[172:175], v[216:219], v[36:39], v[172:175]\n"
    "s_add_u32 m0, s48, 102400\n"
    "ds_read_b64_tr_b16 v[208:209], v146 offset:0\n"
    "v_lshl_add_u32 v130, v154, 8, v130\n"
    "v_mfma_f32_16x16x32_bf16 v[172:175], v[220:223], v[52:55], v[172:175]\n"
    "s_add_u32 s68, s68, 7168\n"
    "ds_read_b64_tr_b16 v[210:211], v146 offset:2048\n"
    "v_lshl_add_u32 v130, v153, 4, v130\n"
    "v_mfma_f32_16x16x32_bf16 v[168:171], v[220:223], v[48:51], v[168:171]\n"
    "ds_read_b64_tr_b16 v[212:213], v146 offset:4096\n"
    "buffer_load_dwordx4 v255, s[56:59], s68 offen lds\n"
    "v_mfma_f32_16x16x32_bf16 v[168:171], v[216:219], v[32:35], v[168:171]\n"
    "s_waitcnt lgkmcnt(7)\n"
    "ds_read_b64_tr_b16 v[214:215], v146 offset:6144\n"
    "v_lshlrev_b32 v247, 5, v153\n"
    "v_mfma_f32_16x16x32_bf16 v[168:171], v[224:227], v[64:67], v[168:171]\n"
    "s_add_u32 m0, s48, 103424\n"
    "ds_read_b64_tr_b16 v[216:217], v146 offset:8192\n"
    "v_add_u32 v247, 130048, v247\n"
    "v_mfma_f32_16x16x32_bf16 v[168:171], v[228:231], v[80:83], v[168:171]\n"
    "s_add_u32 s68, s68, 7168\n"
    "ds_read_b64_tr_b16 v[218:219], v146 offset:10240\n"
    "v_lshrrev_b32 v244, 3, v152\n"
    "v_mfma_f32_16x16x32_bf16 v[172:175], v[228:231], v[84:87], v[172:175]\n"
    "ds_read_b64_tr_b16 v[220:221], v146 offset:12288\n"
    "buffer_load_dwordx4 v255, s[56:59], s68 offen lds\n"
    "v_mfma_f32_16x16x32_bf16 v[172:175], v[224:227], v[68:71], v[172:175]\n"
    "s_waitcnt lgkmcnt(7)\n"
    "ds_read_b64_tr_b16 v[222:223], v146 offset:14336\n"
    "v_mul_u32_u24 v244, 2688, v244\n"
    "v_mfma_f32_16x16x32_bf16 v[172:175], v[232:235], v[100:103], v[172:175]\n"
    "s_add_u32 m0, s48, 104448\n"
    "ds_read_b64_tr_b16 v[224:225], v146 offset:16384\n"
    "v_add_u32 v134, v244, v255\n"
    "v_mfma_f32_16x16x32_bf16 v[172:175], v[236:239], v[116:119], v[172:175]\n"
    "s_add_u32 s68, s68, 7168\n"
    "ds_read_b64_tr_b16 v[226:227], v146 offset:18432\n"
    "v_mov_b32 v248, s74\n"
    "v_mfma_f32_16x16x32_bf16 v[168:171], v[236:239], v[112:115], v[168:171]\n"
    "ds_read_b64_tr_b16 v[228:229], v146 offset:20480\n"
    "buffer_load_dwordx4 v255, s[56:59], s68 offen lds\n"
    "v_mfma_f32_16x16x32_bf16 v[168:171], v[232:235], v[96:99], v[168:171]\n"
    "s_waitcnt lgkmcnt(7)\n"
    "ds_read_b64_tr_b16 v[230:231], v146 offset:22528\n"
    "v_mov_b32 v249, s75\n"
    "v_mfma_f32_16x16x32_bf16 v[176:179], v[208:211], v[0:3], 0\n"
    "s_add_u32 m0, s48, 105472\n"
    "ds_read_b64_tr_b16 v[232:233], v146 offset:24576\n"
    "v_min_u32 v250, 1, v152\n"
    "v_mfma_f32_16x16x32_bf16 v[176:179], v[212:215], v[16:19], v[176:179]\n"
    "s_add_u32 s68, s68, 7168\n"
    "ds_read_b64_tr_b16 v[234:235], v146 offset:26624\n"
    "v_lshlrev_b32 v250, 30, v250\n"
    "v_mfma_f32_16x16x32_bf16 v[180:183], v[212:215], v[20:23], 0\n"
    "ds_read_b64_tr_b16 v[236:237], v146 offset:28672\n"
    "buffer_load_dwordx4 v255, s[56:59], s68 offen lds\n"
    "v_mfma_f32_16x16x32_bf16 v[180:183], v[208:211], v[4:7], v[180:183]\n"
    "s_waitcnt lgkmcnt(7)\n"
    "ds_read_b64_tr_b16 v[238:239], v146 offset:30720\n"
    "v_and_b32 v251, 15, v152\n"
    "v_mfma_f32_16x16x32_bf16 v[180:183], v[216:219], v[36:39], v[180:183]\n"
    "s_mov_b64 s[84:85], s[18:19]\n"
    "ds_read_b64_tr_b16 v[208:209], v147 offset:0\n"
    "v_lshlrev_b32 v251, 5, v251\n"
    "v_mfma_f32_16x16x32_bf16 v[180:183], v[220:223], v[52:55], v[180:183]\n"
    "s_mov_b32 s86, 1605632\n"
    "ds_read_b64_tr_b16 v[210:211], v147 offset:2048\n"
    "v_cvt_pk_bf16_f32 v192, v160, v161\n"
    "v_mfma_f32_16x16x32_bf16 v[176:179], v[220:223], v[48:51], v[176:179]\n"
    "s_mov_b32 s87, 0x20000\n"
    "ds_read_b64_tr_b16 v[212:213], v147 offset:4096\n"
    "v_cvt_pk_bf16_f32 v193, v162, v163\n"
    "v_mfma_f32_16x16x32_bf16 v[176:179], v[216:219], v[32:35], v[176:179]\n"
    "s_waitcnt lgkmcnt(7)\n"
    "ds_read_b64_tr_b16 v[214:215], v147 offset:6144\n"
    "v_cvt_pk_bf16_f32 v194, v168, v169\n"
    "v_mfma_f32_16x16x32_bf16 v[176:179], v[224:227], v[64:67], v[176:179]\n"
    "s_mov_b32 s82, 1605632\n"
    "ds_read_b64_tr_b16 v[216:217], v147 offset:8192\n"
    "v_cvt_pk_bf16_f32 v195, v170, v171\n"
    "v_mfma_f32_16x16x32_bf16 v[176:179], v[228:231], v[80:83], v[176:179]\n"
    "s_mov_b32 s83, 0x20000\n"
    "ds_read_b64_tr_b16 v[218:219], v147 offset:10240\n"
    "buffer_store_dwordx4 v[192:195], v132, s[64:67], s69 offen offset:1024 sc1\n"
    "v_mfma_f32_16x16x32_bf16 v[180:183], v[228:231], v[84:87], v[180:183]\n"
    "s_and_b32 s70, %1, 3\n"
    "ds_read_b64_tr_b16 v[220:221], v147 offset:12288\n"
    "v_cvt_pk_bf16_f32 v196, v164, v165\n"
    "v_mfma_f32_16x16x32_bf16 v[180:183], v[224:227], v[68:71], v[180:183]\n"
    "s_waitcnt lgkmcnt(7)\n"
    "ds_read_b64_tr_b16 v[222:223], v147 offset:14336\n"
    "v_cvt_pk_bf16_f32 v197, v166, v167\n"
    "v_mfma_f32_16x16x32_bf16 v[180:183], v[232:235], v[100:103], v[180:183]\n"
    "s_mul_i32 s70, s70, 28\n"
    "ds_read_b64_tr_b16 v[224:225], v147 offset:16384\n"
    "v_cvt_pk_bf16_f32 v198, v172, v173\n"
    "v_mfma_f32_16x16x32_bf16 v[180:183], v[236:239], v[116:119], v[180:183]\n"
    "s_lshr_b32 s71, %1, 3\n"
    "ds_read_b64_tr_b16 v[226:227], v147 offset:18432\n"
    "v_cvt_pk_bf16_f32 v199, v174, v175\n"
    "v_mfma_f32_16x16x32_bf16 v[176:179], v[236:239], v[112:115], v[176:179]\n"
    "s_min_u32 s71, s71, 27\n"
    "ds_read_b64_tr_b16 v[228:229], v147 offset:20480\n"
    "buffer_store_dwordx4 v[196:199], v133, s[64:67], s69 offen offset:1024 sc1\n"
    "v_mfma_f32_16x16x32_bf16 v[176:179], v[232:235], v[96:99], v[176:179]\n"
    "s_waitcnt lgkmcnt(7)\n"
    "ds_read_b64_tr_b16 v[230:231], v147 offset:22528\n"
    "v_lshrrev_b32 v153, 4, v152\n"
    "v_mfma_f32_16x16x32_bf16 v[184:187], v[208:211], v[0:3], 0\n"
    "s_add_u32 s70, s70, s71\n"
    "ds_read_b64_tr_b16 v[232:233], v147 offset:24576\n"
    "v_bfe_u32 v154, v152, 2, 2\n"
    "v_mfma_f32_16x16x32_bf16 v[184:187], v[212:215], v[16:19], v[184:187]\n"
    "s_lshr_b32 s55, s70, 3\n"
    "ds_read_b64_tr_b16 v[234:235], v147 offset:26624\n"
    "v_cmp_eq_u32 vcc, v153, v154\n"
    "v_mfma_f32_16x16x32_bf16 v[188:191], v[212:215], v[20:23], 0\n"
    "s_waitcnt vmcnt(22)\n"
    "s_barrier\n"
    "v_mfma_f32_16x16x32_bf16 v[188:191], v[208:211], v[4:7], v[188:191]\n"
    "s_waitcnt lgkmcnt(6)\n"
    "ds_read_b64_tr_b16 v[236:237], v147 offset:28672\n"
    "ds_read_b64_tr_b16 v[238:239], v147 offset:30720\n"
    "v_mfma_f32_16x16x32_bf16 v[188:191], v[216:219], v[36:39], v[188:191]\n"
    "s_lshl_b32 s55, s55, 4\n"
    "ds_read_b64_tr_b16 v[208:209], v144 offset:32768\n"
    "v_and_b32 v153, 1, v152\n"
    "v_mfma_f32_16x16x32_bf16 v[188:191], v[220:223], v[52:55], v[188:191]\n"
    "s_and_b32 s70, s70, 7\n"
    "ds_read_b64_tr_b16 v[210:211], v144 offset:34816\n"
    "v_lshlrev_b32 v153, 4, v153\n"
    "v_mfma_f32_16x16x32_bf16 v[184:187], v[220:223], v[48:51], v[184:187]\n"
    "s_add_u32 s55, s55, s70\n"
    "ds_read_b64_tr_b16 v[212:213], v144 offset:36864\n"
    "v_mov_b32 v154, 0x3f80\n"
    "v_mfma_f32_16x16x32_bf16 v[184:187], v[216:219], v[32:35], v[184:187]\n"
    "s_waitcnt lgkmcnt(7)\n"
    "ds_read_b64_tr_b16 v[214:215], v144 offset:38912\n"
    "v_lshlrev_b32 v154, v153, v154\n"
    "v_mfma_f32_16x16x32_bf16 v[184:187], v[224:227], v[64:67], v[184:187]\n"
    "s_and_b32 s70, %1, 4\n"
    "ds_read_b64_tr_b16 v[216:217], v144 offset:40960\n"
    "v_cndmask_b32 v154, 0, v154, vcc\n"
    "v_mfma_f32_16x16x32_bf16 v[184:187], v[228:231], v[80:83], v[184:187]\n"
    "s_lshl_b32 s70, s70, 1\n"
    "ds_read_b64_tr_b16 v[218:219], v144 offset:43008\n"
    "v_and_b32 v153, 2, v152\n"
    "v_mfma_f32_16x16x32_bf16 v[188:191], v[228:231], v[84:87], v[188:191]\n"
    "s_add_u32 s55, s55, s70\n"
    "ds_read_b64_tr_b16 v[220:221], v144 offset:45056\n"
    "v_cmp_eq_u32 vcc, 0, v153\n"
    "v_mfma_f32_16x16x32_bf16 v[188:191], v[224:227], v[68:71], v[188:191]\n"
    "s_waitcnt lgkmcnt(7)\n"
    "ds_read_b64_tr_b16 v[222:223], v144 offset:47104\n"
    "v_mov_b32 v138, 0\n"
    "v_mfma_f32_16x16x32_bf16 v[188:191], v[232:235], v[100:103], v[188:191]\n"
    "s_lshr_b32 s70, s55, 4\n"
    "ds_read_b64_tr_b16 v[224:225], v144 offset:49152\n"
    "v_mov_b32 v139, 0\n"
    "v_mfma_f32_16x16x32_bf16 v[188:191], v[236:239], v[116:119], v[188:191]\n"
    "s_lshl_b32 s70, s70, 8\n"
    "ds_read_b64_tr_b16 v[226:227], v144 offset:51200\n"
    "v_mov_b32 v140, 0\n"
    "v_mfma_f32_16x16x32_bf16 v[184:187], v[236:239], v[112:115], v[184:187]\n"
    "s_and_b32 s71, s72, 1\n"
    "ds_read_b64_tr_b16 v[228:229], v144 offset:53248\n"
    "v_mov_b32 v141, 0\n"
    "v_mfma_f32_16x16x32_bf16 v[184:187], v[232:235], v[96:99], v[184:187]\n"
    "s_waitcnt lgkmcnt(7)\n"
    "ds_read_b64_tr_b16 v[230:231], v144 offset:55296\n"
    "v_cndmask_b32 v136, 0, v154, vcc\n"
    "v_mfma_f32_16x16x32_bf16 v[160:163], v[208:211], v[0:3], 0\n"
    "s_add_u32 m0, s48, 131072\n"
    "ds_read_b64_tr_b16 v[232:233], v144 offset:57344\n"
    "v_cndmask_b32 v137, v154, 0, vcc\n"
    "v_mfma_f32_16x16x32_bf16 v[160:163], v[212:215], v[16:19], v[160:163]\n"
    "s_sub_u32 s68, s68, 50048\n"
    "ds_read_b64_tr_b16 v[234:235], v144 offset:59392\n"
    "v_mov_b32 v142, v136\n"
    "v_mfma_f32_16x16x32_bf16 v[164:167], v[212:215], v[20:23], 0\n"
    "s_mul_i32 s71, s71, 114688\n"
    "ds_read_b64_tr_b16 v[236:237], v144 offset:61440\n"
    "buffer_load_dwordx4 v255, s[56:59], s68 offen lds\n"
    "v_mfma_f32_16x16x32_bf16 v[164:167], v[208:211], v[4:7], v[164:167]\n"
    "s_waitcnt lgkmcnt(7)\n"
    "ds_read_b64_tr_b16 v[238:239], v144 offset:63488\n"
    "v_mov_b32 v143, v137\n"
    "v_mfma_f32_16x16x32_bf16 v[164:167], v[216:219], v[36:39], v[164:167]\n"
    "s_add_u32 m0, s48, 132096\n"
    "ds_read_b64_tr_b16 v[208:209], v145 offset:32768\n"
    "v_mfma_f32_16x16x32_bf16 v[164:167], v[220:223], v[52:55], v[164:167]\n"
    "s_add_u32 s68, s68, 7168\n"
    "ds_read_b64_tr_b16 v[210:211], v145 offset:34816\n"
    "v_cvt_pk_bf16_f32 v200, v176, v177\n"
    "v_mfma_f32_16x16x32_bf16 v[160:163], v[220:223], v[48:51], v[160:163]\n"
    "s_add_u32 s73, s70, s71\n"
    "ds_read_b64_tr_b16 v[212:213], v145 offset:36864\n"
    "buffer_load_dwordx4 v255, s[56:59], s68 offen lds\n"
    "v_mfma_f32_16x16x32_bf16 v[160:163], v[216:219], v[32:35], v[160:163]\n"
    "s_waitcnt lgkmcnt(7)\n"
    "ds_read_b64_tr_b16 v[214:215], v145 offset:38912\n"
    "v_cvt_pk_bf16_f32 v201, v178, v179\n"
    "v_mfma_f32_16x16x32_bf16 v[160:163], v[224:227], v[64:67], v[160:163]\n"
    "s_add_u32 m0, s48, 133120\n"
    "ds_read_b64_tr_b16 v[216:217], v145 offset:40960\n"
    "v_cvt_pk_bf16_f32 v202, v184, v185\n"
    "v_mfma_f32_16x16x32_bf16 v[160:163], v[228:231], v[80:83], v[160:163]\n"
    "s_add_u32 s68, s68, 7168\n"
    "ds_read_b64_tr_b16 v[218:219], v145 offset:43008\n"
    "v_cvt_pk_bf16_f32 v203, v186, v187\n"
    "v_mfma_f32_16x16x32_bf16 v[164:167], v[228:231], v[84:87], v[164:167]\n"
    "s_lshl_b32 s54, s72, 12\n"
    "ds_read_b64_tr_b16 v[220:221], v145 offset:45056\n"
    "buffer_load_dwordx4 v255, s[56:59], s68 offen lds\n"
    "v_mfma_f32_16x16x32_bf16 v[164:167], v[224:227], v[68:71], v[164:167]\n"
    "s_waitcnt lgkmcnt(7)\n"
    "ds_read_b64_tr_b16 v[222:223], v145 offset:47104\n"
    "buffer_store_dwordx4 v[200:203], v132, s[64:67], s69 offen offset:1536 sc1\n"
    "v_mfma_f32_16x16x32_bf16 v[164:167], v[232:235], v[100:103], v[164:167]\n"
    "s_add_u32 m0, s48, 134144\n"
    "ds_read_b64_tr_b16 v[224:225], v145 offset:49152\n"
    "v_cvt_pk_bf16_f32 v204, v180, v181\n"
    "v_mfma_f32_16x16x32_bf16 v[164:167], v[236:239], v[116:119], v[164:167]\n"
    "s_add_u32 s68, s68, 7168\n"
    "ds_read_b64_tr_b16 v[226:227], v145 offset:51200\n"
    "v_cvt_pk_bf16_f32 v205, v182, v183\n"
    "v_mfma_f32_16x16x32_bf16 v[160:163], v[236:239], v[112:115], v[160:163]\n"
    "s_and_b32 s70, s72, 1\n"
    "ds_read_b64_tr_b16 v[228:229], v145 offset:53248\n"
    "buffer_load_dwordx4 v255, s[56:59], s68 offen lds\n"
    "v_mfma_f32_16x16x32_bf16 v[160:163], v[232:235], v[96:99], v[160:163]\n"
    "s_waitcnt lgkmcnt(7)\n"
    "ds_read_b64_tr_b16 v[230:231], v145 offset:55296\n"
    "v_cvt_pk_bf16_f32 v206, v188, v189\n"
    "v_mfma_f32_16x16x32_bf16 v[168:171], v[208:211], v[0:3], 0\n"
    "s_add_u32 m0, s48, 135168\n"
    "ds_read_b64_tr_b16 v[232:233], v145 offset:57344\n"
    "v_cvt_pk_bf16_f32 v207, v190, v191\n"
    "v_mfma_f32_16x16x32_bf16 v[168:171], v[212:215], v[16:19], v[168:171]\n"
    "s_add_u32 s68, s68, 7168\n"
    "ds_read_b64_tr_b16 v[234:235], v145 offset:59392\n"
    "buffer_store_dwordx4 v[204:207], v133, s[64:67], s69 offen offset:1536 sc1\n"
    "v_mfma_f32_16x16x32_bf16 v[172:175], v[212:215], v[20:23], 0\n"
    "s_lshl_b32 s70, s70, 1\n"
    "ds_read_b64_tr_b16 v[236:237], v145 offset:61440\n"
    "buffer_load_dwordx4 v255, s[56:59], s68 offen lds\n"
    "v_mfma_f32_16x16x32_bf16 v[172:175], v[208:211], v[4:7], v[172:175]\n"
    "s_waitcnt lgkmcnt(7)\n"
    "ds_read_b64_tr_b16 v[238:239], v145 offset:63488\n"
    "v_mfma_f32_16x16x32_bf16 v[172:175], v[216:219], v[36:39], v[172:175]\n"
    "s_add_u32 m0, s48, 136192\n"
    "ds_read_b64_tr_b16 v[208:209], v146 offset:32768\n"
    "v_mfma_f32_16x16x32_bf16 v[172:175], v[220:223], v[52:55], v[172:175]\n"
    "s_add_u32 s68, s68, 7168\n"
    "ds_read_b64_tr_b16 v[210:211], v146 offset:34816\n"
    "v_mfma_f32_16x16x32_bf16 v[168:171], v[220:223], v[48:51], v[168:171]\n"
    "s_lshr_b32 s71, s72, 1\n"
    "ds_read_b64_tr_b16 v[212:213], v146 offset:36864\n"
    "buffer_load_dwordx4 v255, s[56:59], s68 offen lds\n"
    "v_mfma_f32_16x16x32_bf16 v[168:171], v[216:219], v[32:35], v[168:171]\n"
    "s_waitcnt lgkmcnt(7)\n"
    "ds_read_b64_tr_b16 v[214:215], v146 offset:38912\n"
    "v_mfma_f32_16x16x32_bf16 v[168:171], v[224:227], v[64:67], v[168:171]\n"
    "s_add_u32 m0, s48, 137216\n"
    "ds_read_b64_tr_b16 v[216:217], v146 offset:40960\n"
    "v_mfma_f32_16x16x32_bf16 v[168:171], v[228:231], v[80:83], v[168:171]\n"
    "s_add_u32 s68, s68, 7168\n"
    "ds_read_b64_tr_b16 v[218:219], v146 offset:43008\n"
    "v_mfma_f32_16x16x32_bf16 v[172:175], v[228:231], v[84:87], v[172:175]\n"
    "s_add_u32 s70, s70, s71\n"
    "ds_read_b64_tr_b16 v[220:221], v146 offset:45056\n"
    "buffer_load_dwordx4 v255, s[56:59], s68 offen lds\n"
    "v_mfma_f32_16x16x32_bf16 v[172:175], v[224:227], v[68:71], v[172:175]\n"
    "s_waitcnt lgkmcnt(7)\n"
    "ds_read_b64_tr_b16 v[222:223], v146 offset:47104\n"
    "v_mfma_f32_16x16x32_bf16 v[172:175], v[232:235], v[100:103], v[172:175]\n"
    "s_add_u32 m0, s48, 138240\n"
    "ds_read_b64_tr_b16 v[224:225], v146 offset:49152\n"
    "v_mfma_f32_16x16x32_bf16 v[172:175], v[236:239], v[116:119], v[172:175]\n"
    "s_add_u32 s68, s68, 7168\n"
    "ds_read_b64_tr_b16 v[226:227], v146 offset:51200\n"
    "v_mfma_f32_16x16x32_bf16 v[168:171], v[236:239], v[112:115], v[168:171]\n"
    "s_lshl_b32 s71, s30, 2\n"
    "ds_read_b64_tr_b16 v[228:229], v146 offset:53248\n"
    "buffer_load_dwordx4 v255, s[56:59], s68 offen lds\n"
    "v_mfma_f32_16x16x32_bf16 v[168:171], v[232:235], v[96:99], v[168:171]\n"
    "s_waitcnt lgkmcnt(7)\n"
    "ds_read_b64_tr_b16 v[230:231], v146 offset:55296\n"
    "v_mfma_f32_16x16x32_bf16 v[176:179], v[208:211], v[0:3], 0\n"
    "s_add_u32 s70, s70, s71\n"
    "ds_read_b64_tr_b16 v[232:233], v146 offset:57344\n"
    "v_mfma_f32_16x16x32_bf16 v[176:179], v[212:215], v[16:19], v[176:179]\n"
    "s_lshl_b32 s70, s70, 9\n"
    "ds_read_b64_tr_b16 v[234:235], v146 offset:59392\n"
    "v_mfma_f32_16x16x32_bf16 v[180:183], v[212:215], v[20:23], 0\n"
    "s_and_b32 s71, %1, 15\n"
    "ds_read_b64_tr_b16 v[236:237], v146 offset:61440\n"
    "v_mfma_f32_16x16x32_bf16 v[180:183], v[208:211], v[4:7], v[180:183]\n"
    "s_waitcnt lgkmcnt(7)\n"
    "ds_read_b64_tr_b16 v[238:239], v146 offset:63488\n"
    "v_mfma_f32_16x16x32_bf16 v[180:183], v[216:219], v[36:39], v[180:183]\n"
    "s_lshl_b32 s71, s71, 5\n"
    "ds_read_b64_tr_b16 v[208:209], v147 offset:32768\n"
    "v_mfma_f32_16x16x32_bf16 v[180:183], v[220:223], v[52:55], v[180:183]\n"
    "s_add_u32 s70, s70, s71\n"
    "ds_read_b64_tr_b16 v[210:211], v147 offset:34816\n"
    "v_cvt_pk_bf16_f32 v192, v160, v161\n"
    "v_mfma_f32_16x16x32_bf16 v[176:179], v[220:223], v[48:51], v[176:179]\n"
    "s_add_u32 s40, s70, 77824\n"
    "ds_read_b64_tr_b16 v[212:213], v147 offset:36864\n"
    "v_cvt_pk_bf16_f32 v193, v162, v163\n"
    "v_mfma_f32_16x16x32_bf16 v[176:179], v[216:219], v[32:35], v[176:179]\n"
    "s_waitcnt lgkmcnt(7)\n"
    "ds_read_b64_tr_b16 v[214:215], v147 offset:38912\n"
    "v_cvt_pk_bf16_f32 v194, v168, v169\n"
    "v_mfma_f32_16x16x32_bf16 v[176:179], v[224:227], v[64:67], v[176:179]\n"
    "s_add_u32 s41, s70, 208896\n"
    "ds_read_b64_tr_b16 v[216:217], v147 offset:40960\n"
    "v_cvt_pk_bf16_f32 v195, v170, v171\n"
    "v_mfma_f32_16x16x32_bf16 v[176:179], v[228:231], v[80:83], v[176:179]\n"
    "s_lshr_b32 s70, %1, 2\n"
    "ds_read_b64_tr_b16 v[218:219], v147 offset:43008\n"
    "buffer_store_dwordx4 v[192:195], v132, s[64:67], s69 offen offset:2048 sc1\n"
    "v_mfma_f32_16x16x32_bf16 v[180:183], v[228:231], v[84:87], v[180:183]\n"
    "s_lshl_b32 s71, s70, 9\n"
    "ds_read_b64_tr_b16 v[220:221], v147 offset:45056\n"
    "v_cvt_pk_bf16_f32 v196, v164, v165\n"
    "v_mfma_f32_16x16x32_bf16 v[180:183], v[224:227], v[68:71], v[180:183]\n"
    "s_waitcnt lgkmcnt(7)\n"
    "ds_read_b64_tr_b16 v[222:223], v147 offset:47104\n"
    "v_cvt_pk_bf16_f32 v197, v166, v167\n"
    "v_mfma_f32_16x16x32_bf16 v[180:183], v[232:235], v[100:103], v[180:183]\n"
    "s_add_u32 s42, s71, 77824\n"
    "ds_read_b64_tr_b16 v[224:225], v147 offset:49152\n"
    "v_cvt_pk_bf16_f32 v198, v172, v173\n"
    "v_mfma_f32_16x16x32_bf16 v[180:183], v[236:239], v[116:119], v[180:183]\n"
    "s_add_u32 s43, s71, 208896\n"
    "ds_read_b64_tr_b16 v[226:227], v147 offset:51200\n"
    "v_cvt_pk_bf16_f32 v199, v174, v175\n"
    "v_mfma_f32_16x16x32_bf16 v[176:179], v[236:239], v[112:115], v[176:179]\n"
    "s_and_b32 s47, %1, 3\n"
    "ds_read_b64_tr_b16 v[228:229], v147 offset:53248\n"
    "buffer_store_dwordx4 v[196:199], v133, s[64:67], s69 offen offset:2048 sc1\n"
    "v_mfma_f32_16x16x32_bf16 v[176:179], v[232:235], v[96:99], v[176:179]\n"
    "s_waitcnt lgkmcnt(7)\n"
    "ds_read_b64_tr_b16 v[230:231], v147 offset:55296\n"
    "v_mfma_f32_16x16x32_bf16 v[184:187], v[208:211], v[0:3], 0\n"
    "s_lshl_b32 s47, s47, 2\n"
    "ds_read_b64_tr_b16 v[232:233], v147 offset:57344\n"
    "v_mfma_f32_16x16x32_bf16 v[184:187], v[212:215], v[16:19], v[184:187]\n"
    "s_add_u32 s47, s47, s72\n"
    "ds_read_b64_tr_b16 v[234:235], v147 offset:59392\n"
    "v_mfma_f32_16x16x32_bf16 v[188:191], v[212:215], v[20:23], 0\n"
    "s_waitcnt vmcnt(24)\n"
    "s_barrier\n"
    "v_mfma_f32_16x16x32_bf16 v[188:191], v[208:211], v[4:7], v[188:191]\n"
    "s_waitcnt lgkmcnt(6)\n"
    "ds_read_b64_tr_b16 v[236:237], v147 offset:61440\n"
    "ds_read_b64_tr_b16 v[238:239], v147 offset:63488\n"
    "v_mfma_f32_16x16x32_bf16 v[188:191], v[216:219], v[36:39], v[188:191]\n"
    "s_lshl_b32 s47, s47, 5\n"
    "ds_read_b64_tr_b16 v[208:209], v148 offset:0\n"
    "v_mfma_f32_16x16x32_bf16 v[188:191], v[220:223], v[52:55], v[188:191]\n"
    "s_add_u32 s47, s47, s71\n"
    "ds_read_b64_tr_b16 v[210:211], v148 offset:2048\n"
    "v_mfma_f32_16x16x32_bf16 v[184:187], v[220:223], v[48:51], v[184:187]\n"
    "s_add_u32 s47, s47, 339968\n"
    "ds_read_b64_tr_b16 v[212:213], v148 offset:4096\n"
    "v_mfma_f32_16x16x32_bf16 v[184:187], v[216:219], v[32:35], v[184:187]\n"
    "s_waitcnt lgkmcnt(7)\n"
    "ds_read_b64_tr_b16 v[214:215], v148 offset:6144\n"
    "v_mfma_f32_16x16x32_bf16 v[184:187], v[224:227], v[64:67], v[184:187]\n"
    "s_mul_i32 s70, s70, 28672\n"
    "ds_read_b64_tr_b16 v[216:217], v148 offset:8192\n"
    "v_mfma_f32_16x16x32_bf16 v[184:187], v[228:231], v[80:83], v[184:187]\n"
    "s_bfe_u32 s71, %1, 0x10001\n"
    "ds_read_b64_tr_b16 v[218:219], v148 offset:10240\n"
    "v_mfma_f32_16x16x32_bf16 v[188:191], v[228:231], v[84:87], v[188:191]\n"
    "s_lshl_b32 s71, s71, 10\n"
    "ds_read_b64_tr_b16 v[220:221], v148 offset:12288\n"
    "v_mfma_f32_16x16x32_bf16 v[188:191], v[224:227], v[68:71], v[188:191]\n"
    "s_waitcnt lgkmcnt(7)\n"
    "ds_read_b64_tr_b16 v[222:223], v148 offset:14336\n"
    "v_mfma_f32_16x16x32_bf16 v[188:191], v[232:235], v[100:103], v[188:191]\n"
    "s_add_u32 s70, s70, s71\n"
    "ds_read_b64_tr_b16 v[224:225], v148 offset:16384\n"
    "v_mfma_f32_16x16x32_bf16 v[188:191], v[236:239], v[116:119], v[188:191]\n"
    "s_and_b32 s71, %1, 1\n"
    "ds_read_b64_tr_b16 v[226:227], v148 offset:18432\n"
    "v_mfma_f32_16x16x32_bf16 v[184:187], v[236:239], v[112:115], v[184:187]\n"
    "s_lshl_b32 s71, s71, 7\n"
    "ds_read_b64_tr_b16 v[228:229], v148 offset:20480\n"
    "v_mfma_f32_16x16x32_bf16 v[184:187], v[232:235], v[96:99], v[184:187]\n"
    "s_waitcnt lgkmcnt(7)\n"
    "ds_read_b64_tr_b16 v[230:231], v148 offset:22528\n"
    "v_mfma_f32_16x16x32_bf16 v[160:163], v[208:211], v[0:3], 0\n"
    "s_add_u32 m0, s48, 0\n"
    "ds_read_b64_tr_b16 v[232:233], v148 offset:24576\n"
    "v_mfma_f32_16x16x32_bf16 v[160:163], v[212:215], v[16:19], v[160:163]\n"
    "s_sub_u32 s68, s68, 50048\n"
    "ds_read_b64_tr_b16 v[234:235], v148 offset:26624\n"
    "v_mfma_f32_16x16x32_bf16 v[164:167], v[212:215], v[20:23], 0\n"
    "s_add_u32 s70, s70, s71\n"
    "ds_read_b64_tr_b16 v[236:237], v148 offset:28672\n"
    "buffer_load_dwordx4 v255, s[56:59], s68 offen lds\n"
    "v_mfma_f32_16x16x32_bf16 v[164:167], v[208:211], v[4:7], v[164:167]\n"
    "s_waitcnt lgkmcnt(7)\n"
    "ds_read_b64_tr_b16 v[238:239], v148 offset:30720\n"
    "v_mfma_f32_16x16x32_bf16 v[164:167], v[216:219], v[36:39], v[164:167]\n"
    "s_add_u32 m0, s48, 1024\n"
    "ds_read_b64_tr_b16 v[208:209], v149 offset:0\n"
    "v_mfma_f32_16x16x32_bf16 v[164:167], v[220:223], v[52:55], v[164:167]\n"
    "s_add_u32 s68, s68, 7168\n"
    "ds_read_b64_tr_b16 v[210:211], v149 offset:2048\n"
    "v_cvt_pk_bf16_f32 v200, v176, v177\n"
    "v_mfma_f32_16x16x32_bf16 v[160:163], v[220:223], v[48:51], v[160:163]\n"
    "s_lshl_b32 s71, s72, 12\n"
    "ds_read_b64_tr_b16 v[212:213], v149 offset:4096\n"
    "buffer_load_dwordx4 v255, s[56:59], s68 offen lds\n"
    "v_mfma_f32_16x16x32_bf16 v[160:163], v[216:219], v[32:35], v[160:163]\n"
    "s_waitcnt lgkmcnt(7)\n"
    "ds_read_b64_tr_b16 v[214:215], v149 offset:6144\n"
    "v_cvt_pk_bf16_f32 v201, v178, v179\n"
    "v_mfma_f32_16x16x32_bf16 v[160:163], v[224:227], v[64:67], v[160:163]\n"
    "s_add_u32 m0, s48, 2048\n"
    "ds_read_b64_tr_b16 v[216:217], v149 offset:8192\n"
    "v_cvt_pk_bf16_f32 v202, v184, v185\n"
    "v_mfma_f32_16x16x32_bf16 v[160:163], v[228:231], v[80:83], v[160:163]\n"
    "s_add_u32 s68, s68, 7168\n"
    "ds_read_b64_tr_b16 v[218:219], v149 offset:10240\n"
    "v_cvt_pk_bf16_f32 v203, v186, v187\n"
    "v_mfma_f32_16x16x32_bf16 v[164:167], v[228:231], v[84:87], v[164:167]\n"
    "s_add_u32 s70, s70, s71\n"
    "ds_read_b64_tr_b16 v[220:221], v149 offset:12288\n"
    "buffer_load_dwordx4 v255, s[56:59], s68 offen lds\n"
    "v_mfma_f32_16x16x32_bf16 v[164:167], v[224:227], v[68:71], v[164:167]\n"
    "s_waitcnt lgkmcnt(7)\n"
    "ds_read_b64_tr_b16 v[222:223], v149 offset:14336\n"
    "buffer_store_dwordx4 v[200:203], v132, s[64:67], s69 offen offset:2560 sc1\n"
    "v_mfma_f32_16x16x32_bf16 v[164:167], v[232:235], v[100:103], v[164:167]\n"
    "s_add_u32 m0, s48, 3072\n"
    "ds_read_b64_tr_b16 v[224:225], v149 offset:16384\n"
    "v_cvt_pk_bf16_f32 v204, v180, v181\n"
    "v_mfma_f32_16x16x32_bf16 v[164:167], v[236:239], v[116:119], v[164:167]\n"
    "s_add_u32 s68, s68, 7168\n"
    "ds_read_b64_tr_b16 v[226:227], v149 offset:18432\n"
    "v_cvt_pk_bf16_f32 v205, v182, v183\n"
    "v_mfma_f32_16x16x32_bf16 v[160:163], v[236:239], v[112:115], v[160:163]\n"
    "s_add_u32 s46, s70, 2568192\n"
    "ds_read_b64_tr_b16 v[228:229], v149 offset:20480\n"
    "buffer_load_dwordx4 v255, s[56:59], s68 offen lds\n"
    "v_mfma_f32_16x16x32_bf16 v[160:163], v[232:235], v[96:99], v[160:163]\n"
    "s_waitcnt lgkmcnt(7)\n"
    "ds_read_b64_tr_b16 v[230:231], v149 offset:22528\n"
    "v_cvt_pk_bf16_f32 v206, v188, v189\n"
    "v_mfma_f32_16x16x32_bf16 v[168:171], v[208:211], v[0:3], 0\n"
    "s_add_u32 m0, s48, 4096\n"
    "ds_read_b64_tr_b16 v[232:233], v149 offset:24576\n"
    "v_cvt_pk_bf16_f32 v207, v190, v191\n"
    "v_mfma_f32_16x16x32_bf16 v[168:171], v[212:215], v[16:19], v[168:171]\n"
    "s_add_u32 s68, s68, 7168\n"
    "ds_read_b64_tr_b16 v[234:235], v149 offset:26624\n"
    "buffer_store_dwordx4 v[204:207], v133, s[64:67], s69 offen offset:2560 sc1\n"
    "v_mfma_f32_16x16x32_bf16 v[172:175], v[212:215], v[20:23], 0\n"
    "s_add_u32 s45, s70, 733184\n"
    "ds_read_b64_tr_b16 v[236:237], v149 offset:28672\n"
    "buffer_load_dwordx4 v255, s[56:59], s68 offen lds\n"
    "v_mfma_f32_16x16x32_bf16 v[172:175], v[208:211], v[4:7], v[172:175]\n"
    "s_waitcnt lgkmcnt(7)\n"
    "ds_read_b64_tr_b16 v[238:239], v149 offset:30720\n"
    "v_mfma_f32_16x16x32_bf16 v[172:175], v[216:219], v[36:39], v[172:175]\n"
    "s_add_u32 m0, s48, 5120\n"
    "ds_read_b64_tr_b16 v[208:209], v150 offset:0\n"
    "v_mfma_f32_16x16x32_bf16 v[172:175], v[220:223], v[52:55], v[172:175]\n"
    "s_add_u32 s68, s68, 7168\n"
    "ds_read_b64_tr_b16 v[210:211], v150 offset:2048\n"
    "v_mfma_f32_16x16x32_bf16 v[168:171], v[220:223], v[48:51], v[168:171]\n"
    "s_mul_i32 s44, %1, 114688\n"
    "ds_read_b64_tr_b16 v[212:213], v150 offset:4096\n"
    "buffer_load_dwordx4 v255, s[56:59], s68 offen lds\n"
    "v_mfma_f32_16x16x32_bf16 v[168:171], v[216:219], v[32:35], v[168:171]\n"
    "s_waitcnt lgkmcnt(7)\n"
    "ds_read_b64_tr_b16 v[214:215], v150 offset:6144\n"
    "v_mfma_f32_16x16x32_bf16 v[168:171], v[224:227], v[64:67], v[168:171]\n"
    "s_add_u32 m0, s48, 6144\n"
    "ds_read_b64_tr_b16 v[216:217], v150 offset:8192\n"
    "v_mfma_f32_16x16x32_bf16 v[168:171], v[228:231], v[80:83], v[168:171]\n"
    "s_add_u32 s68, s68, 7168\n"
    "ds_read_b64_tr_b16 v[218:219], v150 offset:10240\n"
    "v_mfma_f32_16x16x32_bf16 v[172:175], v[228:231], v[84:87], v[172:175]\n"
    "s_lshl_b32 s71, s72, 10\n"
    "ds_read_b64_tr_b16 v[220:221], v150 offset:12288\n"
    "buffer_load_dwordx4 v255, s[56:59], s68 offen lds\n"
    "v_mfma_f32_16x16x32_bf16 v[172:175], v[224:227], v[68:71], v[172:175]\n"
    "s_waitcnt lgkmcnt(7)\n"
    "ds_read_b64_tr_b16 v[222:223], v150 offset:14336\n"
    "v_mfma_f32_16x16x32_bf16 v[172:175], v[232:235], v[100:103], v[172:175]\n"
    "s_add_u32 m0, s48, 7168\n"
    "ds_read_b64_tr_b16 v[224:225], v150 offset:16384\n"
    "v_mfma_f32_16x16x32_bf16 v[172:175], v[236:239], v[116:119], v[172:175]\n"
    "s_add_u32 s68, s68, 7168\n"
    "ds_read_b64_tr_b16 v[226:227], v150 offset:18432\n"
    "v_mfma_f32_16x16x32_bf16 v[168:171], v[236:239], v[112:115], v[168:171]\n"
    "s_add_u32 s44, s44, s71\n"
    "ds_read_b64_tr_b16 v[228:229], v150 offset:20480\n"
    "buffer_load_dwordx4 v255, s[56:59], s68 offen lds\n"
    "v_mfma_f32_16x16x32_bf16 v[168:171], v[232:235], v[96:99], v[168:171]\n"
    "s_waitcnt lgkmcnt(7)\n"
    "ds_read_b64_tr_b16 v[230:231], v150 offset:22528\n"
    "v_mfma_f32_16x16x32_bf16 v[176:179], v[208:211], v[0:3], 0\n"
    "s_add_u32 s44, s44, 11743232\n"
    "ds_read_b64_tr_b16 v[232:233], v150 offset:24576\n"
    "v_mfma_f32_16x16x32_bf16 v[176:179], v[212:215], v[16:19], v[176:179]\n"
    "s_and_b32 s51, s55, 15\n"
    "ds_read_b64_tr_b16 v[234:235], v150 offset:26624\n"
    "v_mfma_f32_16x16x32_bf16 v[180:183], v[212:215], v[20:23], 0\n"
    "s_lshl_b32 s51, s51, 2\n"
    "ds_read_b64_tr_b16 v[236:237], v150 offset:28672\n"
    "v_mfma_f32_16x16x32_bf16 v[180:183], v[208:211], v[4:7], v[180:183]\n"
    "s_waitcnt lgkmcnt(7)\n"
    "ds_read_b64_tr_b16 v[238:239], v150 offset:30720\n"
    "v_mfma_f32_16x16x32_bf16 v[180:183], v[216:219], v[36:39], v[180:183]\n"
    "s_add_u32 s51, s51, s72\n"
    "ds_read_b64_tr_b16 v[208:209], v151 offset:0\n"
    "v_mfma_f32_16x16x32_bf16 v[180:183], v[220:223], v[52:55], v[180:183]\n"
    "s_lshl_b32 s70, s51, 9\n"
    "ds_read_b64_tr_b16 v[210:211], v151 offset:2048\n"
    "v_cvt_pk_bf16_f32 v192, v160, v161\n"
    "v_mfma_f32_16x16x32_bf16 v[176:179], v[220:223], v[48:51], v[176:179]\n"
    "s_add_u32 s62, s70, 339968\n"
    "ds_read_b64_tr_b16 v[212:213], v151 offset:4096\n"
    "v_cvt_pk_bf16_f32 v193, v162, v163\n"
    "v_mfma_f32_16x16x32_bf16 v[176:179], v[216:219], v[32:35], v[176:179]\n"
    "s_waitcnt lgkmcnt(7)\n"
    "ds_read_b64_tr_b16 v[214:215], v151 offset:6144\n"
    "v_cvt_pk_bf16_f32 v194, v168, v169\n"
    "v_mfma_f32_16x16x32_bf16 v[176:179], v[224:227], v[64:67], v[176:179]\n"
    "s_mul_i32 s63, s51, 28672\n"
    "ds_read_b64_tr_b16 v[216:217], v151 offset:8192\n"
    "v_cvt_pk_bf16_f32 v195, v170, v171\n"
    "v_mfma_f32_16x16x32_bf16 v[176:179], v[228:231], v[80:83], v[176:179]\n"
    "s_add_u32 s63, s63, 733184\n"
    "ds_read_b64_tr_b16 v[218:219], v151 offset:10240\n"
    "buffer_store_dwordx4 v[192:195], v132, s[64:67], s69 offen offset:3072 sc1\n"
    "v_mfma_f32_16x16x32_bf16 v[180:183], v[228:231], v[84:87], v[180:183]\n"
    "s_mov_b32 s61, 0x3c000000\n"
    "ds_read_b64_tr_b16 v[220:221], v151 offset:12288\n"
    "v_cvt_pk_bf16_f32 v196, v164, v165\n"
    "v_mfma_f32_16x16x32_bf16 v[180:183], v[224:227], v[68:71], v[180:183]\n"
    "s_waitcnt lgkmcnt(7)\n"
    "ds_read_b64_tr_b16 v[222:223], v151 offset:14336\n"
    "v_cvt_pk_bf16_f32 v197, v166, v167\n"
    "v_mfma_f32_16x16x32_bf16 v[180:183], v[232:235], v[100:103], v[180:183]\n"
    "s_nop 0\n"
    "ds_read_b64_tr_b16 v[224:225], v151 offset:16384\n"
    "v_cvt_pk_bf16_f32 v198, v172, v173\n"
    "v_mfma_f32_16x16x32_bf16 v[180:183], v[236:239], v[116:119], v[180:183]\n"
    "ds_read_b64_tr_b16 v[226:227], v151 offset:18432\n"
    "v_cvt_pk_bf16_f32 v199, v174, v175\n"
    "v_mfma_f32_16x16x32_bf16 v[176:179], v[236:239], v[112:115], v[176:179]\n"
    "ds_read_b64_tr_b16 v[228:229], v151 offset:20480\n"
    "buffer_store_dwordx4 v[196:199], v133, s[64:67], s69 offen offset:3072 sc1\n"
    "v_mfma_f32_16x16x32_bf16 v[176:179], v[232:235], v[96:99], v[176:179]\n"
    "s_waitcnt lgkmcnt(7)\n"
    "ds_read_b64_tr_b16 v[230:231], v151 offset:22528\n"
    "v_mov_b32 v246, 0\n"
    "v_mfma_f32_16x16x32_bf16 v[184:187], v[208:211], v[0:3], 0\n"
    "ds_read_b64_tr_b16 v[232:233], v151 offset:24576\n"
    "v_xor_b32 v244, s74, v9\n"
    "v_mfma_f32_16x16x32_bf16 v[184:187], v[212:215], v[16:19], v[184:187]\n"
    "ds_read_b64_tr_b16 v[234:235], v151 offset:26624\n"
    "v_xor_b32 v245, s74, v11\n"
    "v_mfma_f32_16x16x32_bf16 v[188:191], v[212:215], v[20:23], 0\n"
    "s_waitcnt vmcnt(26)\n"
    "s_barrier\n"
    "v_mfma_f32_16x16x32_bf16 v[188:191], v[208:211], v[4:7], v[188:191]\n"
    "s_waitcnt lgkmcnt(6)\n"
    "ds_read_b64_tr_b16 v[236:237], v151 offset:28672\n"
    "ds_read_b64_tr_b16 v[238:239], v151 offset:30720\n"
    "v_mfma_f32_16x16x32_bf16 v[188:191], v[216:219], v[36:39], v[188:191]\n"
    "ds_read_b64_tr_b16 v[208:209], v148 offset:32768\n"
    "v_or3_b32 v246, v246, v244, v245\n"
    "v_mfma_f32_16x16x32_bf16 v[188:191], v[220:223], v[52:55], v[188:191]\n"
    "ds_read_b64_tr_b16 v[210:211], v148 offset:34816\n"
    "v_and_b32 v9, 0xffff0000, v8\n"
    "v_mfma_f32_16x16x32_bf16 v[184:187], v[220:223], v[48:51], v[184:187]\n"
    "ds_read_b64_tr_b16 v[212:213], v148 offset:36864\n"
    "v_lshlrev_b32 v8, 16, v8\n"
    "v_mfma_f32_16x16x32_bf16 v[184:187], v[216:219], v[32:35], v[184:187]\n"
    "s_waitcnt lgkmcnt(7)\n"
    "ds_read_b64_tr_b16 v[214:215], v148 offset:38912\n"
    "v_and_b32 v11, 0xffff0000, v10\n"
    "v_mfma_f32_16x16x32_bf16 v[184:187], v[224:227], v[64:67], v[184:187]\n"
    "ds_read_b64_tr_b16 v[216:217], v148 offset:40960\n"
    "v_lshlrev_b32 v10, 16, v10\n"
    "v_mfma_f32_16x16x32_bf16 v[184:187], v[228:231], v[80:83], v[184:187]\n"
    "ds_read_b64_tr_b16 v[218:219], v148 offset:43008\n"
    "v_add_f32 v8, v242, v8\n"
    "v_mfma_f32_16x16x32_bf16 v[188:191], v[228:231], v[84:87], v[188:191]\n"
    "ds_read_b64_tr_b16 v[220:221], v148 offset:45056\n"
    "v_add_f32 v9, v242, v9\n"
    "v_mfma_f32_16x16x32_bf16 v[188:191], v[224:227], v[68:71], v[188:191]\n"
    "s_waitcnt lgkmcnt(7)\n"
    "ds_read_b64_tr_b16 v[222:223], v148 offset:47104\n"
    "v_add_f32 v10, v242, v10\n"
    "v_mfma_f32_16x16x32_bf16 v[188:191], v[232:235], v[100:103], v[188:191]\n"
    "ds_read_b64_tr_b16 v[224:225], v148 offset:49152\n"
    "v_add_f32 v11, v242, v11\n"
    "v_mfma_f32_16x16x32_bf16 v[188:191], v[236:239], v[116:119], v[188:191]\n"
    "ds_read_b64_tr_b16 v[226:227], v148 offset:51200\n"
    "v_cvt_pk_bf16_f32 v8, 0, v8\n"
    "v_mfma_f32_16x16x32_bf16 v[184:187], v[236:239], v[112:115], v[184:187]\n"
    "ds_read_b64_tr_b16 v[228:229], v148 offset:53248\n"
    "v_cvt_pk_bf16_f32 v9, 0, v9\n"
    "v_mfma_f32_16x16x32_bf16 v[184:187], v[232:235], v[96:99], v[184:187]\n"
    "s_waitcnt lgkmcnt(7)\n"
    "ds_read_b64_tr_b16 v[230:231], v148 offset:55296\n"
    "v_cvt_pk_bf16_f32 v10, 0, v10\n"
    "v_mfma_f32_16x16x32_bf16 v[160:163], v[208:211], v[0:3], 0\n"
    "s_cmp_lt_u32 s72, 2\n"
    "ds_read_b64_tr_b16 v[232:233], v148 offset:57344\n"
    "v_cvt_pk_bf16_f32 v11, 0, v11\n"
    "v_mfma_f32_16x16x32_bf16 v[160:163], v[212:215], v[16:19], v[160:163]\n"
    "s_cselect_b64 s[80:81], s[14:15], s[16:17]\n"
    "ds_read_b64_tr_b16 v[234:235], v148 offset:59392\n"
    "v_xor_b32 v244, s74, v13\n"
    "v_mfma_f32_16x16x32_bf16 v[164:167], v[212:215], v[20:23], 0\n"
    "ds_read_b64_tr_b16 v[236:237], v148 offset:61440\n"
    "v_xor_b32 v245, s74, v15\n"
    "v_mfma_f32_16x16x32_bf16 v[164:167], v[208:211], v[4:7], v[164:167]\n"
    "s_waitcnt lgkmcnt(7)\n"
    "ds_read_b64_tr_b16 v[238:239], v148 offset:63488\n"
    "v_or3_b32 v246, v246, v244, v245\n"
    "v_mfma_f32_16x16x32_bf16 v[164:167], v[216:219], v[36:39], v[164:167]\n"
    "s_cmp_eq_u32 s72, 3\n"
    "ds_read_b64_tr_b16 v[208:209], v149 offset:32768\n"
    "v_and_b32 v13, 0xffff0000, v12\n"
    "v_mfma_f32_16x16x32_bf16 v[164:167], v[220:223], v[52:55], v[164:167]\n"
    "s_cselect_b32 s60, 0, 1.0\n"
    "ds_read_b64_tr_b16 v[210:211], v149 offset:34816\n"
    "v_cvt_pk_bf16_f32 v200, v176, v177\n"
    "v_mfma_f32_16x16x32_bf16 v[160:163], v[220:223], v[48:51], v[160:163]\n"
    "s_cselect_b32 s61, 0x3c800000, s61\n"
    "ds_read_b64_tr_b16 v[212:213], v149 offset:36864\n"
    "v_cvt_pk_bf16_f32 v201, v178, v179\n"
    "v_mfma_f32_16x16x32_bf16 v[160:163], v[216:219], v[32:35], v[160:163]\n"
    "s_waitcnt lgkmcnt(7)\n"
    "ds_read_b64_tr_b16 v[214:215], v149 offset:38912\n"
    "v_cvt_pk_bf16_f32 v202, v184, v185\n"
    "v_mfma_f32_16x16x32_bf16 v[160:163], v[224:227], v[64:67], v[160:163]\n"
    "s_cmp_eq_u32 s72, 3\n"
    "ds_read_b64_tr_b16 v[216:217], v149 offset:40960\n"
    "v_cvt_pk_bf16_f32 v203, v186, v187\n"
    "v_mfma_f32_16x16x32_bf16 v[160:163], v[228:231], v[80:83], v[160:163]\n"
    "s_cselect_b32 s70, 2.0, 4096\n"
    "ds_read_b64_tr_b16 v[218:219], v149 offset:43008\n"
    "buffer_store_dwordx4 v[200:203], v132, s[64:67], s69 offen offset:3584 sc1\n"
    "v_mfma_f32_16x16x32_bf16 v[164:167], v[228:231], v[84:87], v[164:167]\n"
    "s_cselect_b32 s71, 2.0, 16384\n"
    "ds_read_b64_tr_b16 v[220:221], v149 offset:45056\n"
    "v_cvt_pk_bf16_f32 v204, v180, v181\n"
    "v_mfma_f32_16x16x32_bf16 v[164:167], v[224:227], v[68:71], v[164:167]\n"
    "s_waitcnt lgkmcnt(7)\n"
    "ds_read_b64_tr_b16 v[222:223], v149 offset:47104\n"
    "v_cvt_pk_bf16_f32 v205, v182, v183\n"
    "v_mfma_f32_16x16x32_bf16 v[164:167], v[232:235], v[100:103], v[164:167]\n"
    "ds_read_b64_tr_b16 v[224:225], v149 offset:49152\n"
    "v_cvt_pk_bf16_f32 v206, v188, v189\n"
    "v_mfma_f32_16x16x32_bf16 v[164:167], v[236:239], v[116:119], v[164:167]\n"
    "ds_read_b64_tr_b16 v[226:227], v149 offset:51200\n"
    "v_cvt_pk_bf16_f32 v207, v190, v191\n"
    "v_mfma_f32_16x16x32_bf16 v[160:163], v[236:239], v[112:115], v[160:163]\n"
    "ds_read_b64_tr_b16 v[228:229], v149 offset:53248\n"
    "buffer_store_dwordx4 v[204:207], v133, s[64:67], s69 offen offset:3584 sc1\n"
    "v_mfma_f32_16x16x32_bf16 v[160:163], v[232:235], v[96:99], v[160:163]\n"
    "s_waitcnt lgkmcnt(7)\n"
    "ds_read_b64_tr_b16 v[230:231], v149 offset:55296\n"
    "v_lshlrev_b32 v12, 16, v12\n"
    "v_mfma_f32_16x16x32_bf16 v[168:171], v[208:211], v[0:3], 0\n"
    "s_mov_b32 s69, 4096\n"
    "ds_read_b64_tr_b16 v[232:233], v149 offset:57344\n"
    "v_and_b32 v15, 0xffff0000, v14\n"
    "v_mfma_f32_16x16x32_bf16 v[168:171], v[212:215], v[16:19], v[168:171]\n"
    "ds_read_b64_tr_b16 v[234:235], v149 offset:59392\n"
    "v_lshlrev_b32 v14, 16, v14\n"
    "v_mfma_f32_16x16x32_bf16 v[172:175], v[212:215], v[20:23], 0\n"
    "ds_read_b64_tr_b16 v[236:237], v149 offset:61440\n"
    "v_add_f32 v12, v243, v12\n"
    "v_mfma_f32_16x16x32_bf16 v[172:175], v[208:211], v[4:7], v[172:175]\n"
    "s_waitcnt lgkmcnt(7)\n"
    "ds_read_b64_tr_b16 v[238:239], v149 offset:63488\n"
    "v_add_f32 v13, v243, v13\n"
    "v_mfma_f32_16x16x32_bf16 v[172:175], v[216:219], v[36:39], v[172:175]\n"
    "ds_read_b64_tr_b16 v[208:209], v150 offset:32768\n"
    "v_add_f32 v14, v243, v14\n"
    "v_mfma_f32_16x16x32_bf16 v[172:175], v[220:223], v[52:55], v[172:175]\n"
    "ds_read_b64_tr_b16 v[210:211], v150 offset:34816\n"
    "v_add_f32 v15, v243, v15\n"
    "v_mfma_f32_16x16x32_bf16 v[168:171], v[220:223], v[48:51], v[168:171]\n"
    "ds_read_b64_tr_b16 v[212:213], v150 offset:36864\n"
    "v_cvt_pk_bf16_f32 v12, 0, v12\n"
    "v_mfma_f32_16x16x32_bf16 v[168:171], v[216:219], v[32:35], v[168:171]\n"
    "s_waitcnt lgkmcnt(7)\n"
    "ds_read_b64_tr_b16 v[214:215], v150 offset:38912\n"
    "v_cvt_pk_bf16_f32 v13, 0, v13\n"
    "v_mfma_f32_16x16x32_bf16 v[168:171], v[224:227], v[64:67], v[168:171]\n"
    "ds_read_b64_tr_b16 v[216:217], v150 offset:40960\n"
    "v_cvt_pk_bf16_f32 v14, 0, v14\n"
    "v_mfma_f32_16x16x32_bf16 v[168:171], v[228:231], v[80:83], v[168:171]\n"
    "ds_read_b64_tr_b16 v[218:219], v150 offset:43008\n"
    "v_cvt_pk_bf16_f32 v15, 0, v15\n"
    "v_mfma_f32_16x16x32_bf16 v[172:175], v[228:231], v[84:87], v[172:175]\n"
    "ds_read_b64_tr_b16 v[220:221], v150 offset:45056\n"
    "v_add_u32 v135, s70, v254\n"
    "v_mfma_f32_16x16x32_bf16 v[172:175], v[224:227], v[68:71], v[172:175]\n"
    "s_waitcnt lgkmcnt(7)\n"
    "ds_read_b64_tr_b16 v[222:223], v150 offset:47104\n"
    "v_add_u32 v131, s71, v130\n"
    "v_mfma_f32_16x16x32_bf16 v[172:175], v[232:235], v[100:103], v[172:175]\n"
    "ds_read_b64_tr_b16 v[224:225], v150 offset:49152\n"
    "v_cmp_ne_u32 s[56:57], 0, v246\n"
    "v_mfma_f32_16x16x32_bf16 v[172:175], v[236:239], v[116:119], v[172:175]\n"
    "ds_read_b64_tr_b16 v[226:227], v150 offset:51200\n"
    "v_mfma_f32_16x16x32_bf16 v[168:171], v[236:239], v[112:115], v[168:171]\n"
    "ds_read_b64_tr_b16 v[228:229], v150 offset:53248\n"
    "v_mfma_f32_16x16x32_bf16 v[168:171], v[232:235], v[96:99], v[168:171]\n"
    "s_waitcnt lgkmcnt(7)\n"
    "ds_read_b64_tr_b16 v[230:231], v150 offset:55296\n"
    "v_mfma_f32_16x16x32_bf16 v[176:179], v[208:211], v[0:3], 0\n"
    "ds_read_b64_tr_b16 v[232:233], v150 offset:57344\n"
    "v_mfma_f32_16x16x32_bf16 v[176:179], v[212:215], v[16:19], v[176:179]\n"
    "ds_read_b64_tr_b16 v[234:235], v150 offset:59392\n"
    "v_mfma_f32_16x16x32_bf16 v[180:183], v[212:215], v[20:23], 0\n"
    "ds_read_b64_tr_b16 v[236:237], v150 offset:61440\n"
    "v_mfma_f32_16x16x32_bf16 v[180:183], v[208:211], v[4:7], v[180:183]\n"
    "s_waitcnt lgkmcnt(7)\n"
    "ds_read_b64_tr_b16 v[238:239], v150 offset:63488\n"
    "v_mfma_f32_16x16x32_bf16 v[180:183], v[216:219], v[36:39], v[180:183]\n"
    "ds_read_b64_tr_b16 v[208:209], v151 offset:32768\n"
    "v_mfma_f32_16x16x32_bf16 v[180:183], v[220:223], v[52:55], v[180:183]\n"
    "ds_read_b64_tr_b16 v[210:211], v151 offset:34816\n"
    "v_cvt_pk_bf16_f32 v192, v160, v161\n"
    "v_mfma_f32_16x16x32_bf16 v[176:179], v[220:223], v[48:51], v[176:179]\n"
    "ds_read_b64_tr_b16 v[212:213], v151 offset:36864\n"
    "v_cvt_pk_bf16_f32 v193, v162, v163\n"
    "v_mfma_f32_16x16x32_bf16 v[176:179], v[216:219], v[32:35], v[176:179]\n"
    "s_waitcnt lgkmcnt(7)\n"
    "ds_read_b64_tr_b16 v[214:215], v151 offset:38912\n"
    "v_cvt_pk_bf16_f32 v194, v168, v169\n"
    "v_mfma_f32_16x16x32_bf16 v[176:179], v[224:227], v[64:67], v[176:179]\n"
    "ds_read_b64_tr_b16 v[216:217], v151 offset:40960\n"
    "v_cvt_pk_bf16_f32 v195, v170, v171\n"
    "v_mfma_f32_16x16x32_bf16 v[176:179], v[228:231], v[80:83], v[176:179]\n"
    "ds_read_b64_tr_b16 v[218:219], v151 offset:43008\n"
    "buffer_store_dwordx4 v[192:195], v132, s[64:67], s69 offen offset:0 sc1\n"
    "v_mfma_f32_16x16x32_bf16 v[180:183], v[228:231], v[84:87], v[180:183]\n"
    "ds_read_b64_tr_b16 v[220:221], v151 offset:45056\n"
    "v_cvt_pk_bf16_f32 v196, v164, v165\n"
    "v_mfma_f32_16x16x32_bf16 v[180:183], v[224:227], v[68:71], v[180:183]\n"
    "s_waitcnt lgkmcnt(7)\n"
    "ds_read_b64_tr_b16 v[222:223], v151 offset:47104\n"
    "v_cvt_pk_bf16_f32 v197, v166, v167\n"
    "v_mfma_f32_16x16x32_bf16 v[180:183], v[232:235], v[100:103], v[180:183]\n"
    "ds_read_b64_tr_b16 v[224:225], v151 offset:49152\n"
    "v_cvt_pk_bf16_f32 v198, v172, v173\n"
    "v_mfma_f32_16x16x32_bf16 v[180:183], v[236:239], v[116:119], v[180:183]\n"
    "ds_read_b64_tr_b16 v[226:227], v151 offset:51200\n"
    "v_cvt_pk_bf16_f32 v199, v174, v175\n"
    "v_mfma_f32_16x16x32_bf16 v[176:179], v[236:239], v[112:115], v[176:179]\n"
    "ds_read_b64_tr_b16 v[228:229], v151 offset:53248\n"
    "buffer_store_dwordx4 v[196:199], v133, s[64:67], s69 offen offset:0 sc1\n"
    "v_mfma_f32_16x16x32_bf16 v[176:179], v[232:235], v[96:99], v[176:179]\n"
    "s_waitcnt lgkmcnt(7)\n"
    "ds_read_b64_tr_b16 v[230:231], v151 offset:55296\n"
    "v_mfma_f32_16x16x32_bf16 v[184:187], v[208:211], v[0:3], 0\n"
    "ds_read_b64_tr_b16 v[232:233], v151 offset:57344\n"
    "v_mfma_f32_16x16x32_bf16 v[184:187], v[212:215], v[16:19], v[184:187]\n"
    "ds_read_b64_tr_b16 v[234:235], v151 offset:59392\n"
    "v_mfma_f32_16x16x32_bf16 v[188:191], v[212:215], v[20:23], 0\n"
    "s_waitcnt vmcnt(18)\n"
    "s_barrier\n"
    "v_mfma_f32_16x16x32_bf16 v[188:191], v[208:211], v[4:7], v[188:191]\n"
    "s_waitcnt lgkmcnt(6)\n"
    "ds_read_b64_tr_b16 v[236:237], v151 offset:61440\n"
    "ds_read_b64_tr_b16 v[238:239], v151 offset:63488\n"
    "v_mfma_f32_16x16x32_bf16 v[188:191], v[216:219], v[36:39], v[188:191]\n"
    "ds_read_b64_tr_b16 v[208:209], v156 offset:0\n"
    "v_mfma_f32_16x16x32_bf16 v[188:191], v[220:223], v[52:55], v[188:191]\n"
    "ds_read_b64_tr_b16 v[210:211], v156 offset:2048\n"
    "v_mfma_f32_16x16x32_bf16 v[184:187], v[220:223], v[48:51], v[184:187]\n"
    "ds_read_b64_tr_b16 v[212:213], v156 offset:4096\n"
    "v_mfma_f32_16x16x32_bf16 v[184:187], v[216:219], v[32:35], v[184:187]\n"
    "s_waitcnt lgkmcnt(7)\n"
    "ds_read_b64_tr_b16 v[214:215], v156 offset:6144\n"
    "v_mfma_f32_16x16x32_bf16 v[184:187], v[224:227], v[64:67], v[184:187]\n"
    "ds_read_b64_tr_b16 v[216:217], v156 offset:8192\n"
    "v_mfma_f32_16x16x32_bf16 v[184:187], v[228:231], v[80:83], v[184:187]\n"
    "ds_read_b64_tr_b16 v[218:219], v156 offset:10240\n"
    "v_mfma_f32_16x16x32_bf16 v[188:191], v[228:231], v[84:87], v[188:191]\n"
    "ds_read_b64_tr_b16 v[220:221], v156 offset:12288\n"
    "v_mfma_f32_16x16x32_bf16 v[188:191], v[224:227], v[68:71], v[188:191]\n"
    "s_waitcnt lgkmcnt(7)\n"
    "ds_read_b64_tr_b16 v[222:223], v156 offset:14336\n"
    "v_mfma_f32_16x16x32_bf16 v[188:191], v[232:235], v[100:103], v[188:191]\n"
    "ds_read_b64_tr_b16 v[224:225], v156 offset:16384\n"
    "v_mfma_f32_16x16x32_bf16 v[188:191], v[236:239], v[116:119], v[188:191]\n"
    "ds_read_b64_tr_b16 v[226:227], v156 offset:18432\n"
    "v_mfma_f32_16x16x32_bf16 v[184:187], v[236:239], v[112:115], v[184:187]\n"
    "ds_read_b64_tr_b16 v[228:229], v156 offset:20480\n"
    "v_mfma_f32_16x16x32_bf16 v[184:187], v[232:235], v[96:99], v[184:187]\n"
    "s_waitcnt lgkmcnt(7)\n"
    "ds_read_b64_tr_b16 v[230:231], v156 offset:22528\n"
    "v_mfma_f32_16x16x32_bf16 v[160:163], v[208:211], v[0:3], 0\n"
    "ds_read_b64_tr_b16 v[232:233], v156 offset:24576\n"
    "v_mfma_f32_16x16x32_bf16 v[160:163], v[212:215], v[16:19], v[160:163]\n"
    "ds_read_b64_tr_b16 v[234:235], v156 offset:26624\n"
    "v_mfma_f32_16x16x32_bf16 v[164:167], v[212:215], v[20:23], 0\n"
    "ds_read_b64_tr_b16 v[236:237], v156 offset:28672\n"
    "v_mfma_f32_16x16x32_bf16 v[164:167], v[208:211], v[4:7], v[164:167]\n"
    "s_waitcnt lgkmcnt(7)\n"
    "ds_read_b64_tr_b16 v[238:239], v156 offset:30720\n"
    "v_mfma_f32_16x16x32_bf16 v[164:167], v[216:219], v[36:39], v[164:167]\n"
    "ds_read_b64_tr_b16 v[208:209], v157 offset:0\n"
    "v_mfma_f32_16x16x32_bf16 v[164:167], v[220:223], v[52:55], v[164:167]\n"
    "ds_read_b64_tr_b16 v[210:211], v157 offset:2048\n"
    "v_cvt_pk_bf16_f32 v200, v176, v177\n"
    "v_mfma_f32_16x16x32_bf16 v[160:163], v[220:223], v[48:51], v[160:163]\n"
    "ds_read_b64_tr_b16 v[212:213], v157 offset:4096\n"
    "v_cvt_pk_bf16_f32 v201, v178, v179\n"
    "v_mfma_f32_16x16x32_bf16 v[160:163], v[216:219], v[32:35], v[160:163]\n"
    "s_waitcnt lgkmcnt(7)\n"
    "ds_read_b64_tr_b16 v[214:215], v157 offset:6144\n"
    "v_cvt_pk_bf16_f32 v202, v184, v185\n"
    "v_mfma_f32_16x16x32_bf16 v[160:163], v[224:227], v[64:67], v[160:163]\n"
    "ds_read_b64_tr_b16 v[216:217], v157 offset:8192\n"
    "v_cvt_pk_bf16_f32 v203, v186, v187\n"
    "v_mfma_f32_16x16x32_bf16 v[160:163], v[228:231], v[80:83], v[160:163]\n"
    "ds_read_b64_tr_b16 v[218:219], v157 offset:10240\n"
    "buffer_store_dwordx4 v[200:203], v132, s[64:67], s69 offen offset:512 sc1\n"
    "v_mfma_f32_16x16x32_bf16 v[164:167], v[228:231], v[84:87], v[164:167]\n"
    "ds_read_b64_tr_b16 v[220:221], v157 offset:12288\n"
    "v_cvt_pk_bf16_f32 v204, v180, v181\n"
    "v_mfma_f32_16x16x32_bf16 v[164:167], v[224:227], v[68:71], v[164:167]\n"
    "s_waitcnt lgkmcnt(7)\n"
    "ds_read_b64_tr_b16 v[222:223], v157 offset:14336\n"
    "v_cvt_pk_bf16_f32 v205, v182, v183\n"
    "v_mfma_f32_16x16x32_bf16 v[164:167], v[232:235], v[100:103], v[164:167]\n"
    "ds_read_b64_tr_b16 v[224:225], v157 offset:16384\n"
    "v_cvt_pk_bf16_f32 v206, v188, v189\n"
    "v_mfma_f32_16x16x32_bf16 v[164:167], v[236:239], v[116:119], v[164:167]\n"
    "ds_read_b64_tr_b16 v[226:227], v157 offset:18432\n"
    "v_cvt_pk_bf16_f32 v207, v190, v191\n"
    "v_mfma_f32_16x16x32_bf16 v[160:163], v[236:239], v[112:115], v[160:163]\n"
    "ds_read_b64_tr_b16 v[228:229], v157 offset:20480\n"
    "buffer_store_dwordx4 v[204:207], v133, s[64:67], s69 offen offset:512 sc1\n"
    "v_mfma_f32_16x16x32_bf16 v[160:163], v[232:235], v[96:99], v[160:163]\n"
    "s_waitcnt lgkmcnt(7)\n"
    "ds_read_b64_tr_b16 v[230:231], v157 offset:22528\n"
    "v_mfma_f32_16x16x32_bf16 v[168:171], v[208:211], v[0:3], 0\n"
    "ds_read_b64_tr_b16 v[232:233], v157 offset:24576\n"
    "v_mfma_f32_16x16x32_bf16 v[168:171], v[212:215], v[16:19], v[168:171]\n"
    "ds_read_b64_tr_b16 v[234:235], v157 offset:26624\n"
    "v_mfma_f32_16x16x32_bf16 v[172:175], v[212:215], v[20:23], 0\n"
    "ds_read_b64_tr_b16 v[236:237], v157 offset:28672\n"
    "v_mfma_f32_16x16x32_bf16 v[172:175], v[208:211], v[4:7], v[172:175]\n"
    "s_waitcnt lgkmcnt(7)\n"
    "ds_read_b64_tr_b16 v[238:239], v157 offset:30720\n"
    "v_mfma_f32_16x16x32_bf16 v[172:175], v[216:219], v[36:39], v[172:175]\n"
    "ds_read_b64_tr_b16 v[208:209], v158 offset:0\n"
    "v_mfma_f32_16x16x32_bf16 v[172:175], v[220:223], v[52:55], v[172:175]\n"
    "ds_read_b64_tr_b16 v[210:211], v158 offset:2048\n"
    "v_mfma_f32_16x16x32_bf16 v[168:171], v[220:223], v[48:51], v[168:171]\n"
    "ds_read_b64_tr_b16 v[212:213], v158 offset:4096\n"
    "v_mfma_f32_16x16x32_bf16 v[168:171], v[216:219], v[32:35], v[168:171]\n"
    "s_waitcnt lgkmcnt(7)\n"
    "ds_read_b64_tr_b16 v[214:215], v158 offset:6144\n"
    "v_mfma_f32_16x16x32_bf16 v[168:171], v[224:227], v[64:67], v[168:171]\n"
    "ds_read_b64_tr_b16 v[216:217], v158 offset:8192\n"
    "v_mfma_f32_16x16x32_bf16 v[168:171], v[228:231], v[80:83], v[168:171]\n"
    "ds_read_b64_tr_b16 v[218:219], v158 offset:10240\n"
    "v_mfma_f32_16x16x32_bf16 v[172:175], v[228:231], v[84:87], v[172:175]\n"
    "ds_read_b64_tr_b16 v[220:221], v158 offset:12288\n"
    "v_mfma_f32_16x16x32_bf16 v[172:175], v[224:227], v[68:71], v[172:175]\n"
    "s_waitcnt lgkmcnt(7)\n"
    "ds_read_b64_tr_b16 v[222:223], v158 offset:14336\n"
    "v_mfma_f32_16x16x32_bf16 v[172:175], v[232:235], v[100:103], v[172:175]\n"
    "ds_read_b64_tr_b16 v[224:225], v158 offset:16384\n"
    "v_mfma_f32_16x16x32_bf16 v[172:175], v[236:239], v[116:119], v[172:175]\n"
    "ds_read_b64_tr_b16 v[226:227], v158 offset:18432\n"
    "v_mfma_f32_16x16x32_bf16 v[168:171], v[236:239], v[112:115], v[168:171]\n"
    "ds_read_b64_tr_b16 v[228:229], v158 offset:20480\n"
    "v_mfma_f32_16x16x32_bf16 v[168:171], v[232:235], v[96:99], v[168:171]\n"
    "s_waitcnt lgkmcnt(7)\n"
    "ds_read_b64_tr_b16 v[230:231], v158 offset:22528\n"
    "v_mfma_f32_16x16x32_bf16 v[176:179], v[208:211], v[0:3], 0\n"
    "ds_read_b64_tr_b16 v[232:233], v158 offset:24576\n"
    "v_mfma_f32_16x16x32_bf16 v[176:179], v[212:215], v[16:19], v[176:179]\n"
    "ds_read_b64_tr_b16 v[234:235], v158 offset:26624\n"
    "v_mfma_f32_16x16x32_bf16 v[180:183], v[212:215], v[20:23], 0\n"
    "s_waitcnt vmcnt(4)\n"
    "ds_read_b64_tr_b16 v[236:237], v158 offset:28672\n"
    "v_mfma_f32_16x16x32_bf16 v[180:183], v[208:211], v[4:7], v[180:183]\n"
    "s_waitcnt lgkmcnt(7)\n"
    "ds_read_b64_tr_b16 v[238:239], v158 offset:30720\n"
    "buffer_store_dwordx2 v[248:249], v250, s[76:79], s40 offen sc1\n"
    "v_mfma_f32_16x16x32_bf16 v[180:183], v[216:219], v[36:39], v[180:183]\n"
    "ds_read_b64_tr_b16 v[208:209], v159 offset:0\n"
    "v_mfma_f32_16x16x32_bf16 v[180:183], v[220:223], v[52:55], v[180:183]\n"
    "ds_read_b64_tr_b16 v[210:211], v159 offset:2048\n"
    "v_cvt_pk_bf16_f32 v192, v160, v161\n"
    "v_mfma_f32_16x16x32_bf16 v[176:179], v[220:223], v[48:51], v[176:179]\n"
    "ds_read_b64_tr_b16 v[212:213], v159 offset:4096\n"
    "v_cvt_pk_bf16_f32 v193, v162, v163\n"
    "v_mfma_f32_16x16x32_bf16 v[176:179], v[216:219], v[32:35], v[176:179]\n"
    "s_waitcnt lgkmcnt(7)\n"
    "ds_read_b64_tr_b16 v[214:215], v159 offset:6144\n"
    "v_cvt_pk_bf16_f32 v194, v168, v169\n"
    "v_mfma_f32_16x16x32_bf16 v[176:179], v[224:227], v[64:67], v[176:179]\n"
    "ds_read_b64_tr_b16 v[216:217], v159 offset:8192\n"
    "v_cvt_pk_bf16_f32 v195, v170, v171\n"
    "v_mfma_f32_16x16x32_bf16 v[176:179], v[228:231], v[80:83], v[176:179]\n"
    "ds_read_b64_tr_b16 v[218:219], v159 offset:10240\n"
    "buffer_store_dwordx4 v[192:195], v132, s[64:67], s69 offen offset:1024 sc1\n"
    "v_mfma_f32_16x16x32_bf16 v[180:183], v[228:231], v[84:87], v[180:183]\n"
    "ds_read_b64_tr_b16 v[220:221], v159 offset:12288\n"
    "v_cvt_pk_bf16_f32 v196, v164, v165\n"
    "v_mfma_f32_16x16x32_bf16 v[180:183], v[224:227], v[68:71], v[180:183]\n"
    "s_waitcnt lgkmcnt(7)\n"
    "ds_read_b64_tr_b16 v[222:223], v159 offset:14336\n"
    "v_cvt_pk_bf16_f32 v197, v166, v167\n"
    "v_mfma_f32_16x16x32_bf16 v[180:183], v[232:235], v[100:103], v[180:183]\n"
    "ds_read_b64_tr_b16 v[224:225], v159 offset:16384\n"
    "v_cvt_pk_bf16_f32 v198, v172, v173\n"
    "v_mfma_f32_16x16x32_bf16 v[180:183], v[236:239], v[116:119], v[180:183]\n"
    "ds_read_b64_tr_b16 v[226:227], v159 offset:18432\n"
    "v_cvt_pk_bf16_f32 v199, v174, v175\n"
    "v_mfma_f32_16x16x32_bf16 v[176:179], v[236:239], v[112:115], v[176:179]\n"
    "ds_read_b64_tr_b16 v[228:229], v159 offset:20480\n"
    "buffer_store_dwordx4 v[196:199], v133, s[64:67], s69 offen offset:1024 sc1\n"
    "v_mfma_f32_16x16x32_bf16 v[176:179], v[232:235], v[96:99], v[176:179]\n"
    "s_waitcnt lgkmcnt(7)\n"
    "ds_read_b64_tr_b16 v[230:231], v159 offset:22528\n"
    "v_mfma_f32_16x16x32_bf16 v[184:187], v[208:211], v[0:3], 0\n"
    "ds_read_b64_tr_b16 v[232:233], v159 offset:24576\n"
    "v_mfma_f32_16x16x32_bf16 v[184:187], v[212:215], v[16:19], v[184:187]\n"
    "ds_read_b64_tr_b16 v[234:235], v159 offset:26624\n"
    "v_mfma_f32_16x16x32_bf16 v[188:191], v[212:215], v[20:23], 0\n"
    "s_nop 0\n"
    "s_barrier\n"
    "v_mfma_f32_16x16x32_bf16 v[188:191], v[208:211], v[4:7], v[188:191]\n"
    "s_waitcnt lgkmcnt(6)\n"
    "ds_read_b64_tr_b16 v[236:237], v159 offset:28672\n"
    "ds_read_b64_tr_b16 v[238:239], v159 offset:30720\n"
    "v_mfma_f32_16x16x32_bf16 v[188:191], v[216:219], v[36:39], v[188:191]\n"
    "ds_read_b64_tr_b16 v[208:209], v144 offset:0\n"
    "v_mfma_f32_16x16x32_bf16 v[188:191], v[220:223], v[52:55], v[188:191]\n"
    "ds_read_b64_tr_b16 v[210:211], v144 offset:2048\n"
    "v_mfma_f32_16x16x32_bf16 v[184:187], v[220:223], v[48:51], v[184:187]\n"
    "ds_read_b64_tr_b16 v[212:213], v144 offset:4096\n"
    "v_mfma_f32_16x16x32_bf16 v[184:187], v[216:219], v[32:35], v[184:187]\n"
    "s_waitcnt lgkmcnt(7)\n"
    "ds_read_b64_tr_b16 v[214:215], v144 offset:6144\n"
    "v_mfma_f32_16x16x32_bf16 v[184:187], v[224:227], v[64:67], v[184:187]\n"
    "ds_read_b64_tr_b16 v[216:217], v144 offset:8192\n"
    "v_mfma_f32_16x16x32_bf16 v[184:187], v[228:231], v[80:83], v[184:187]\n"
    "ds_read_b64_tr_b16 v[218:219], v144 offset:10240\n"
    "v_mfma_f32_16x16x32_bf16 v[188:191], v[228:231], v[84:87], v[188:191]\n"
    "ds_read_b64_tr_b16 v[220:221], v144 offset:12288\n"
    "v_mfma_f32_16x16x32_bf16 v[188:191], v[224:227], v[68:71], v[188:191]\n"
    "s_waitcnt lgkmcnt(7)\n"
    "ds_read_b64_tr_b16 v[222:223], v144 offset:14336\n"
    "v_mfma_f32_16x16x32_bf16 v[188:191], v[232:235], v[100:103], v[188:191]\n"
    "ds_read_b64_tr_b16 v[224:225], v144 offset:16384\n"
    "v_mfma_f32_16x16x32_bf16 v[188:191], v[236:239], v[116:119], v[188:191]\n"
    "ds_read_b64_tr_b16 v[226:227], v144 offset:18432\n"
    "v_mfma_f32_16x16x32_bf16 v[184:187], v[236:239], v[112:115], v[184:187]\n"
    "ds_read_b64_tr_b16 v[228:229], v144 offset:20480\n"
    "v_mfma_f32_16x16x32_bf16 v[184:187], v[232:235], v[96:99], v[184:187]\n"
    "s_waitcnt lgkmcnt(7)\n"
    "ds_read_b64_tr_b16 v[230:231], v144 offset:22528\n"
    "v_mfma_f32_16x16x32_bf16 v[160:163], v[208:211], v[0:3], 0\n"
    "ds_read_b64_tr_b16 v[232:233], v144 offset:24576\n"
    "v_mfma_f32_16x16x32_bf16 v[160:163], v[212:215], v[16:19], v[160:163]\n"
    "ds_read_b64_tr_b16 v[234:235], v144 offset:26624\n"
    "v_mfma_f32_16x16x32_bf16 v[164:167], v[212:215], v[20:23], 0\n"
    "ds_read_b64_tr_b16 v[236:237], v144 offset:28672\n"
    "v_mfma_f32_16x16x32_bf16 v[164:167], v[208:211], v[4:7], v[164:167]\n"
    "s_waitcnt lgkmcnt(7)\n"
    "ds_read_b64_tr_b16 v[238:239], v144 offset:30720\n"
    "ds_write_b128 v40, v[8:11] offset:0\n"
    "v_mfma_f32_16x16x32_bf16 v[164:167], v[216:219], v[36:39], v[164:167]\n"
    "ds_read_b64_tr_b16 v[208:209], v145 offset:0\n"
    "v_mfma_f32_16x16x32_bf16 v[164:167], v[220:223], v[52:55], v[164:167]\n"
    "ds_read_b64_tr_b16 v[210:211], v145 offset:2048\n"
    "v_cvt_pk_bf16_f32 v200, v176, v177\n"
    "v_mfma_f32_16x16x32_bf16 v[160:163], v[220:223], v[48:51], v[160:163]\n"
    "ds_read_b64_tr_b16 v[212:213], v145 offset:4096\n"
    "v_cvt_pk_bf16_f32 v201, v178, v179\n"
    "v_mfma_f32_16x16x32_bf16 v[160:163], v[216:219], v[32:35], v[160:163]\n"
    "s_waitcnt lgkmcnt(8)\n"
    "ds_read_b64_tr_b16 v[214:215], v145 offset:6144\n"
    "v_cvt_pk_bf16_f32 v202, v184, v185\n"
    "v_mfma_f32_16x16x32_bf16 v[160:163], v[224:227], v[64:67], v[160:163]\n"
    "ds_read_b64_tr_b16 v[216:217], v145 offset:8192\n"
    "v_cvt_pk_bf16_f32 v203, v186, v187\n"
    "v_mfma_f32_16x16x32_bf16 v[160:163], v[228:231], v[80:83], v[160:163]\n"
    "ds_read_b64_tr_b16 v[218:219], v145 offset:10240\n"
    "buffer_store_dwordx4 v[200:203], v132, s[64:67], s69 offen offset:1536 sc1\n"
    "v_mfma_f32_16x16x32_bf16 v[164:167], v[228:231], v[84:87], v[164:167]\n"
    "ds_read_b64_tr_b16 v[220:221], v145 offset:12288\n"
    "v_cvt_pk_bf16_f32 v204, v180, v181\n"
    "v_mfma_f32_16x16x32_bf16 v[164:167], v[224:227], v[68:71], v[164:167]\n"
    "s_waitcnt lgkmcnt(8)\n"
    "ds_read_b64_tr_b16 v[222:223], v145 offset:14336\n"
    "v_cvt_pk_bf16_f32 v205, v182, v183\n"
    "v_mfma_f32_16x16x32_bf16 v[164:167], v[232:235], v[100:103], v[164:167]\n"
    "ds_read_b64_tr_b16 v[224:225], v145 offset:16384\n"
    "v_cvt_pk_bf16_f32 v206, v188, v189\n"
    "v_mfma_f32_16x16x32_bf16 v[164:167], v[236:239], v[116:119], v[164:167]\n"
    "ds_read_b64_tr_b16 v[226:227], v145 offset:18432\n"
    "v_cvt_pk_bf16_f32 v207, v190, v191\n"
    "v_mfma_f32_16x16x32_bf16 v[160:163], v[236:239], v[112:115], v[160:163]\n"
    "ds_read_b64_tr_b16 v[228:229], v145 offset:20480\n"
    "buffer_store_dwordx4 v[204:207], v133, s[64:67], s69 offen offset:1536 sc1\n"
    "v_mfma_f32_16x16x32_bf16 v[160:163], v[232:235], v[96:99], v[160:163]\n"
    "s_waitcnt lgkmcnt(7)\n"
    "ds_read_b64_tr_b16 v[230:231], v145 offset:22528\n"
    "ds_write_b128 v41, v[12:15] offset:0\n"
    "v_mfma_f32_16x16x32_bf16 v[168:171], v[208:211], v[0:3], 0\n"
    "ds_read_b64_tr_b16 v[232:233], v145 offset:24576\n"
    "v_mfma_f32_16x16x32_bf16 v[168:171], v[212:215], v[16:19], v[168:171]\n"
    "ds_read_b64_tr_b16 v[234:235], v145 offset:26624\n"
    "v_mfma_f32_16x16x32_bf16 v[172:175], v[212:215], v[20:23], 0\n"
    "ds_read_b64_tr_b16 v[236:237], v145 offset:28672\n"
    "ds_write_b128 v40, v[24:27] offset:7168\n"
    "v_mfma_f32_16x16x32_bf16 v[172:175], v[208:211], v[4:7], v[172:175]\n"
    "s_waitcnt lgkmcnt(9)\n"
    "ds_read_b64_tr_b16 v[238:239], v145 offset:30720\n"
    "v_mfma_f32_16x16x32_bf16 v[172:175], v[216:219], v[36:39], v[172:175]\n"
    "ds_read_b64_tr_b16 v[208:209], v146 offset:0\n"
    "v_mfma_f32_16x16x32_bf16 v[172:175], v[220:223], v[52:55], v[172:175]\n"
    "ds_read_b64_tr_b16 v[210:211], v146 offset:2048\n"
    "ds_write_b128 v41, v[28:31] offset:7168\n"
    "v_mfma_f32_16x16x32_bf16 v[168:171], v[220:223], v[48:51], v[168:171]\n"
    "ds_read_b64_tr_b16 v[212:213], v146 offset:4096\n"
    "v_mfma_f32_16x16x32_bf16 v[168:171], v[216:219], v[32:35], v[168:171]\n"
    "s_waitcnt lgkmcnt(10)\n"
    "ds_read_b64_tr_b16 v[214:215], v146 offset:6144\n"
    "v_mfma_f32_16x16x32_bf16 v[168:171], v[224:227], v[64:67], v[168:171]\n"
    "ds_read_b64_tr_b16 v[216:217], v146 offset:8192\n"
    "v_mfma_f32_16x16x32_bf16 v[168:171], v[228:231], v[80:83], v[168:171]\n"
    "ds_read_b64_tr_b16 v[218:219], v146 offset:10240\n"
    "v_mfma_f32_16x16x32_bf16 v[172:175], v[228:231], v[84:87], v[172:175]\n"
    "ds_read_b64_tr_b16 v[220:221], v146 offset:12288\n"
    "buffer_load_dwordx2 v[240:241], v251, s[76:79], s42 offen sc1\n"
    "v_mfma_f32_16x16x32_bf16 v[172:175], v[224:227], v[68:71], v[172:175]\n"
    "s_waitcnt lgkmcnt(8)\n"
    "ds_read_b64_tr_b16 v[222:223], v146 offset:14336\n"
    "v_mfma_f32_16x16x32_bf16 v[172:175], v[232:235], v[100:103], v[172:175]\n"
    "ds_read_b64_tr_b16 v[224:225], v146 offset:16384\n"
    "v_mfma_f32_16x16x32_bf16 v[172:175], v[236:239], v[116:119], v[172:175]\n"
    "ds_read_b64_tr_b16 v[226:227], v146 offset:18432\n"
    "v_mfma_f32_16x16x32_bf16 v[168:171], v[236:239], v[112:115], v[168:171]\n"
    "ds_read_b64_tr_b16 v[228:229], v146 offset:20480\n"
    "v_mfma_f32_16x16x32_bf16 v[168:171], v[232:235], v[96:99], v[168:171]\n"
    "s_waitcnt lgkmcnt(7)\n"
    "ds_read_b64_tr_b16 v[230:231], v146 offset:22528\n"
    "v_mfma_f32_16x16x32_bf16 v[176:179], v[208:211], v[0:3], 0\n"
    "ds_read_b64_tr_b16 v[232:233], v146 offset:24576\n"
    "v_mfma_f32_16x16x32_bf16 v[176:179], v[212:215], v[16:19], v[176:179]\n"
    "ds_read_b64_tr_b16 v[234:235], v146 offset:26624\n"
    "v_mfma_f32_16x16x32_bf16 v[180:183], v[212:215], v[20:23], 0\n"
    "ds_read_b64_tr_b16 v[236:237], v146 offset:28672\n"
    "v_mfma_f32_16x16x32_bf16 v[180:183], v[208:211], v[4:7], v[180:183]\n"
    "s_waitcnt lgkmcnt(7)\n"
    "ds_read_b64_tr_b16 v[238:239], v146 offset:30720\n"
    "v_mfma_f32_16x16x32_bf16 v[180:183], v[216:219], v[36:39], v[180:183]\n"
    "ds_read_b64_tr_b16 v[208:209], v147 offset:0\n"
    "v_mfma_f32_16x16x32_bf16 v[180:183], v[220:223], v[52:55], v[180:183]\n"
    "ds_read_b64_tr_b16 v[210:211], v147 offset:2048\n"
    "v_cvt_pk_bf16_f32 v192, v160, v161\n"
    "v_mfma_f32_16x16x32_bf16 v[176:179], v[220:223], v[48:51], v[176:179]\n"
    "ds_read_b64_tr_b16 v[212:213], v147 offset:4096\n"
    "v_cvt_pk_bf16_f32 v193, v162, v163\n"
    "v_mfma_f32_16x16x32_bf16 v[176:179], v[216:219], v[32:35], v[176:179]\n"
    "s_waitcnt lgkmcnt(7)\n"
    "ds_read_b64_tr_b16 v[214:215], v147 offset:6144\n"
    "v_cvt_pk_bf16_f32 v194, v168, v169\n"
    "v_mfma_f32_16x16x32_bf16 v[176:179], v[224:227], v[64:67], v[176:179]\n"
    "ds_read_b64_tr_b16 v[216:217], v147 offset:8192\n"
    "v_cvt_pk_bf16_f32 v195, v170, v171\n"
    "v_mfma_f32_16x16x32_bf16 v[176:179], v[228:231], v[80:83], v[176:179]\n"
    "ds_read_b64_tr_b16 v[218:219], v147 offset:10240\n"
    "buffer_store_dwordx4 v[192:195], v132, s[64:67], s69 offen offset:2048 sc1\n"
    "v_mfma_f32_16x16x32_bf16 v[180:183], v[228:231], v[84:87], v[180:183]\n"
    "ds_read_b64_tr_b16 v[220:221], v147 offset:12288\n"
    "v_cvt_pk_bf16_f32 v196, v164, v165\n"
    "v_mfma_f32_16x16x32_bf16 v[180:183], v[224:227], v[68:71], v[180:183]\n"
    "s_waitcnt lgkmcnt(7)\n"
    "ds_read_b64_tr_b16 v[222:223], v147 offset:14336\n"
    "v_cvt_pk_bf16_f32 v197, v166, v167\n"
    "v_mfma_f32_16x16x32_bf16 v[180:183], v[232:235], v[100:103], v[180:183]\n"
    "ds_read_b64_tr_b16 v[224:225], v147 offset:16384\n"
    "v_cvt_pk_bf16_f32 v198, v172, v173\n"
    "v_mfma_f32_16x16x32_bf16 v[180:183], v[236:239], v[116:119], v[180:183]\n"
    "ds_read_b64_tr_b16 v[226:227], v147 offset:18432\n"
    "v_cvt_pk_bf16_f32 v199, v174, v175\n"
    "v_mfma_f32_16x16x32_bf16 v[176:179], v[236:239], v[112:115], v[176:179]\n"
    "ds_read_b64_tr_b16 v[228:229], v147 offset:20480\n"
    "buffer_store_dwordx4 v[196:199], v133, s[64:67], s69 offen offset:2048 sc1\n"
    "v_mfma_f32_16x16x32_bf16 v[176:179], v[232:235], v[96:99], v[176:179]\n"
    "s_waitcnt lgkmcnt(7)\n"
    "ds_read_b64_tr_b16 v[230:231], v147 offset:22528\n"
    "v_mfma_f32_16x16x32_bf16 v[184:187], v[208:211], v[0:3], 0\n"
    "ds_read_b64_tr_b16 v[232:233], v147 offset:24576\n"
    "v_mfma_f32_16x16x32_bf16 v[184:187], v[212:215], v[16:19], v[184:187]\n"
    "ds_read_b64_tr_b16 v[234:235], v147 offset:26624\n"
    "v_mfma_f32_16x16x32_bf16 v[188:191], v[212:215], v[20:23], 0\n"
    "ds_read_b64_tr_b16 v[236:237], v147 offset:28672\n"
    "v_mfma_f32_16x16x32_bf16 v[188:191], v[208:211], v[4:7], v[188:191]\n"
    "s_waitcnt lgkmcnt(7)\n"
    "ds_read_b64_tr_b16 v[238:239], v147 offset:30720\n"
    "v_mfma_f32_16x16x32_bf16 v[188:191], v[216:219], v[36:39], v[188:191]\n"
    "v_mfma_f32_16x16x32_bf16 v[188:191], v[220:223], v[52:55], v[188:191]\n"
    "v_mfma_f32_16x16x32_bf16 v[184:187], v[220:223], v[48:51], v[184:187]\n"
    "v_mfma_f32_16x16x32_bf16 v[184:187], v[216:219], v[32:35], v[184:187]\n"
    "s_waitcnt lgkmcnt(4)\n"
    "v_mfma_f32_16x16x32_bf16 v[184:187], v[224:227], v[64:67], v[184:187]\n"
    "v_mfma_f32_16x16x32_bf16 v[184:187], v[228:231], v[80:83], v[184:187]\n"
    "v_mfma_f32_16x16x32_bf16 v[188:191], v[228:231], v[84:87], v[188:191]\n"
    "v_mfma_f32_16x16x32_bf16 v[188:191], v[224:227], v[68:71], v[188:191]\n"
    "s_waitcnt lgkmcnt(0)\n"
    "v_mfma_f32_16x16x32_bf16 v[188:191], v[232:235], v[100:103], v[188:191]\n"
    "v_mfma_f32_16x16x32_bf16 v[188:191], v[236:239], v[116:119], v[188:191]\n"
    "v_mfma_f32_16x16x32_bf16 v[184:187], v[236:239], v[112:115], v[184:187]\n"
    "v_mfma_f32_16x16x32_bf16 v[184:187], v[232:235], v[96:99], v[184:187]\n"
    "s_nop 7\n"
    "v_cvt_pk_bf16_f32 v200, v176, v177\n"
    "v_cvt_pk_bf16_f32 v201, v178, v179\n"
    "v_cvt_pk_bf16_f32 v202, v184, v185\n"
    "v_cvt_pk_bf16_f32 v203, v186, v187\n"
    "buffer_store_dwordx4 v[200:203], v132, s[64:67], s69 offen offset:2560 sc1\n"
    "v_cvt_pk_bf16_f32 v204, v180, v181\n"
    "v_cvt_pk_bf16_f32 v205, v182, v183\n"
    "v_cvt_pk_bf16_f32 v206, v188, v189\n"
    "v_cvt_pk_bf16_f32 v207, v190, v191\n"
    "buffer_store_dwordx4 v[204:207], v133, s[64:67], s69 offen offset:2560 sc1\n"
    "s_cmp_lg_u64 s[56:57], 0\n"
    "s_cbranch_scc1 GRE0_%=\n"
    "GOK1_%=:\n"
    "s_waitcnt vmcnt(4)\n"
    "v_cmp_ne_u64 vcc, s[74:75], v[240:241]\n"
    "s_barrier\n"
    "s_cbranch_vccnz SLOWA_%=\n"
    "RETA_%=:\n"
    "s_mov_b32 s49, s44\n"
    "buffer_load_dwordx4 a[0:3], v254, s[76:79], s49 offen sc1\n"
    "s_add_u32 m0, s54, 32768\n"
    "s_add_u32 s49, s49, 7168\n"
    "buffer_load_dwordx4 v134, s[80:83], s73 offen lds\n"
    "buffer_load_dwordx4 a[8:11], v254, s[76:79], s49 offen sc1\n"
    "s_add_u32 m0, s54, 33792\n"
    "s_add_u32 s73, s73, 28672\n"
    "s_add_u32 s49, s49, 7168\n"
    "buffer_load_dwordx4 v134, s[80:83], s73 offen lds\n"
    "buffer_load_dwordx4 a[16:19], v254, s[76:79], s49 offen sc1\n"
    "s_add_u32 m0, s54, 34816\n"
    "s_add_u32 s73, s73, 28672\n"
    "s_add_u32 s49, s49, 7168\n"
    "buffer_load_dwordx4 v134, s[80:83], s73 offen lds\n"
    "buffer_load_dwordx4 a[24:27], v254, s[76:79], s49 offen sc1\n"
    "s_add_u32 m0, s54, 35840\n"
    "s_add_u32 s73, s73, 28672\n"
    "s_add_u32 s49, s49, 7168\n"
    "buffer_load_dwordx4 v134, s[80:83], s73 offen lds\n"
    "buffer_load_dwordx4 a[32:35], v254, s[76:79], s49 offen sc1\n"
    "s_add_u32 m0, s54, 49152\n"
    "s_add_u32 s73, s73, 143360\n"
    "s_add_u32 s49, s49, 7168\n"
    "buffer_load_dwordx4 v134, s[80:83], s73 offen lds\n"
    "buffer_load_dwordx4 a[40:43], v254, s[76:79], s49 offen sc1\n"
    "s_waitcnt vmcnt(11)\n"
    "buffer_store_dwordx2 v[248:249], v250, s[76:79], s41 offen sc1\n"
    "s_add_u32 m0, s54, 50176\n"
    "s_add_u32 s73, s73, 28672\n"
    "s_add_u32 s49, s49, 7168\n"
    "buffer_load_dwordx4 v134, s[80:83], s73 offen lds\n"
    "buffer_load_dwordx4 a[48:51], v254, s[76:79], s49 offen sc1\n"
    "s_add_u32 m0, s54, 51200\n"
    "s_add_u32 s73, s73, 28672\n"
    "s_add_u32 s49, s49, 7168\n"
    "buffer_load_dwordx4 v134, s[80:83], s73 offen lds\n"
    "buffer_load_dwordx4 a[56:59], v254, s[76:79], s49 offen sc1\n"
    "s_add_u32 m0, s54, 52224\n"
    "s_add_u32 s73, s73, 28672\n"
    "s_add_u32 s49, s49, 7168\n"
    "buffer_load_dwordx4 v134, s[80:83], s73 offen lds\n"
    "buffer_load_dwordx4 a[64:67], v254, s[76:79], s49 offen sc1\n"
    "s_add_u32 m0, s54, 65536\n"
    "s_add_u32 s73, s73, 143360\n"
    "s_add_u32 s49, s49, 7168\n"
    "buffer_load_dwordx4 v134, s[80:83], s73 offen lds\n"
    "buffer_load_dwordx4 a[72:75], v254, s[76:79], s49 offen sc1\n"
    "s_add_u32 m0, s54, 66560\n"
    "s_add_u32 s73, s73, 28672\n"
    "s_add_u32 s49, s49, 7168\n"
    "buffer_load_dwordx4 v134, s[80:83], s73 offen lds\n"
    "buffer_load_dwordx4 a[80:83], v254, s[76:79], s49 offen sc1\n"
    "s_add_u32 m0, s54, 67584\n"
    "s_add_u32 s73, s73, 28672\n"
    "s_add_u32 s49, s49, 7168\n"
    "buffer_load_dwordx4 v134, s[80:83], s73 offen lds\n"
    "buffer_load_dwordx4 a[88:91], v254, s[76:79], s49 offen sc1\n"
    "s_add_u32 m0, s54, 68608\n"
    "s_add_u32 s73, s73, 28672\n"
    "s_add_u32 s49, s49, 7168\n"
    "buffer_load_dwordx4 v134, s[80:83], s73 offen lds\n"
    "buffer_load_dwordx4 a[96:99], v254, s[76:79], s49 offen sc1\n"
    "s_add_u32 m0, s54, 81920\n"
    "s_add_u32 s73, s73, 143360\n"
    "s_add_u32 s49, s49, 7168\n"
    "buffer_load_dwordx4 v134, s[80:83], s73 offen lds\n"
    "buffer_load_dwordx4 a[104:107], v254, s[76:79], s49 offen sc1\n"
    "s_add_u32 m0, s54, 82944\n"
    "s_add_u32 s73, s73, 28672\n"
    "s_add_u32 s49, s49, 7168\n"
    "buffer_load_dwordx4 v134, s[80:83], s73 offen lds\n"
    "buffer_load_dwordx4 a[112:115], v254, s[76:79], s49 offen sc1\n"
    "s_add_u32 m0, s54, 83968\n"
    "s_add_u32 s73, s73, 28672\n"
    "s_add_u32 s49, s49, 7168\n"
    "buffer_load_dwordx4 v134, s[80:83], s73 offen lds\n"
    "buffer_load_dwordx4 a[120:123], v254, s[76:79], s49 offen sc1\n"
    "s_add_u32 m0, s54, 84992\n"
    "s_add_u32 s73, s73, 28672\n"
    "buffer_load_dwordx4 v134, s[80:83], s73 offen lds\n"
    "buffer_load_dwordx2 v[242:243], v251, s[76:79], s43 offen sc1\n"
    "ds_read_b64 v[48:49], v129 offset:0\n"
    "ds_read_b64 v[50:51], v129 offset:32\n"
    "ds_read_b64 v[52:53], v129 offset:512\n"
    "ds_read_b64 v[54:55], v129 offset:544\n"
    "ds_read_b128 v[0:3], v128 offset:0\n"
    "ds_read_b128 v[4:7], v128 offset:64\n"
    "ds_read_b128 v[8:11], v128 offset:1792\n"
    "ds_read_b128 v[12:15], v128 offset:1856\n"
    "ds_read_b128 v[16:19], v128 offset:3584\n"
    "ds_read_b128 v[20:23], v128 offset:3648\n"
    "ds_read_b128 v[24:27], v128 offset:1024\n"
    "ds_read_b128 v[28:31], v128 offset:1088\n"
    "s_waitcnt lgkmcnt(8)\n"
    "ds_read_b128 v[32:35], v128 offset:2816\n"
    "ds_read_b128 v[36:39], v128 offset:2880\n"
    "ds_read_b128 v[40:43], v128 offset:4608\n"
    "ds_read_b128 v[44:47], v128 offset:4672\n"
    "s_lshr_b32 s70, %1, 5\n"
    "s_min_u32 s70, s70, 6\n"
    "s_lshl_b32 s70, s70, 8\n"
    "s_add_u32 s70, s70, 120064\n"
    "v_lshrrev_b32 v244, 8, v254\n"
    "v_lshl_add_u32 v244, v244, 4, s70\n"
    "s_waitcnt lgkmcnt(6)\n"
    "ds_read_b128 v[176:179], v244 offset:0\n"
    "ds_read_b128 v[180:183], v244 offset:64\n"
    "ds_read_b128 v[184:187], v244 offset:128\n"
    "ds_read_b128 v[188:191], v244 offset:192\n"
    "v_lshlrev_b32 v64, 16, v48\n"
    "v_and_b32 v65, 0xffff0000, v48\n"
    "v_lshlrev_b32 v66, 16, v49\n"
    "v_and_b32 v67, 0xffff0000, v49\n"
    "v_lshlrev_b32 v68, 16, v50\n"
    "v_and_b32 v69, 0xffff0000, v50\n"
    "v_lshlrev_b32 v70, 16, v51\n"
    "v_and_b32 v71, 0xffff0000, v51\n"
    "v_lshlrev_b32 v72, 16, v52\n"
    "v_and_b32 v73, 0xffff0000, v52\n"
    "v_lshlrev_b32 v74, 16, v53\n"
    "v_and_b32 v75, 0xffff0000, v53\n"
    "v_lshlrev_b32 v76, 16, v54\n"
    "v_and_b32 v77, 0xffff0000, v54\n"
    "v_lshlrev_b32 v78, 16, v55\n"
    "v_and_b32 v79, 0xffff0000, v55\n"
    "s_waitcnt lgkmcnt(0)\n"
    "s_waitcnt vmcnt(33)\n"
    "v_mfma_f32_16x16x32_bf16 v[160:163], v[136:139], a[0:3], 0\n"
    "v_mfma_f32_16x16x32_bf16 v[164:167], v[140:143], a[0:3], 0\n"
    "s_waitcnt vmcnt(31)\n"
    "v_mfma_f32_16x16x32_bf16 v[160:163], v[136:139], a[8:11], v[160:163]\n"
    "v_mfma_f32_16x16x32_bf16 v[164:167], v[140:143], a[8:11], v[164:167]\n"
    "s_waitcnt vmcnt(29)\n"
    "v_mfma_f32_16x16x32_bf16 v[160:163], v[136:139], a[16:19], v[160:163]\n"
    "v_mfma_f32_16x16x32_bf16 v[164:167], v[140:143], a[16:19], v[164:167]\n"
    "s_waitcnt vmcnt(27)\n"
    "v_mfma_f32_16x16x32_bf16 v[160:163], v[136:139], a[24:27], v[160:163]\n"
    "v_mfma_f32_16x16x32_bf16 v[164:167], v[140:143], a[24:27], v[164:167]\n"
    "s_waitcnt vmcnt(25)\n"
    "v_mfma_f32_16x16x32_bf16 v[160:163], v[136:139], a[32:35], v[160:163]\n"
    "v_mfma_f32_16x16x32_bf16 v[164:167], v[140:143], a[32:35], v[164:167]\n"
    "s_waitcnt vmcnt(23)\n"
    "v_mfma_f32_16x16x32_bf16 v[160:163], v[136:139], a[40:43], v[160:163]\n"
    "v_mfma_f32_16x16x32_bf16 v[164:167], v[140:143], a[40:43], v[164:167]\n"
    "s_waitcnt vmcnt(20)\n"
    "v_mfma_f32_16x16x32_bf16 v[160:163], v[136:139], a[48:51], v[160:163]\n"
    "v_mfma_f32_16x16x32_bf16 v[164:167], v[140:143], a[48:51], v[164:167]\n"
    "s_waitcnt vmcnt(18)\n"
    "v_mfma_f32_16x16x32_bf16 v[160:163], v[136:139], a[56:59], v[160:163]\n"
    "v_mfma_f32_16x16x32_bf16 v[164:167], v[140:143], a[56:59], v[164:167]\n"
    "s_waitcnt vmcnt(16)\n"
    "v_mfma_f32_16x16x32_bf16 v[160:163], v[136:139], a[64:67], v[160:163]\n"
    "v_mfma_f32_16x16x32_bf16 v[164:167], v[140:143], a[64:67], v[164:167]\n"
    "s_waitcnt vmcnt(14)\n"
    "v_mfma_f32_16x16x32_bf16 v[160:163], v[136:139], a[72:75], v[160:163]\n"
    "v_mfma_f32_16x16x32_bf16 v[164:167], v[140:143], a[72:75], v[164:167]\n"
    "s_waitcnt vmcnt(12)\n"
    "v_mfma_f32_16x16x32_bf16 v[160:163], v[136:139], a[80:83], v[160:163]\n"
    "v_mfma_f32_16x16x32_bf16 v[164:167], v[140:143], a[80:83], v[164:167]\n"
    "s_waitcnt vmcnt(10)\n"
    "v_mfma_f32_16x16x32_bf16 v[160:163], v[136:139], a[88:91], v[160:163]\n"
    "v_mfma_f32_16x16x32_bf16 v[164:167], v[140:143], a[88:91], v[164:167]\n"
    "s_waitcnt vmcnt(8)\n"
    "v_mfma_f32_16x16x32_bf16 v[160:163], v[136:139], a[96:99], v[160:163]\n"
    "v_mfma_f32_16x16x32_bf16 v[164:167], v[140:143], a[96:99], v[164:167]\n"
    "s_waitcnt vmcnt(6)\n"
    "v_mfma_f32_16x16x32_bf16 v[160:163], v[136:139], a[104:107], v[160:163]\n"
    "v_mfma_f32_16x16x32_bf16 v[164:167], v[140:143], a[104:107], v[164:167]\n"
    "s_waitcnt vmcnt(4)\n"
    "v_mfma_f32_16x16x32_bf16 v[160:163], v[136:139], a[112:115], v[160:163]\n"
    "v_mfma_f32_16x16x32_bf16 v[164:167], v[140:143], a[112:115], v[164:167]\n"
    "s_waitcnt vmcnt(2)\n"
    "v_mfma_f32_16x16x32_bf16 v[160:163], v[136:139], a[120:123], v[160:163]\n"
    "v_mfma_f32_16x16x32_bf16 v[164:167], v[140:143], a[120:123], v[164:167]\n"
    "s_waitcnt vmcnt(0)\n"
    "v_cmp_ne_u64 vcc, s[74:75], v[242:243]\n"
    "s_mov_b32 s53, 1048576\n"
    "s_cbranch_vccz PDB_%=\n"
    "PLB_%=:\n"
    "buffer_load_dwordx2 v[242:243], v251, s[76:79], s43 offen sc1\n"
    "s_sub_u32 s53, s53, 1\n"
    "s_waitcnt vmcnt(0)\n"
    "v_cmp_ne_u64 vcc, s[74:75], v[242:243]\n"
    "s_cbranch_scc1 PDB_%=\n"
    "s_cbranch_vccnz PLB_%=\n"
    "PDB_%=:\n"
    "s_mov_b32 s49, s44\n"
    "buffer_load_dwordx4 a[4:7], v135, s[76:79], s49 offen sc1\n"
    "s_add_u32 m0, s54, 98304\n"
    "s_add_u32 s73, s73, 143360\n"
    "s_add_u32 s49, s49, 7168\n"
    "buffer_load_dwordx4 v134, s[80:83], s73 offen lds\n"
    "buffer_load_dwordx4 a[12:15], v135, s[76:79], s49 offen sc1\n"
    "s_add_u32 m0, s54, 99328\n"
    "s_add_u32 s73, s73, 28672\n"
    "s_add_u32 s49, s49, 7168\n"
    "buffer_load_dwordx4 v134, s[80:83], s73 offen lds\n"
    "buffer_load_dwordx4 a[20:23], v135, s[76:79], s49 offen sc1\n"
    "s_add_u32 m0, s54, 100352\n"
    "s_add_u32 s73, s73, 28672\n"
    "s_add_u32 s49, s49, 7168\n"
    "buffer_load_dwordx4 v134, s[80:83], s73 offen lds\n"
    "buffer_load_dwordx4 a[28:31], v135, s[76:79], s49 offen sc1\n"
    "s_add_u32 m0, s54, 101376\n"
    "s_add_u32 s73, s73, 28672\n"
    "s_add_u32 s49, s49, 7168\n"
    "buffer_load_dwordx4 v134, s[80:83], s73 offen lds\n"
    "buffer_load_dwordx4 a[36:39], v135, s[76:79], s49 offen sc1\n"
    "s_add_u32 m0, s54, 131072\n"
    "s_add_u32 s73, s73, 143360\n"
    "s_add_u32 s49, s49, 7168\n"
    "buffer_load_dwordx4 v134, s[80:83], s73 offen lds\n"
    "buffer_load_dwordx4 a[44:47], v135, s[76:79], s49 offen sc1\n"
    "s_add_u32 m0, s54, 132096\n"
    "s_add_u32 s73, s73, 28672\n"
    "s_add_u32 s49, s49, 7168\n"
    "buffer_load_dwordx4 v134, s[80:83], s73 offen lds\n"
    "buffer_load_dwordx4 a[52:55], v135, s[76:79], s49 offen sc1\n"
    "s_add_u32 m0, s54, 133120\n"
    "s_add_u32 s73, s73, 28672\n"
    "s_add_u32 s49, s49, 7168\n"
    "buffer_load_dwordx4 v134, s[80:83], s73 offen lds\n"
    "buffer_load_dwordx4 a[60:63], v135, s[76:79], s49 offen sc1\n"
    "s_add_u32 m0, s54, 134144\n"
    "s_add_u32 s73, s73, 28672\n"
    "s_add_u32 s49, s49, 7168\n"
    "buffer_load_dwordx4 v134, s[80:83], s73 offen lds\n"
    "buffer_load_dwordx4 a[68:71], v135, s[76:79], s49 offen sc1\n"
    "s_add_u32 s49, s49, 7168\n"
    "buffer_load_dwordx4 a[76:79], v135, s[76:79], s49 offen sc1\n"
    "s_add_u32 s49, s49, 7168\n"
    "buffer_load_dwordx4 a[84:87], v135, s[76:79], s49 offen sc1\n"
    "s_add_u32 s49, s49, 7168\n"
    "buffer_load_dwordx4 a[92:95], v135, s[76:79], s49 offen sc1\n"
    "s_add_u32 s49, s49, 7168\n"
    "buffer_load_dwordx4 a[100:103], v135, s[76:79], s49 offen sc1\n"
    "s_add_u32 s49, s49, 7168\n"
    "buffer_load_dwordx4 a[108:111], v135, s[76:79], s49 offen sc1\n"
    "s_add_u32 s49, s49, 7168\n"
    "buffer_load_dwordx4 a[116:119], v135, s[76:79], s49 offen sc1\n"
    "s_add_u32 s49, s49, 7168\n"
    "buffer_load_dwordx4 a[124:127], v135, s[76:79], s49 offen sc1\n"
    "s_lshl_b32 s64, s72, 11\n"
    "s_lshr_b32 s71, s72, 1\n"
    "s_mul_i32 s52, s71, 802816\n"
    "s_and_b32 s70, s72, 1\n"
    "s_mul_i32 s70, s70, 14336\n"
    "s_add_u32 s52, s52, s70\n"
    "s_lshr_b32 s50, %1, 5\n"
    "s_lshl_b32 s70, s50, 7\n"
    "s_add_u32 s52, s52, s70\n"
    "s_lshl_b32 s65, s51, 4\n"
    "s_lshr_b32 s70, s55, 4\n"
    "s_add_u32 s65, s65, s70\n"
    "s_lshl_b32 s65, s65, 5\n"
    "s_add_u32 s66, s65, 602112\n"
    "s_add_u32 s65, s65, 471040\n"
    "s_mul_i32 s57, s51, 114688\n"
    "s_lshl_b32 s70, s70, 12\n"
    "s_add_u32 s57, s57, s70\n"
    "s_add_u32 s57, s57, 4403200\n"
    "s_add_u32 s58, s57, 57344\n"
    "s_and_b32 s51, %1, 7\n"
    "s_lshl_b32 s51, s51, 2\n"
    "s_bfe_u32 s70, %1, 0x20003\n"
    "s_add_u32 s51, s51, s70\n"
    "s_lshl_b32 s51, s51, 1\n"
    "s_and_b32 s70, s72, 1\n"
    "s_add_u32 s51, s51, s70\n"
    "s_lshl_b32 s67, s51, 4\n"
    "s_mul_i32 s70, s71, 7\n"
    "s_add_u32 s67, s67, s70\n"
    "s_lshl_b32 s67, s67, 5\n"
    "s_add_u32 s56, s67, 602112\n"
    "s_add_u32 s67, s67, 471040\n"
    "s_mul_i32 s59, s51, 114688\n"
    "s_mul_i32 s70, s71, 28672\n"
    "s_add_u32 s59, s59, s70\n"
    "s_add_u32 s59, s59, 4403200\n"
    "s_mul_i32 s48, s51, 28672\n"
    "s_lshl_b32 s70, s50, 12\n"
    "s_add_u32 s48, s48, s70\n"
    "s_lshl_b32 s70, s71, 10\n"
    "s_add_u32 s48, s48, s70\n"
    "s_add_u32 s48, s48, 2568192\n"
    "s_lshl_b32 s68, s51, 1\n"
    "s_add_u32 s68, s68, s71\n"
    "s_mul_i32 s68, s68, 14336\n"
    "s_lshl_b32 s70, s50, 7\n"
    "s_add_u32 s68, s68, s70\n"
    "s_mov_b64 s[88:89], s[24:25]\n"
    "s_mov_b32 s90, 1835008\n"
    "s_mov_b32 s91, 0x20000\n"
    "v_pk_fma_f32 v[64:65], v[0:1], v[160:161], v[64:65]\n"
    "v_pk_fma_f32 v[66:67], v[2:3], v[162:163], v[66:67]\n"
    "v_pk_fma_f32 v[68:69], v[4:5], v[164:165], v[68:69]\n"
    "v_pk_fma_f32 v[70:71], v[6:7], v[166:167], v[70:71]\n"
    "v_cvt_pk_bf16_f32 v192, v64, v65\n"
    "v_cvt_pk_bf16_f32 v193, v66, v67\n"
    "v_cvt_pk_bf16_f32 v194, v68, v69\n"
    "v_cvt_pk_bf16_f32 v195, v70, v71\n"
    "buffer_store_dwordx4 v[192:195], v130, s[76:79], s46 offen sc1\n"
    "v_lshlrev_b32 v64, 16, v192\n"
    "v_and_b32 v65, 0xffff0000, v192\n"
    "v_lshlrev_b32 v66, 16, v193\n"
    "v_and_b32 v67, 0xffff0000, v193\n"
    "v_lshlrev_b32 v68, 16, v194\n"
    "v_and_b32 v69, 0xffff0000, v194\n"
    "v_lshlrev_b32 v70, 16, v195\n"
    "v_and_b32 v71, 0xffff0000, v195\n"
    "v_pk_add_f32 v[200:201], v[64:65], v[66:67]\n"
    "v_pk_add_f32 v[204:205], v[68:69], v[70:71]\n"
    "v_pk_add_f32 v[200:201], v[200:201], v[204:205]\n"
    "v_add_f32 v200, v200, v201\n"
    "s_waitcnt vmcnt(24)\n"
    "v_mfma_f32_16x16x32_bf16 v[168:171], v[136:139], a[4:7], 0\n"
    "v_mfma_f32_16x16x32_bf16 v[172:175], v[140:143], a[4:7], 0\n"
    "s_waitcnt vmcnt(22)\n"
    "v_mfma_f32_16x16x32_bf16 v[168:171], v[136:139], a[12:15], v[168:171]\n"
    "v_mfma_f32_16x16x32_bf16 v[172:175], v[140:143], a[12:15], v[172:175]\n"
    "s_waitcnt vmcnt(20)\n"
    "v_mfma_f32_16x16x32_bf16 v[168:171], v[136:139], a[20:23], v[168:171]\n"
    "v_mfma_f32_16x16x32_bf16 v[172:175], v[140:143], a[20:23], v[172:175]\n"
    "s_waitcnt vmcnt(18)\n"
    "v_mfma_f32_16x16x32_bf16 v[168:171], v[136:139], a[28:31], v[168:171]\n"
    "v_mfma_f32_16x16x32_bf16 v[172:175], v[140:143], a[28:31], v[172:175]\n"
    "s_waitcnt vmcnt(16)\n"
    "v_mfma_f32_16x16x32_bf16 v[168:171], v[136:139], a[36:39], v[168:171]\n"
    "v_mfma_f32_16x16x32_bf16 v[172:175], v[140:143], a[36:39], v[172:175]\n"
    "s_waitcnt vmcnt(14)\n"
    "v_mfma_f32_16x16x32_bf16 v[168:171], v[136:139], a[44:47], v[168:171]\n"
    "v_mfma_f32_16x16x32_bf16 v[172:175], v[140:143], a[44:47], v[172:175]\n"
    "s_waitcnt vmcnt(12)\n"
    "v_mfma_f32_16x16x32_bf16 v[168:171], v[136:139], a[52:55], v[168:171]\n"
    "v_mfma_f32_16x16x32_bf16 v[172:175], v[140:143], a[52:55], v[172:175]\n"
    "s_waitcnt vmcnt(10)\n"
    "v_mfma_f32_16x16x32_bf16 v[168:171], v[136:139], a[60:63], v[168:171]\n"
    "v_mfma_f32_16x16x32_bf16 v[172:175], v[140:143], a[60:63], v[172:175]\n"
    "s_waitcnt vmcnt(8)\n"
    "v_mfma_f32_16x16x32_bf16 v[168:171], v[136:139], a[68:71], v[168:171]\n"
    "v_mfma_f32_16x16x32_bf16 v[172:175], v[140:143], a[68:71], v[172:175]\n"
    "s_waitcnt vmcnt(7)\n"
    "v_mfma_f32_16x16x32_bf16 v[168:171], v[136:139], a[76:79], v[168:171]\n"
    "v_mfma_f32_16x16x32_bf16 v[172:175], v[140:143], a[76:79], v[172:175]\n"
    "s_waitcnt vmcnt(6)\n"
    "v_mfma_f32_16x16x32_bf16 v[168:171], v[136:139], a[84:87], v[168:171]\n"
    "v_mfma_f32_16x16x32_bf16 v[172:175], v[140:143], a[84:87], v[172:175]\n"
    "s_waitcnt vmcnt(5)\n"
    "v_mfma_f32_16x16x32_bf16 v[168:171], v[136:139], a[92:95], v[168:171]\n"
    "v_mfma_f32_16x16x32_bf16 v[172:175], v[140:143], a[92:95], v[172:175]\n"
    "s_waitcnt vmcnt(4)\n"
    "v_mfma_f32_16x16x32_bf16 v[168:171], v[136:139], a[100:103], v[168:171]\n"
    "v_mfma_f32_16x16x32_bf16 v[172:175], v[140:143], a[100:103], v[172:175]\n"
    "s_waitcnt vmcnt(3)\n"
    "v_mfma_f32_16x16x32_bf16 v[168:171], v[136:139], a[108:111], v[168:171]\n"
    "v_mfma_f32_16x16x32_bf16 v[172:175], v[140:143], a[108:111], v[172:175]\n"
    "s_waitcnt vmcnt(2)\n"
    "v_mfma_f32_16x16x32_bf16 v[168:171], v[136:139], a[116:119], v[168:171]\n"
    "v_mfma_f32_16x16x32_bf16 v[172:175], v[140:143], a[116:119], v[172:175]\n"
    "s_waitcnt vmcnt(1)\n"
    "v_mfma_f32_16x16x32_bf16 v[168:171], v[136:139], a[124:127], v[168:171]\n"
    "v_mfma_f32_16x16x32_bf16 v[172:175], v[140:143], a[124:127], v[172:175]\n"
    "s_nop 6\n"
    "v_pk_fma_f32 v[72:73], v[24:25], v[168:169], v[72:73]\n"
    "v_pk_fma_f32 v[74:75], v[26:27], v[170:171], v[74:75]\n"
    "v_pk_fma_f32 v[76:77], v[28:29], v[172:173], v[76:77]\n"
    "v_pk_fma_f32 v[78:79], v[30:31], v[174:175], v[78:79]\n"
    "v_cvt_pk_bf16_f32 v196, v72, v73\n"
    "v_cvt_pk_bf16_f32 v197, v74, v75\n"
    "v_cvt_pk_bf16_f32 v198, v76, v77\n"
    "v_cvt_pk_bf16_f32 v199, v78, v79\n"
    "buffer_store_dwordx4 v[196:199], v131, s[76:79], s46 offen sc1\n"
    "v_lshlrev_b32 v72, 16, v196\n"
    "v_and_b32 v73, 0xffff0000, v196\n"
    "v_lshlrev_b32 v74, 16, v197\n"
    "v_and_b32 v75, 0xffff0000, v197\n"
    "v_lshlrev_b32 v76, 16, v198\n"
    "v_and_b32 v77, 0xffff0000, v198\n"
    "v_lshlrev_b32 v78, 16, v199\n"
    "v_and_b32 v79, 0xffff0000, v199\n"
    "v_pk_add_f32 v[202:203], v[72:73], v[74:75]\n"
    "v_pk_add_f32 v[204:205], v[76:77], v[78:79]\n"
    "v_pk_add_f32 v[202:203], v[202:203], v[204:205]\n"
    "v_add_f32 v202, v202, v203\n"
    "v_fmac_f32 v200, s60, v202\n"
    "s_nop 1\n"
    "v_add_f32_dpp v200, v200, v200 row_ror:8 row_mask:0xf bank_mask:0xf\n"
    "v_mov_b32 v201, v200\n"
    "s_nop 1\n"
    "v_permlane16_swap_b32 v200, v201\n"
    "s_nop 1\n"
    "v_add_f32 v200, v200, v201\n"
    "v_mov_b32 v201, v200\n"
    "s_nop 1\n"
    "v_permlane32_swap_b32 v200, v201\n"
    "s_nop 1\n"
    "v_add_f32 v200, v200, v201\n"
    "v_mul_f32 v200, s61, v200\n"
    "v_pk_add_f32 v[208:209], v[64:65], v[200:201] op_sel_hi:[1,0] neg_lo:[0,1] neg_hi:[0,1]\n"
    "v_pk_add_f32 v[210:211], v[66:67], v[200:201] op_sel_hi:[1,0] neg_lo:[0,1] neg_hi:[0,1]\n"
    "v_pk_add_f32 v[212:213], v[68:69], v[200:201] op_sel_hi:[1,0] neg_lo:[0,1] neg_hi:[0,1]\n"
    "v_pk_add_f32 v[214:215], v[70:71], v[200:201] op_sel_hi:[1,0] neg_lo:[0,1] neg_hi:[0,1]\n"
    "v_pk_add_f32 v[216:217], v[72:73], v[200:201] op_sel_hi:[1,0] neg_lo:[0,1] neg_hi:[0,1]\n"
    "v_pk_add_f32 v[218:219], v[74:75], v[200:201] op_sel_hi:[1,0] neg_lo:[0,1] neg_hi:[0,1]\n"
    "v_pk_add_f32 v[220:221], v[76:77], v[200:201] op_sel_hi:[1,0] neg_lo:[0,1] neg_hi:[0,1]\n"
    "v_pk_add_f32 v[222:223], v[78:79], v[200:201] op_sel_hi:[1,0] neg_lo:[0,1] neg_hi:[0,1]\n"
    "v_pk_mul_f32 v[202:203], v[208:209], v[208:209]\n"
    "v_pk_mul_f32 v[204:205], v[216:217], v[216:217]\n"
    "v_pk_fma_f32 v[202:203], v[210:211], v[210:211], v[202:203]\n"
    "v_pk_fma_f32 v[204:205], v[218:219], v[218:219], v[204:205]\n"
    "v_pk_fma_f32 v[202:203], v[212:213], v[212:213], v[202:203]\n"
    "v_pk_fma_f32 v[204:205], v[220:221], v[220:221], v[204:205]\n"
    "v_pk_fma_f32 v[202:203], v[214:215], v[214:215], v[202:203]\n"
    "v_pk_fma_f32 v[204:205], v[222:223], v[222:223], v[204:205]\n"
    "v_add_f32 v201, v202, v203\n"
    "v_add_f32 v204, v204, v205\n"
    "v_fmac_f32 v201, s60, v204\n"
    "s_nop 1\n"
    "v_add_f32_dpp v201, v201, v201 row_ror:8 row_mask:0xf bank_mask:0xf\n"
    "v_mov_b32 v202, v201\n"
    "s_nop 1\n"
    "v_permlane16_swap_b32 v201, v202\n"
    "s_nop 1\n"
    "v_add_f32 v201, v201, v202\n"
    "v_mov_b32 v202, v201\n"
    "s_nop 1\n"
    "v_permlane32_swap_b32 v201, v202\n"
    "s_nop 1\n"
    "v_add_f32 v201, v201, v202\n"
    "s_lshl_b32 s71, s72, 3\n"
    "v_add_u32 v202, s71, v247\n"
    "ds_write_b64 v202, v[200:201] offset:0\n"
    "s_waitcnt lgkmcnt(0)\n"
    "s_barrier\n"
    "ds_read_b128 v[208:211], v247 offset:0\n"
    "ds_read_b128 v[212:215], v247 offset:16\n"
    "v_mov_b32 v203, 0x3727c5ac\n"
    "s_waitcnt lgkmcnt(0)\n"
    "v_pk_add_f32 v[200:201], v[208:209], v[210:211]\n"
    "v_pk_add_f32 v[200:201], v[200:201], v[212:213]\n"
    "v_fmac_f32 v200, 0.5, v214\n"
    "v_add_f32 v201, v201, v215\n"
    "v_mul_f32 v200, 0x3e924925, v200\n"
    "v_sub_f32 v214, v214, v200\n"
    "v_sub_f32 v208, v208, v200\n"
    "v_sub_f32 v210, v210, v200\n"
    "v_sub_f32 v212, v212, v200\n"
    "v_mul_f32 v202, v214, v214\n"
    "v_mul_f32 v202, 0.5, v202\n"
    "v_fmac_f32 v202, v208, v208\n"
    "v_fmac_f32 v202, v210, v210\n"
    "v_fmac_f32 v202, v212, v212\n"
    "v_fmac_f32 v201, 0x43000000, v202\n"
    "v_fmac_f32 v203, 0x3b124925, v201\n"
    "v_rsq_f32 v202, v203\n"
    "s_nop 0\n"
    "v_mul_f32 v200, -v200, v202\n"
    "v_pk_fma_f32 v[64:65], v[64:65], v[202:203], v[200:201] op_sel_hi:[1,0,0]\n"
    "v_pk_fma_f32 v[66:67], v[66:67], v[202:203], v[200:201] op_sel_hi:[1,0,0]\n"
    "v_pk_fma_f32 v[68:69], v[68:69], v[202:203], v[200:201] op_sel_hi:[1,0,0]\n"
    "v_pk_fma_f32 v[70:71], v[70:71], v[202:203], v[200:201] op_sel_hi:[1,0,0]\n"
    "v_pk_fma_f32 v[64:65], v[64:65], v[16:17], v[8:9]\n"
    "v_pk_fma_f32 v[66:67], v[66:67], v[18:19], v[10:11]\n"
    "v_pk_fma_f32 v[68:69], v[68:69], v[20:21], v[12:13]\n"
    "v_pk_fma_f32 v[70:71], v[70:71], v[22:23], v[14:15]\n"
    "v_cvt_pk_bf16_f32 v208, v64, v65\n"
    "v_cvt_pk_bf16_f32 v209, v66, v67\n"
    "v_cvt_pk_bf16_f32 v210, v68, v69\n"
    "v_cvt_pk_bf16_f32 v211, v70, v71\n"
    "buffer_store_dwordx4 v[208:211], v130, s[76:79], s45 offen sc1\n"
    "v_pk_fma_f32 v[72:73], v[72:73], v[202:203], v[200:201] op_sel_hi:[1,0,0]\n"
    "v_pk_fma_f32 v[74:75], v[74:75], v[202:203], v[200:201] op_sel_hi:[1,0,0]\n"
    "v_pk_fma_f32 v[76:77], v[76:77], v[202:203], v[200:201] op_sel_hi:[1,0,0]\n"
    "v_pk_fma_f32 v[78:79], v[78:79], v[202:203], v[200:201] op_sel_hi:[1,0,0]\n"
    "v_pk_fma_f32 v[72:73], v[72:73], v[40:41], v[32:33]\n"
    "v_pk_fma_f32 v[74:75], v[74:75], v[42:43], v[34:35]\n"
    "v_pk_fma_f32 v[76:77], v[76:77], v[44:45], v[36:37]\n"
    "v_pk_fma_f32 v[78:79], v[78:79], v[46:47], v[38:39]\n"
    "v_cvt_pk_bf16_f32 v212, v72, v73\n"
    "v_cvt_pk_bf16_f32 v213, v74, v75\n"
    "v_cvt_pk_bf16_f32 v214, v76, v77\n"
    "v_cvt_pk_bf16_f32 v215, v78, v79\n"
    "buffer_store_dwordx4 v[212:215], v131, s[76:79], s45 offen sc1\n"
    "s_cmp_ge_u32 %1, 224\n"
    "s_waitcnt vmcnt(0)\n"
    "buffer_store_dwordx2 v[248:249], v250, s[76:79], s47 offen sc1\n"
    "s_cbranch_scc1 EPO_%=\n"
    "s_barrier\n"
    "s_add_u32 m0, s64, 147456\n"
    "s_nop 0\n"
    "buffer_load_dwordx4 v255, s[84:87], s52 offen lds\n"
    "s_add_u32 m0, s64, 148480\n"
    "s_add_u32 s52, s52, 7168\n"
    "buffer_load_dwordx4 v255, s[84:87], s52 offen lds\n"
    "s_add_u32 m0, s64, 155648\n"
    "s_add_u32 s52, s52, 21504\n"
    "buffer_load_dwordx4 v255, s[84:87], s52 offen lds\n"
    "s_add_u32 m0, s64, 156672\n"
    "s_add_u32 s52, s52, 7168\n"
    "buffer_load_dwordx4 v255, s[84:87], s52 offen lds\n"
    "s_add_u32 m0, s64, 0\n"
    "s_add_u32 s52, s52, 78848\n"
    "buffer_load_dwordx4 v255, s[84:87], s52 offen lds\n"
    "s_add_u32 m0, s64, 1024\n"
    "s_add_u32 s52, s52, 7168\n"
    "buffer_load_dwordx4 v255, s[84:87], s52 offen lds\n"
    "s_add_u32 m0, s64, 8192\n"
    "s_add_u32 s52, s52, 21504\n"
    "buffer_load_dwordx4 v255, s[84:87], s52 offen lds\n"
    "s_add_u32 m0, s64, 9216\n"
    "s_add_u32 s52, s52, 7168\n"
    "buffer_load_dwordx4 v255, s[84:87], s52 offen lds\n"
    "s_add_u32 m0, s64, 16384\n"
    "s_add_u32 s52, s52, 78848\n"
    "buffer_load_dwordx4 v255, s[84:87], s52 offen lds\n"
    "s_add_u32 m0, s64, 17408\n"
    "s_add_u32 s52, s52, 7168\n"
    "buffer_load_dwordx4 v255, s[84:87], s52 offen lds\n"
    "s_add_u32 m0, s64, 24576\n"
    "s_add_u32 s52, s52, 21504\n"
    "buffer_load_dwordx4 v255, s[84:87], s52 offen lds\n"
    "s_add_u32 m0, s64, 25600\n"
    "s_add_u32 s52, s52, 7168\n"
    "buffer_load_dwordx4 v255, s[84:87], s52 offen lds\n"
    "s_add_u32 m0, s64, 114688\n"
    "s_add_u32 s52, s52, 78848\n"
    "buffer_load_dwordx4 v255, s[84:87], s52 offen lds\n"
    "s_add_u32 m0, s64, 115712\n"
    "s_add_u32 s52, s52, 7168\n"
    "buffer_load_dwordx4 v255, s[84:87], s52 offen lds\n"
    "s_add_u32 m0, s64, 122880\n"
    "s_add_u32 s52, s52, 21504\n"
    "buffer_load_dwordx4 v255, s[84:87], s52 offen lds\n"
    "s_add_u32 m0, s64, 123904\n"
    "s_add_u32 s52, s52, 7168\n"
    "buffer_load_dwordx4 v255, s[84:87], s52 offen lds\n"
    "buffer_load_dwordx2 v[240:241], v251, s[76:79], s62 offen sc1\n"
    "s_mov_b32 s49, s63\n"
    "s_lshr_b32 s71, s72, 1\n"
    "s_lshl_b32 s70, s71, 12\n"
    "v_add_u32 v136, s70, v144\n"
    "v_add_u32 v137, s70, v145\n"
    "v_add_u32 v138, s70, v146\n"
    "v_add_u32 v139, s70, v147\n"
    "v_add_u32 v140, s70, v148\n"
    "v_add_u32 v141, s70, v149\n"
    "v_add_u32 v142, s70, v150\n"
    "v_add_u32 v143, s70, v151\n"
    "v_add_u32 v168, s70, v156\n"
    "v_add_u32 v169, s70, v157\n"
    "v_add_u32 v170, s70, v158\n"
    "v_add_u32 v171, s70, v159\n"
    "s_mul_i32 s70, s71, 57344\n"
    "v_add_u32 v128, s70, v254\n"
    "s_sub_u32 s70, 57344, s70\n"
    "v_add_u32 v129, s70, v254\n"
    "s_lshl_b32 s70, s72, 12\n"
    "s_add_u32 s70, s70, 98304\n"
    "v_add_u32 v131, s70, v254\n"
    "v_xor_b32 v132, 8192, v131\n"
    "v_lshrrev_b32 v130, 8, v254\n"
    "v_bfe_u32 v133, v254, 4, 4\n"
    "v_mul_u32_u24 v133, 896, v133\n"
    "v_lshl_add_u32 v133, v130, 4, v133\n"
    "s_mov_b32 s50, 0xbdd2d3e8\n"
    "v_mov_b32 v242, 0xc0135761\n"
    "v_mov_b32 v243, 0xc0135761\n"
    "v_mov_b32 v244, 0xbdd2d3e8\n"
    "v_mov_b32 v245, 0xbdd2d3e8\n"
    "v_mov_b32 v246, 1.0\n"
    "v_mov_b32 v247, 1.0\n"
    "ds_read_b64_tr_b16 v[208:209], v144 offset:32768\n"
    "ds_read_b64_tr_b16 v[210:211], v144 offset:34816\n"
    "ds_read_b64_tr_b16 v[212:213], v144 offset:36864\n"
    "ds_read_b64_tr_b16 v[214:215], v144 offset:38912\n"
    "ds_read_b64_tr_b16 v[216:217], v144 offset:40960\n"
    "ds_read_b64_tr_b16 v[218:219], v144 offset:43008\n"
    "ds_read_b64_tr_b16 v[220:221], v144 offset:45056\n"
    "ds_read_b64_tr_b16 v[222:223], v144 offset:47104\n"
    "ds_read_b64_tr_b16 v[224:225], v145 offset:32768\n"
    "ds_read_b64_tr_b16 v[226:227], v145 offset:34816\n"
    "ds_read_b64_tr_b16 v[228:229], v145 offset:36864\n"
    "ds_read_b64_tr_b16 v[230:231], v145 offset:38912\n"
    "s_waitcnt vmcnt(0)\n"
    "v_cmp_ne_u64 vcc, s[74:75], v[240:241]\n"
    "s_mov_b32 s53, 1048576\n"
    "s_cbranch_vccz PD2_%=\n"
    "PL2_%=:\n"
    "buffer_load_dwordx2 v[240:241], v251, s[76:79], s62 offen sc1\n"
    "s_sub_u32 s53, s53, 1\n"
    "s_waitcnt vmcnt(0)\n"
    "v_cmp_ne_u64 vcc, s[74:75], v[240:241]\n"
    "s_cbranch_scc1 PD2_%=\n"
    "s_cbranch_vccnz PL2_%=\n"
    "PD2_%=:\n"
    "buffer_load_dwordx4 a[0:3], v254, s[76:79], s49 offen offset:0 sc1\n"
    "buffer_load_dwordx4 a[4:7], v254, s[76:79], s49 offen offset:1024 sc1\n"
    "buffer_load_dwordx4 a[8:11], v254, s[76:79], s49 offen offset:2048 sc1\n"
    "buffer_load_dwordx4 a[12:15], v254, s[76:79], s49 offen offset:3072 sc1\n"
    "s_add_u32 s49, s49, 4096\n"
    "buffer_load_dwordx4 a[16:19], v254, s[76:79], s49 offen offset:0 sc1\n"
    "buffer_load_dwordx4 a[20:23], v254, s[76:79], s49 offen offset:1024 sc1\n"
    "buffer_load_dwordx4 a[24:27], v254, s[76:79], s49 offen offset:2048 sc1\n"
    "buffer_load_dwordx4 a[28:31], v254, s[76:79], s49 offen offset:3072 sc1\n"
    "s_add_u32 s49, s49, 4096\n"
    "buffer_load_dwordx4 a[32:35], v254, s[76:79], s49 offen offset:0 sc1\n"
    "buffer_load_dwordx4 a[36:39], v254, s[76:79], s49 offen offset:1024 sc1\n"
    "buffer_load_dwordx4 a[40:43], v254, s[76:79], s49 offen offset:2048 sc1\n"
    "buffer_load_dwordx4 a[44:47], v254, s[76:79], s49 offen offset:3072 sc1\n"
    "s_add_u32 s49, s49, 4096\n"
    "buffer_load_dwordx4 a[48:51], v254, s[76:79], s49 offen offset:0 sc1\n"
    "buffer_load_dwordx4 a[52:55], v254, s[76:79], s49 offen offset:1024 sc1\n"
    "buffer_load_dwordx4 a[56:59], v254, s[76:79], s49 offen offset:2048 sc1\n"
    "buffer_load_dwordx4 a[60:63], v254, s[76:79], s49 offen offset:3072 sc1\n"
    "s_add_u32 s49, s49, 4096\n"
    "v_min_u32 v251, 192, v251\n"
    "s_waitcnt vmcnt(12)\n"
    "s_waitcnt lgkmcnt(8)\n"
    "v_mfma_f32_16x16x32_bf16 v[0:3], v[208:211], a[0:3], 0\n"
    "ds_read_b64_tr_b16 v[232:233], v145 offset:40960\n"
    "v_mfma_f32_16x16x32_bf16 v[0:3], v[212:215], a[8:11], v[0:3]\n"
    "ds_read_b64_tr_b16 v[234:235], v145 offset:43008\n"
    "buffer_load_dwordx4 a[64:67], v254, s[76:79], s49 offen offset:0 sc1\n"
    "v_mfma_f32_16x16x32_bf16 v[4:7], v[212:215], a[12:15], 0\n"
    "ds_read_b64_tr_b16 v[236:237], v145 offset:45056\n"
    "v_mfma_f32_16x16x32_bf16 v[4:7], v[208:211], a[4:7], v[4:7]\n"
    "s_waitcnt lgkmcnt(7)\n"
    "ds_read_b64_tr_b16 v[238:239], v145 offset:47104\n"
    "v_mfma_f32_16x16x32_bf16 v[36:39], v[216:219], a[4:7], 0\n"
    "ds_read_b64_tr_b16 v[208:209], v146 offset:32768\n"
    "v_mfma_f32_16x16x32_bf16 v[36:39], v[220:223], a[12:15], v[36:39]\n"
    "s_nop 0\n"
    "s_barrier\n"
    "v_mfma_f32_16x16x32_bf16 v[32:35], v[220:223], a[8:11], 0\n"
    "ds_read_b64_tr_b16 v[210:211], v146 offset:34816\n"
    "ds_read_b64_tr_b16 v[212:213], v146 offset:36864\n"
    "v_mfma_f32_16x16x32_bf16 v[32:35], v[216:219], a[0:3], v[32:35]\n"
    "s_waitcnt lgkmcnt(7)\n"
    "ds_read_b64_tr_b16 v[214:215], v146 offset:38912\n"
    "buffer_load_dwordx4 a[68:71], v254, s[76:79], s49 offen offset:1024 sc1\n"
    "v_mfma_f32_16x16x32_bf16 v[8:11], v[224:227], a[0:3], 0\n"
    "ds_read_b64_tr_b16 v[216:217], v146 offset:40960\n"
    "ds_read_b64_tr_b16 a[128:129], v168 offset:16384\n"
    "v_mfma_f32_16x16x32_bf16 v[8:11], v[228:231], a[8:11], v[8:11]\n"
    "ds_read_b64_tr_b16 v[218:219], v146 offset:43008\n"
    "ds_read_b64_tr_b16 a[130:131], v168 offset:18432\n"
    "v_mfma_f32_16x16x32_bf16 v[12:15], v[228:231], a[12:15], 0\n"
    "ds_read_b64_tr_b16 v[220:221], v146 offset:45056\n"
    "ds_read_b64_tr_b16 a[132:133], v168 offset:24576\n"
    "v_mfma_f32_16x16x32_bf16 v[12:15], v[224:227], a[4:7], v[12:15]\n"
    "s_waitcnt lgkmcnt(10)\n"
    "ds_read_b64_tr_b16 v[222:223], v146 offset:47104\n"
    "buffer_load_dwordx4 a[72:75], v254, s[76:79], s49 offen offset:2048 sc1\n"
    "v_mfma_f32_16x16x32_bf16 v[44:47], v[232:235], a[4:7], 0\n"
    "ds_read_b64_tr_b16 v[224:225], v147 offset:32768\n"
    "ds_read_b64_tr_b16 a[134:135], v168 offset:26624\n"
    "v_mfma_f32_16x16x32_bf16 v[44:47], v[236:239], a[12:15], v[44:47]\n"
    "ds_read_b64_tr_b16 v[226:227], v147 offset:34816\n"
    "v_mfma_f32_16x16x32_bf16 v[40:43], v[236:239], a[8:11], 0\n"
    "ds_read_b64_tr_b16 v[228:229], v147 offset:36864\n"
    "ds_read_b64_tr_b16 a[136:137], v169 offset:16384\n"
    "v_mfma_f32_16x16x32_bf16 v[40:43], v[232:235], a[0:3], v[40:43]\n"
    "s_waitcnt lgkmcnt(12)\n"
    "ds_read_b64_tr_b16 v[230:231], v147 offset:38912\n"
    "buffer_load_dwordx4 a[76:79], v254, s[76:79], s49 offen offset:3072 sc1\n"
    "v_mfma_f32_16x16x32_bf16 v[16:19], v[208:211], a[0:3], 0\n"
    "s_add_u32 s49, s49, 4096\n"
    "ds_read_b64_tr_b16 v[232:233], v147 offset:40960\n"
    "ds_read_b64_tr_b16 a[138:139], v169 offset:18432\n"
    "v_mfma_f32_16x16x32_bf16 v[16:19], v[212:215], a[8:11], v[16:19]\n"
    "ds_read_b64_tr_b16 v[234:235], v147 offset:43008\n"
    "ds_read_b64_tr_b16 a[140:141], v169 offset:24576\n"
    "v_mfma_f32_16x16x32_bf16 v[20:23], v[212:215], a[12:15], 0\n"
    "ds_read_b64_tr_b16 v[236:237], v147 offset:45056\n"
    "buffer_load_dwordx4 a[80:83], v254, s[76:79], s49 offen offset:0 sc1\n"
    "v_mfma_f32_16x16x32_bf16 v[20:23], v[208:211], a[4:7], v[20:23]\n"
    "s_waitcnt lgkmcnt(11)\n"
    "ds_read_b64_tr_b16 v[238:239], v147 offset:47104\n"
    "ds_read_b64_tr_b16 a[142:143], v169 offset:26624\n"
    "v_mfma_f32_16x16x32_bf16 v[52:55], v[216:219], a[4:7], 0\n"
    "ds_read_b64_tr_b16 v[208:209], v144 offset:49152\n"
    "v_mfma_f32_16x16x32_bf16 v[52:55], v[220:223], a[12:15], v[52:55]\n"
    "ds_read_b64_tr_b16 v[210:211], v144 offset:51200\n"
    "ds_read_b64_tr_b16 a[144:145], v170 offset:16384\n"
    "v_mfma_f32_16x16x32_bf16 v[48:51], v[220:223], a[8:11], 0\n"
    "ds_read_b64_tr_b16 v[212:213], v144 offset:53248\n"
    "buffer_load_dwordx4 a[84:87], v254, s[76:79], s49 offen offset:1024 sc1\n"
    "v_mfma_f32_16x16x32_bf16 v[48:51], v[216:219], a[0:3], v[48:51]\n"
    "s_waitcnt lgkmcnt(11)\n"
    "ds_read_b64_tr_b16 v[214:215], v144 offset:55296\n"
    "ds_read_b64_tr_b16 a[146:147], v170 offset:18432\n"
    "v_mfma_f32_16x16x32_bf16 v[24:27], v[224:227], a[0:3], 0\n"
    "ds_read_b64_tr_b16 v[216:217], v144 offset:57344\n"
    "ds_read_b64_tr_b16 a[148:149], v170 offset:24576\n"
    "v_mfma_f32_16x16x32_bf16 v[24:27], v[228:231], a[8:11], v[24:27]\n"
    "ds_read_b64_tr_b16 v[218:219], v144 offset:59392\n"
    "ds_read_b64_tr_b16 a[150:151], v170 offset:26624\n"
    "v_mfma_f32_16x16x32_bf16 v[28:31], v[228:231], a[12:15], 0\n"
    "ds_read_b64_tr_b16 v[220:221], v144 offset:61440\n"
    "buffer_load_dwordx4 a[88:91], v254, s[76:79], s49 offen offset:2048 sc1\n"
    "v_mfma_f32_16x16x32_bf16 v[28:31], v[224:227], a[4:7], v[28:31]\n"
    "s_waitcnt lgkmcnt(12)\n"
    "ds_read_b64_tr_b16 v[222:223], v144 offset:63488\n"
    "ds_read_b64_tr_b16 a[152:153], v171 offset:16384\n"
    "v_mfma_f32_16x16x32_bf16 v[60:63], v[232:235], a[4:7], 0\n"
    "ds_read_b64_tr_b16 v[224:225], v145 offset:49152\n"
    "ds_read_b64_tr_b16 a[154:155], v171 offset:18432\n"
    "v_mfma_f32_16x16x32_bf16 v[60:63], v[236:239], a[12:15], v[60:63]\n"
    "ds_read_b64_tr_b16 v[226:227], v145 offset:51200\n"
    "ds_read_b64_tr_b16 a[156:157], v171 offset:24576\n"
    "v_mfma_f32_16x16x32_bf16 v[56:59], v[236:239], a[8:11], 0\n"
    "s_waitcnt vmcnt(15)\n"
    "ds_read_b64_tr_b16 v[228:229], v145 offset:53248\n"
    "buffer_load_dwordx4 a[92:95], v254, s[76:79], s49 offen offset:3072 sc1\n"
    "v_mfma_f32_16x16x32_bf16 v[56:59], v[232:235], a[0:3], v[56:59]\n"
    "s_waitcnt lgkmcnt(13)\n"
    "ds_read_b64_tr_b16 v[230:231], v145 offset:55296\n"
    "ds_read_b64_tr_b16 a[158:159], v171 offset:26624\n"
    "v_mfma_f32_16x16x32_bf16 v[0:3], v[208:211], a[16:19], v[0:3]\n"
    "s_add_u32 s49, s49, 4096\n"
    "ds_read_b64_tr_b16 v[232:233], v145 offset:57344\n"
    "v_mfma_f32_16x16x32_bf16 v[0:3], v[212:215], a[24:27], v[0:3]\n"
    "ds_read_b64_tr_b16 v[234:235], v145 offset:59392\n"
    "ds_read_b64_tr_b16 a[160:161], v136 offset:0\n"
    "v_mfma_f32_16x16x32_bf16 v[4:7], v[212:215], a[28:31], v[4:7]\n"
    "ds_read_b64_tr_b16 v[236:237], v145 offset:61440\n"
    "buffer_load_dwordx4 a[96:99], v254, s[76:79], s49 offen offset:0 sc1\n"
    "v_mfma_f32_16x16x32_bf16 v[4:7], v[208:211], a[20:23], v[4:7]\n"
    "s_waitcnt lgkmcnt(12)\n"
    "ds_read_b64_tr_b16 v[238:239], v145 offset:63488\n"
    "ds_read_b64_tr_b16 a[162:163], v136 offset:2048\n"
    "v_mfma_f32_16x16x32_bf16 v[36:39], v[216:219], a[20:23], v[36:39]\n"
    "ds_read_b64_tr_b16 v[208:209], v146 offset:49152\n"
    "ds_read_b64_tr_b16 a[164:165], v136 offset:8192\n"
    "v_mfma_f32_16x16x32_bf16 v[36:39], v[220:223], a[28:31], v[36:39]\n"
    "ds_read_b64_tr_b16 v[210:211], v146 offset:51200\n"
    "ds_read_b64_tr_b16 a[166:167], v136 offset:10240\n"
    "v_mfma_f32_16x16x32_bf16 v[32:35], v[220:223], a[24:27], v[32:35]\n"
    "ds_read_b64_tr_b16 v[212:213], v146 offset:53248\n"
    "buffer_load_dwordx4 a[100:103], v254, s[76:79], s49 offen offset:1024 sc1\n"
    "v_mfma_f32_16x16x32_bf16 v[32:35], v[216:219], a[16:19], v[32:35]\n"
    "s_waitcnt lgkmcnt(12)\n"
    "ds_read_b64_tr_b16 v[214:215], v146 offset:55296\n"
    "ds_read_b64_tr_b16 a[168:169], v137 offset:0\n"
    "v_mfma_f32_16x16x32_bf16 v[8:11], v[224:227], a[16:19], v[8:11]\n"
    "ds_read_b64_tr_b16 v[216:217], v146 offset:57344\n"
    "ds_read_b64_tr_b16 a[170:171], v137 offset:2048\n"
    "v_mfma_f32_16x16x32_bf16 v[8:11], v[228:231], a[24:27], v[8:11]\n"
    "ds_read_b64_tr_b16 v[218:219], v146 offset:59392\n"
    "ds_read_b64_tr_b16 a[172:173], v137 offset:8192\n"
    "v_mfma_f32_16x16x32_bf16 v[12:15], v[228:231], a[28:31], v[12:15]\n"
    "ds_read_b64_tr_b16 v[220:221], v146 offset:61440\n"
    "buffer_load_dwordx4 a[104:107], v254, s[76:79], s49 offen offset:2048 sc1\n"
    "v_mfma_f32_16x16x32_bf16 v[12:15], v[224:227], a[20:23], v[12:15]\n"
    "s_waitcnt lgkmcnt(13)\n"
    "ds_read_b64_tr_b16 v[222:223], v146 offset:63488\n"
    "ds_read_b64_tr_b16 a[174:175], v137 offset:10240\n"
    "v_mfma_f32_16x16x32_bf16 v[44:47], v[232:235], a[20:23], v[44:47]\n"
    "ds_read_b64_tr_b16 v[224:225], v147 offset:49152\n"
    "v_mfma_f32_16x16x32_bf16 v[44:47], v[236:239], a[28:31], v[44:47]\n"
    "ds_read_b64_tr_b16 v[226:227], v147 offset:51200\n"
    "ds_read_b64_tr_b16 a[176:177], v138 offset:0\n"
    "v_mfma_f32_16x16x32_bf16 v[40:43], v[236:239], a[24:27], v[40:43]\n"
    "ds_read_b64_tr_b16 v[228:229], v147 offset:53248\n"
    "buffer_load_dwordx4 a[108:111], v254, s[76:79], s49 offen offset:3072 sc1\n"
    "v_mfma_f32_16x16x32_bf16 v[40:43], v[232:235], a[16:19], v[40:43]\n"
    "s_waitcnt lgkmcnt(12)\n"
    "ds_read_b64_tr_b16 v[230:231], v147 offset:55296\n"
    "ds_read_b64_tr_b16 a[178:179], v138 offset:2048\n"
    "v_mfma_f32_16x16x32_bf16 v[16:19], v[208:211], a[16:19], v[16:19]\n"
    "s_add_u32 s49, s49, 4096\n"
    "ds_read_b64_tr_b16 v[232:233], v147 offset:57344\n"
    "ds_read_b64_tr_b16 a[180:181], v138 offset:8192\n"
    "v_mfma_f32_16x16x32_bf16 v[16:19], v[212:215], a[24:27], v[16:19]\n"
    "ds_read_b64_tr_b16 v[234:235], v147 offset:59392\n"
    "ds_read_b64_tr_b16 a[182:183], v138 offset:10240\n"
    "v_mfma_f32_16x16x32_bf16 v[20:23], v[212:215], a[28:31], v[20:23]\n"
    "ds_read_b64_tr_b16 v[236:237], v147 offset:61440\n"
    "v_mfma_f32_16x16x32_bf16 v[20:23], v[208:211], a[20:23], v[20:23]\n"
    "s_waitcnt lgkmcnt(12)\n"
    "ds_read_b64_tr_b16 v[238:239], v147 offset:63488\n"
    "ds_read_b64_tr_b16 a[184:185], v139 offset:0\n"
    "v_mfma_f32_16x16x32_bf16 v[52:55], v[216:219], a[20:23], v[52:55]\n"
    "ds_read_b64_tr_b16 v[208:209], v148 offset:0\n"
    "ds_read_b64_tr_b16 a[186:187], v139 offset:2048\n"
    "v_mfma_f32_16x16x32_bf16 v[52:55], v[220:223], a[28:31], v[52:55]\n"
    "ds_read_b64_tr_b16 v[210:211], v148 offset:2048\n"
    "ds_read_b64_tr_b16 a[188:189], v139 offset:8192\n"
    "v_mfma_f32_16x16x32_bf16 v[48:51], v[220:223], a[24:27], v[48:51]\n"
    "ds_read_b64_tr_b16 v[212:213], v148 offset:4096\n"
    "ds_read_b64_tr_b16 a[190:191], v139 offset:10240\n"
    "v_mfma_f32_16x16x32_bf16 v[48:51], v[216:219], a[16:19], v[48:51]\n"
    "s_waitcnt lgkmcnt(14)\n"
    "ds_read_b64_tr_b16 v[214:215], v148 offset:6144\n"
    "v_mfma_f32_16x16x32_bf16 v[24:27], v[224:227], a[16:19], v[24:27]\n"
    "ds_read_b64_tr_b16 v[216:217], v148 offset:8192\n"
    "ds_read_b64_tr_b16 a[192:193], v136 offset:16384\n"
    "v_mfma_f32_16x16x32_bf16 v[24:27], v[228:231], a[24:27], v[24:27]\n"
    "ds_read_b64_tr_b16 v[218:219], v148 offset:10240\n"
    "ds_read_b64_tr_b16 a[194:195], v136 offset:18432\n"
    "v_mfma_f32_16x16x32_bf16 v[28:31], v[228:231], a[28:31], v[28:31]\n"
    "ds_read_b64_tr_b16 v[220:221], v148 offset:12288\n"
    "ds_read_b64_tr_b16 a[196:197], v136 offset:24576\n"
    "v_mfma_f32_16x16x32_bf16 v[28:31], v[224:227], a[20:23], v[28:31]\n"
    "s_waitcnt lgkmcnt(14)\n"
    "ds_read_b64_tr_b16 v[222:223], v148 offset:14336\n"
    "ds_read_b64_tr_b16 a[198:199], v136 offset:26624\n"
    "v_mfma_f32_16x16x32_bf16 v[60:63], v[232:235], a[20:23], v[60:63]\n"
    "ds_read_b64_tr_b16 v[224:225], v149 offset:0\n"
    "v_mfma_f32_16x16x32_bf16 v[60:63], v[236:239], a[28:31], v[60:63]\n"
    "ds_read_b64_tr_b16 v[226:227], v149 offset:2048\n"
    "ds_read_b64_tr_b16 a[200:201], v137 offset:16384\n"
    "v_mfma_f32_16x16x32_bf16 v[56:59], v[236:239], a[24:27], v[56:59]\n"
    "s_waitcnt vmcnt(16)\n"
    "ds_read_b64_tr_b16 v[228:229], v149 offset:4096\n"
    "ds_read_b64_tr_b16 a[202:203], v137 offset:18432\n"
    "v_mfma_f32_16x16x32_bf16 v[56:59], v[232:235], a[16:19], v[56:59]\n"
    "s_waitcnt lgkmcnt(13)\n"
    "ds_read_b64_tr_b16 v[230:231], v149 offset:6144\n"
    "ds_read_b64_tr_b16 a[204:205], v137 offset:24576\n"
    "v_mfma_f32_16x16x32_bf16 v[0:3], v[208:211], a[32:35], v[0:3]\n"
    "ds_read_b64_tr_b16 v[232:233], v149 offset:8192\n"
    "ds_read_b64_tr_b16 a[206:207], v137 offset:26624\n"
    "v_mfma_f32_16x16x32_bf16 v[0:3], v[212:215], a[40:43], v[0:3]\n"
    "ds_read_b64_tr_b16 v[234:235], v149 offset:10240\n"
    "v_mfma_f32_16x16x32_bf16 v[4:7], v[212:215], a[44:47], v[4:7]\n"
    "ds_read_b64_tr_b16 v[236:237], v149 offset:12288\n"
    "ds_read_b64_tr_b16 a[208:209], v138 offset:16384\n"
    "v_mfma_f32_16x16x32_bf16 v[4:7], v[208:211], a[36:39], v[4:7]\n"
    "s_waitcnt lgkmcnt(13)\n"
    "ds_read_b64_tr_b16 v[238:239], v149 offset:14336\n"
    "ds_read_b64_tr_b16 a[210:211], v138 offset:18432\n"
    "v_mfma_f32_16x16x32_bf16 v[36:39], v[216:219], a[36:39], v[36:39]\n"
    "ds_read_b64_tr_b16 v[208:209], v150 offset:0\n"
    "ds_read_b64_tr_b16 a[212:213], v138 offset:24576\n"
    "v_mfma_f32_16x16x32_bf16 v[36:39], v[220:223], a[44:47], v[36:39]\n"
    "ds_read_b64_tr_b16 v[210:211], v150 offset:2048\n"
    "ds_read_b64_tr_b16 a[214:215], v138 offset:26624\n"
    "v_mfma_f32_16x16x32_bf16 v[32:35], v[220:223], a[40:43], v[32:35]\n"
    "ds_read_b64_tr_b16 v[212:213], v150 offset:4096\n"
    "v_mfma_f32_16x16x32_bf16 v[32:35], v[216:219], a[32:35], v[32:35]\n"
    "s_waitcnt lgkmcnt(13)\n"
    "ds_read_b64_tr_b16 v[214:215], v150 offset:6144\n"
    "ds_read_b64_tr_b16 a[216:217], v139 offset:16384\n"
    "v_mfma_f32_16x16x32_bf16 v[8:11], v[224:227], a[32:35], v[8:11]\n"
    "ds_read_b64_tr_b16 v[216:217], v150 offset:8192\n"
    "ds_read_b64_tr_b16 a[218:219], v139 offset:18432\n"
    "v_mfma_f32_16x16x32_bf16 v[8:11], v[228:231], a[40:43], v[8:11]\n"
    "ds_read_b64_tr_b16 v[218:219], v150 offset:10240\n"
    "ds_read_b64_tr_b16 a[220:221], v139 offset:24576\n"
    "v_mfma_f32_16x16x32_bf16 v[12:15], v[228:231], a[44:47], v[12:15]\n"
    "ds_read_b64_tr_b16 v[220:221], v150 offset:12288\n"
    "ds_read_b64_tr_b16 a[222:223], v139 offset:26624\n"
    "v_mfma_f32_16x16x32_bf16 v[12:15], v[224:227], a[36:39], v[12:15]\n"
    "s_waitcnt lgkmcnt(14)\n"
    "ds_read_b64_tr_b16 v[222:223], v150 offset:14336\n"
    "v_mfma_f32_16x16x32_bf16 v[44:47], v[232:235], a[36:39], v[44:47]\n"
    "ds_read_b64_tr_b16 v[224:225], v151 offset:0\n"
    "ds_read_b64_tr_b16 a[224:225], v140 offset:49152\n"
    "v_mfma_f32_16x16x32_bf16 v[44:47], v[236:239], a[44:47], v[44:47]\n"
    "ds_read_b64_tr_b16 v[226:227], v151 offset:2048\n"
    "ds_read_b64_tr_b16 a[226:227], v140 offset:51200\n"
    "v_mfma_f32_16x16x32_bf16 v[40:43], v[236:239], a[40:43], v[40:43]\n"
    "ds_read_b64_tr_b16 v[228:229], v151 offset:4096\n"
    "ds_read_b64_tr_b16 a[228:229], v140 offset:57344\n"
    "v_mfma_f32_16x16x32_bf16 v[40:43], v[232:235], a[32:35], v[40:43]\n"
    "s_waitcnt lgkmcnt(14)\n"
    "ds_read_b64_tr_b16 v[230:231], v151 offset:6144\n"
    "ds_read_b64_tr_b16 a[230:231], v140 offset:59392\n"
    "v_mfma_f32_16x16x32_bf16 v[16:19], v[208:211], a[32:35], v[16:19]\n"
    "ds_read_b64_tr_b16 v[232:233], v151 offset:8192\n"
    "v_mfma_f32_16x16x32_bf16 v[16:19], v[212:215], a[40:43], v[16:19]\n"
    "ds_read_b64_tr_b16 v[234:235], v151 offset:10240\n"
    "ds_read_b64_tr_b16 a[232:233], v141 offset:49152\n"
    "v_mfma_f32_16x16x32_bf16 v[20:23], v[212:215], a[44:47], v[20:23]\n"
    "ds_read_b64_tr_b16 v[236:237], v151 offset:12288\n"
    "ds_read_b64_tr_b16 a[234:235], v141 offset:51200\n"
    "v_mfma_f32_16x16x32_bf16 v[20:23], v[208:211], a[36:39], v[20:23]\n"
    "s_waitcnt lgkmcnt(13)\n"
    "ds_read_b64_tr_b16 v[238:239], v151 offset:14336\n"
    "ds_read_b64_tr_b16 a[236:237], v141 offset:57344\n"
    "v_mfma_f32_16x16x32_bf16 v[52:55], v[216:219], a[36:39], v[52:55]\n"
    "ds_read_b64_tr_b16 v[208:209], v148 offset:16384\n"
    "ds_read_b64_tr_b16 a[238:239], v141 offset:59392\n"
    "v_mfma_f32_16x16x32_bf16 v[52:55], v[220:223], a[44:47], v[52:55]\n"
    "ds_read_b64_tr_b16 v[210:211], v148 offset:18432\n"
    "v_mfma_f32_16x16x32_bf16 v[48:51], v[220:223], a[40:43], v[48:51]\n"
    "ds_read_b64_tr_b16 v[212:213], v148 offset:20480\n"
    "ds_read_b64_tr_b16 a[240:241], v142 offset:49152\n"
    "v_mfma_f32_16x16x32_bf16 v[48:51], v[216:219], a[32:35], v[48:51]\n"
    "s_waitcnt lgkmcnt(13)\n"
    "ds_read_b64_tr_b16 v[214:215], v148 offset:22528\n"
    "ds_read_b64_tr_b16 a[242:243], v142 offset:51200\n"
    "v_mfma_f32_16x16x32_bf16 v[24:27], v[224:227], a[32:35], v[24:27]\n"
    "ds_read_b64_tr_b16 v[216:217], v148 offset:24576\n"
    "ds_read_b64_tr_b16 a[244:245], v142 offset:57344\n"
    "v_mfma_f32_16x16x32_bf16 v[24:27], v[228:231], a[40:43], v[24:27]\n"
    "ds_read_b64_tr_b16 v[218:219], v148 offset:26624\n"
    "ds_read_b64_tr_b16 a[246:247], v142 offset:59392\n"
    "v_mfma_f32_16x16x32_bf16 v[28:31], v[228:231], a[44:47], v[28:31]\n"
    "ds_read_b64_tr_b16 v[220:221], v148 offset:28672\n"
    "v_mfma_f32_16x16x32_bf16 v[28:31], v[224:227], a[36:39], v[28:31]\n"
    "s_waitcnt lgkmcnt(13)\n"
    "ds_read_b64_tr_b16 v[222:223], v148 offset:30720\n"
    "ds_read_b64_tr_b16 a[248:249], v143 offset:49152\n"
    "v_mfma_f32_16x16x32_bf16 v[60:63], v[232:235], a[36:39], v[60:63]\n"
    "ds_read_b64_tr_b16 v[224:225], v149 offset:16384\n"
    "ds_read_b64_tr_b16 a[250:251], v143 offset:51200\n"
    "v_mfma_f32_16x16x32_bf16 v[60:63], v[236:239], a[44:47], v[60:63]\n"
    "ds_read_b64_tr_b16 v[226:227], v149 offset:18432\n"
    "ds_read_b64_tr_b16 a[252:253], v143 offset:57344\n"
    "v_mfma_f32_16x16x32_bf16 v[56:59], v[236:239], a[40:43], v[56:59]\n"
    "s_waitcnt vmcnt(12)\n"
    "ds_read_b64_tr_b16 v[228:229], v149 offset:20480\n"
    "ds_read_b64_tr_b16 a[254:255], v143 offset:59392\n"
    "v_mfma_f32_16x16x32_bf16 v[56:59], v[232:235], a[32:35], v[56:59]\n"
    "s_waitcnt lgkmcnt(14)\n"
    "ds_read_b64_tr_b16 v[230:231], v149 offset:22528\n"
    "v_mfma_f32_16x16x32_bf16 v[0:3], v[208:211], a[48:51], v[0:3]\n"
    "ds_read_b64_tr_b16 v[232:233], v149 offset:24576\n"
    "v_mfma_f32_16x16x32_bf16 v[0:3], v[212:215], a[56:59], v[0:3]\n"
    "ds_read_b64_tr_b16 v[234:235], v149 offset:26624\n"
    "v_mfma_f32_16x16x32_bf16 v[4:7], v[212:215], a[60:63], v[4:7]\n"
    "ds_read_b64_tr_b16 v[236:237], v149 offset:28672\n"
    "v_mfma_f32_16x16x32_bf16 v[4:7], v[208:211], a[52:55], v[4:7]\n"
    "s_waitcnt lgkmcnt(11)\n"
    "ds_read_b64_tr_b16 v[238:239], v149 offset:30720\n"
    "v_mfma_f32_16x16x32_bf16 v[36:39], v[216:219], a[52:55], v[36:39]\n"
    "ds_read_b64_tr_b16 v[208:209], v150 offset:16384\n"
    "v_mfma_f32_16x16x32_bf16 v[36:39], v[220:223], a[60:63], v[36:39]\n"
    "ds_read_b64_tr_b16 v[210:211], v150 offset:18432\n"
    "v_mfma_f32_16x16x32_bf16 v[32:35], v[220:223], a[56:59], v[32:35]\n"
    "ds_read_b64_tr_b16 v[212:213], v150 offset:20480\n"
    "v_mfma_f32_16x16x32_bf16 v[32:35], v[216:219], a[48:51], v[32:35]\n"
    "s_waitcnt lgkmcnt(7)\n"
    "ds_read_b64_tr_b16 v[214:215], v150 offset:22528\n"
    "v_mfma_f32_16x16x32_bf16 v[8:11], v[224:227], a[48:51], v[8:11]\n"
    "ds_read_b64_tr_b16 v[216:217], v150 offset:24576\n"
    "v_mfma_f32_16x16x32_bf16 v[8:11], v[228:231], a[56:59], v[8:11]\n"
    "ds_read_b64_tr_b16 v[218:219], v150 offset:26624\n"
    "v_mfma_f32_16x16x32_bf16 v[12:15], v[228:231], a[60:63], v[12:15]\n"
    "ds_read_b64_tr_b16 v[220:221], v150 offset:28672\n"
    "v_mfma_f32_16x16x32_bf16 v[12:15], v[224:227], a[52:55], v[12:15]\n"
    "s_waitcnt lgkmcnt(7)\n"
    "ds_read_b64_tr_b16 v[222:223], v150 offset:30720\n"
    "v_mfma_f32_16x16x32_bf16 v[44:47], v[232:235], a[52:55], v[44:47]\n"
    "ds_read_b64_tr_b16 v[224:225], v151 offset:16384\n"
    "v_mfma_f32_16x16x32_bf16 v[44:47], v[236:239], a[60:63], v[44:47]\n"
    "ds_read_b64_tr_b16 v[226:227], v151 offset:18432\n"
    "v_mfma_f32_16x16x32_bf16 v[40:43], v[236:239], a[56:59], v[40:43]\n"
    "ds_read_b64_tr_b16 v[228:229], v151 offset:20480\n"
    "v_mfma_f32_16x16x32_bf16 v[40:43], v[232:235], a[48:51], v[40:43]\n"
    "s_waitcnt lgkmcnt(7)\n"
    "ds_read_b64_tr_b16 v[230:231], v151 offset:22528\n"
    "v_mfma_f32_16x16x32_bf16 v[16:19], v[208:211], a[48:51], v[16:19]\n"
    "ds_read_b64_tr_b16 v[232:233], v151 offset:24576\n"
    "v_mfma_f32_16x16x32_bf16 v[16:19], v[212:215], a[56:59], v[16:19]\n"
    "ds_read_b64_tr_b16 v[234:235], v151 offset:26624\n"
    "v_mfma_f32_16x16x32_bf16 v[20:23], v[212:215], a[60:63], v[20:23]\n"
    "ds_read_b64_tr_b16 v[236:237], v151 offset:28672\n"
    "v_mfma_f32_16x16x32_bf16 v[20:23], v[208:211], a[52:55], v[20:23]\n"
    "s_waitcnt lgkmcnt(7)\n"
    "ds_read_b64_tr_b16 v[238:239], v151 offset:30720\n"
    "v_mfma_f32_16x16x32_bf16 v[52:55], v[216:219], a[52:55], v[52:55]\n"
    "ds_read_b64_tr_b16 v[208:209], v148 offset:32768\n"
    "v_mfma_f32_16x16x32_bf16 v[52:55], v[220:223], a[60:63], v[52:55]\n"
    "ds_read_b64_tr_b16 v[210:211], v148 offset:34816\n"
    "v_mfma_f32_16x16x32_bf16 v[48:51], v[220:223], a[56:59], v[48:51]\n"
    "ds_read_b64_tr_b16 v[212:213], v148 offset:36864\n"
    "v_mfma_f32_16x16x32_bf16 v[48:51], v[216:219], a[48:51], v[48:51]\n"
    "s_waitcnt lgkmcnt(7)\n"
    "ds_read_b64_tr_b16 v[214:215], v148 offset:38912\n"
    "v_mfma_f32_16x16x32_bf16 v[24:27], v[224:227], a[48:51], v[24:27]\n"
    "ds_read_b64_tr_b16 v[216:217], v148 offset:40960\n"
    "v_mfma_f32_16x16x32_bf16 v[24:27], v[228:231], a[56:59], v[24:27]\n"
    "ds_read_b64_tr_b16 v[218:219], v148 offset:43008\n"
    "v_mfma_f32_16x16x32_bf16 v[28:31], v[228:231], a[60:63], v[28:31]\n"
    "ds_read_b64_tr_b16 v[220:221], v148 offset:45056\n"
    "v_mfma_f32_16x16x32_bf16 v[28:31], v[224:227], a[52:55], v[28:31]\n"
    "s_waitcnt lgkmcnt(7)\n"
    "ds_read_b64_tr_b16 v[222:223], v148 offset:47104\n"
    "v_mfma_f32_16x16x32_bf16 v[60:63], v[232:235], a[52:55], v[60:63]\n"
    "ds_read_b64_tr_b16 v[224:225], v149 offset:32768\n"
    "v_mfma_f32_16x16x32_bf16 v[60:63], v[236:239], a[60:63], v[60:63]\n"
    "ds_read_b64_tr_b16 v[226:227], v149 offset:34816\n"
    "v_mfma_f32_16x16x32_bf16 v[56:59], v[236:239], a[56:59], v[56:59]\n"
    "s_waitcnt vmcnt(8)\n"
    "ds_read_b64_tr_b16 v[228:229], v149 offset:36864\n"
    "v_mfma_f32_16x16x32_bf16 v[56:59], v[232:235], a[48:51], v[56:59]\n"
    "s_waitcnt lgkmcnt(7)\n"
    "ds_read_b64_tr_b16 v[230:231], v149 offset:38912\n"
    "v_mfma_f32_16x16x32_bf16 v[0:3], v[208:211], a[64:67], v[0:3]\n"
    "ds_read_b64_tr_b16 v[232:233], v149 offset:40960\n"
    "v_mfma_f32_16x16x32_bf16 v[0:3], v[212:215], a[72:75], v[0:3]\n"
    "s_barrier\n"
    "v_mfma_f32_16x16x32_bf16 v[4:7], v[212:215], a[76:79], v[4:7]\n"
    "s_add_u32 m0, s54, 0\n"
    "ds_read_b64_tr_b16 v[234:235], v149 offset:43008\n"
    "ds_read_b64_tr_b16 v[236:237], v149 offset:45056\n"
    "v_mfma_f32_16x16x32_bf16 v[4:7], v[208:211], a[68:71], v[4:7]\n"
    "s_waitcnt lgkmcnt(7)\n"
    "ds_read_b64_tr_b16 v[238:239], v149 offset:47104\n"
    "v_mfma_f32_16x16x32_bf16 v[36:39], v[216:219], a[68:71], v[36:39]\n"
    "s_add_u32 s73, s73, 143360\n"
    "ds_read_b64_tr_b16 v[208:209], v150 offset:32768\n"
    "v_mfma_f32_16x16x32_bf16 v[36:39], v[220:223], a[76:79], v[36:39]\n"
    "ds_read_b64_tr_b16 v[210:211], v150 offset:34816\n"
    "buffer_load_dwordx4 v134, s[80:83], s73 offen lds\n"
    "v_mfma_f32_16x16x32_bf16 v[32:35], v[220:223], a[72:75], v[32:35]\n"
    "s_add_u32 m0, s54, 1024\n"
    "ds_read_b64_tr_b16 v[212:213], v150 offset:36864\n"
    "v_mfma_f32_16x16x32_bf16 v[32:35], v[216:219], a[64:67], v[32:35]\n"
    "s_waitcnt lgkmcnt(7)\n"
    "ds_read_b64_tr_b16 v[214:215], v150 offset:38912\n"
    "v_mfma_f32_16x16x32_bf16 v[8:11], v[224:227], a[64:67], v[8:11]\n"
    "s_add_u32 s73, s73, 28672\n"
    "ds_read_b64_tr_b16 v[216:217], v150 offset:40960\n"
    "v_mfma_f32_16x16x32_bf16 v[8:11], v[228:231], a[72:75], v[8:11]\n"
    "ds_read_b64_tr_b16 v[218:219], v150 offset:43008\n"
    "buffer_load_dwordx4 v134, s[80:83], s73 offen lds\n"
    "v_mfma_f32_16x16x32_bf16 v[12:15], v[228:231], a[76:79], v[12:15]\n"
    "s_add_u32 m0, s54, 2048\n"
    "ds_read_b64_tr_b16 v[220:221], v150 offset:45056\n"
    "v_mfma_f32_16x16x32_bf16 v[12:15], v[224:227], a[68:71], v[12:15]\n"
    "s_waitcnt lgkmcnt(7)\n"
    "ds_read_b64_tr_b16 v[222:223], v150 offset:47104\n"
    "v_mfma_f32_16x16x32_bf16 v[44:47], v[232:235], a[68:71], v[44:47]\n"
    "s_add_u32 s73, s73, 28672\n"
    "ds_read_b64_tr_b16 v[224:225], v151 offset:32768\n"
    "v_mfma_f32_16x16x32_bf16 v[44:47], v[236:239], a[76:79], v[44:47]\n"
    "ds_read_b64_tr_b16 v[226:227], v151 offset:34816\n"
    "buffer_load_dwordx4 v134, s[80:83], s73 offen lds\n"
    "v_mfma_f32_16x16x32_bf16 v[40:43], v[236:239], a[72:75], v[40:43]\n"
    "s_add_u32 m0, s54, 3072\n"
    "ds_read_b64_tr_b16 v[228:229], v151 offset:36864\n"
    "v_mfma_f32_16x16x32_bf16 v[40:43], v[232:235], a[64:67], v[40:43]\n"
    "s_waitcnt lgkmcnt(7)\n"
    "ds_read_b64_tr_b16 v[230:231], v151 offset:38912\n"
    "v_mfma_f32_16x16x32_bf16 v[16:19], v[208:211], a[64:67], v[16:19]\n"
    "s_add_u32 s73, s73, 28672\n"
    "ds_read_b64_tr_b16 v[232:233], v151 offset:40960\n"
    "v_mfma_f32_16x16x32_bf16 v[16:19], v[212:215], a[72:75], v[16:19]\n"
    "ds_read_b64_tr_b16 v[234:235], v151 offset:43008\n"
    "buffer_load_dwordx4 v134, s[80:83], s73 offen lds\n"
    "v_mfma_f32_16x16x32_bf16 v[20:23], v[212:215], a[76:79], v[20:23]\n"
    "s_add_u32 m0, s54, 16384\n"
    "ds_read_b64_tr_b16 v[236:237], v151 offset:45056\n"
    "v_mfma_f32_16x16x32_bf16 v[20:23], v[208:211], a[68:71], v[20:23]\n"
    "s_waitcnt lgkmcnt(7)\n"
    "ds_read_b64_tr_b16 v[238:239], v151 offset:47104\n"
    "v_mfma_f32_16x16x32_bf16 v[52:55], v[216:219], a[68:71], v[52:55]\n"
    "s_sub_u32 s73, s73, 1462144\n"
    "ds_read_b64_tr_b16 v[208:209], v156 offset:0\n"
    "v_mfma_f32_16x16x32_bf16 v[52:55], v[220:223], a[76:79], v[52:55]\n"
    "ds_read_b64_tr_b16 v[210:211], v156 offset:2048\n"
    "buffer_load_dwordx4 v134, s[80:83], s73 offen lds\n"
    "v_mfma_f32_16x16x32_bf16 v[48:51], v[220:223], a[72:75], v[48:51]\n"
    "s_add_u32 m0, s54, 17408\n"
    "ds_read_b64_tr_b16 v[212:213], v156 offset:4096\n"
    "v_mfma_f32_16x16x32_bf16 v[48:51], v[216:219], a[64:67], v[48:51]\n"
    "s_waitcnt lgkmcnt(7)\n"
    "ds_read_b64_tr_b16 v[214:215], v156 offset:6144\n"
    "v_mfma_f32_16x16x32_bf16 v[24:27], v[224:227], a[64:67], v[24:27]\n"
    "s_add_u32 s73, s73, 28672\n"
    "ds_read_b64_tr_b16 v[216:217], v156 offset:8192\n"
    "v_mfma_f32_16x16x32_bf16 v[24:27], v[228:231], a[72:75], v[24:27]\n"
    "ds_read_b64_tr_b16 v[218:219], v156 offset:10240\n"
    "buffer_load_dwordx4 v134, s[80:83], s73 offen lds\n"
    "v_mfma_f32_16x16x32_bf16 v[28:31], v[228:231], a[76:79], v[28:31]\n"
    "s_add_u32 m0, s54, 18432\n"
    "ds_read_b64_tr_b16 v[220:221], v156 offset:12288\n"
    "v_mfma_f32_16x16x32_bf16 v[28:31], v[224:227], a[68:71], v[28:31]\n"
    "s_waitcnt lgkmcnt(7)\n"
    "ds_read_b64_tr_b16 v[222:223], v156 offset:14336\n"
    "v_mfma_f32_16x16x32_bf16 v[60:63], v[232:235], a[68:71], v[60:63]\n"
    "s_add_u32 s73, s73, 28672\n"
    "ds_read_b64_tr_b16 v[224:225], v157 offset:0\n"
    "v_mfma_f32_16x16x32_bf16 v[60:63], v[236:239], a[76:79], v[60:63]\n"
    "ds_read_b64_tr_b16 v[226:227], v157 offset:2048\n"
    "buffer_load_dwordx4 v134, s[80:83], s73 offen lds\n"
    "v_mfma_f32_16x16x32_bf16 v[56:59], v[236:239], a[72:75], v[56:59]\n"
    "s_waitcnt vmcnt(11)\n"
    "ds_read_b64_tr_b16 v[228:229], v157 offset:4096\n"
    "v_mfma_f32_16x16x32_bf16 v[56:59], v[232:235], a[64:67], v[56:59]\n"
    "s_waitcnt lgkmcnt(7)\n"
    "ds_read_b64_tr_b16 v[230:231], v157 offset:6144\n"
    "v_mfma_f32_16x16x32_bf16 v[0:3], v[208:211], a[80:83], v[0:3]\n"
    "s_add_u32 m0, s54, 19456\n"
    "ds_read_b64_tr_b16 v[232:233], v157 offset:8192\n"
    "v_mfma_f32_16x16x32_bf16 v[0:3], v[212:215], a[88:91], v[0:3]\n"
    "s_add_u32 s73, s73, 28672\n"
    "ds_read_b64_tr_b16 v[234:235], v157 offset:10240\n"
    "v_mfma_f32_16x16x32_bf16 v[4:7], v[212:215], a[92:95], v[4:7]\n"
    "ds_read_b64_tr_b16 v[236:237], v157 offset:12288\n"
    "buffer_load_dwordx4 v134, s[80:83], s73 offen lds\n"
    "v_mfma_f32_16x16x32_bf16 v[4:7], v[208:211], a[84:87], v[4:7]\n"
    "s_waitcnt lgkmcnt(7)\n"
    "ds_read_b64_tr_b16 v[238:239], v157 offset:14336\n"
    "v_mfma_f32_16x16x32_bf16 v[36:39], v[216:219], a[84:87], v[36:39]\n"
    "s_add_u32 m0, s54, 147456\n"
    "ds_read_b64_tr_b16 v[208:209], v158 offset:0\n"
    "v_mfma_f32_16x16x32_bf16 v[36:39], v[220:223], a[92:95], v[36:39]\n"
    "s_add_u32 s73, s73, 143360\n"
    "ds_read_b64_tr_b16 v[210:211], v158 offset:2048\n"
    "v_mfma_f32_16x16x32_bf16 v[32:35], v[220:223], a[88:91], v[32:35]\n"
    "ds_read_b64_tr_b16 v[212:213], v158 offset:4096\n"
    "buffer_load_dwordx4 v134, s[80:83], s73 offen lds\n"
    "v_mfma_f32_16x16x32_bf16 v[32:35], v[216:219], a[80:83], v[32:35]\n"
    "s_waitcnt lgkmcnt(7)\n"
    "ds_read_b64_tr_b16 v[214:215], v158 offset:6144\n"
    "v_mfma_f32_16x16x32_bf16 v[8:11], v[224:227], a[80:83], v[8:11]\n"
    "s_add_u32 m0, s54, 148480\n"
    "ds_read_b64_tr_b16 v[216:217], v158 offset:8192\n"
    "v_mfma_f32_16x16x32_bf16 v[8:11], v[228:231], a[88:91], v[8:11]\n"
    "s_add_u32 s73, s73, 28672\n"
    "ds_read_b64_tr_b16 v[218:219], v158 offset:10240\n"
    "v_mfma_f32_16x16x32_bf16 v[12:15], v[228:231], a[92:95], v[12:15]\n"
    "ds_read_b64_tr_b16 v[220:221], v158 offset:12288\n"
    "buffer_load_dwordx4 v134, s[80:83], s73 offen lds\n"
    "v_mfma_f32_16x16x32_bf16 v[12:15], v[224:227], a[84:87], v[12:15]\n"
    "s_waitcnt lgkmcnt(7)\n"
    "ds_read_b64_tr_b16 v[222:223], v158 offset:14336\n"
    "v_mfma_f32_16x16x32_bf16 v[44:47], v[232:235], a[84:87], v[44:47]\n"
    "s_add_u32 m0, s54, 149504\n"
    "ds_read_b64_tr_b16 v[224:225], v159 offset:0\n"
    "v_mfma_f32_16x16x32_bf16 v[44:47], v[236:239], a[92:95], v[44:47]\n"
    "s_add_u32 s73, s73, 28672\n"
    "ds_read_b64_tr_b16 v[226:227], v159 offset:2048\n"
    "v_mfma_f32_16x16x32_bf16 v[40:43], v[236:239], a[88:91], v[40:43]\n"
    "ds_read_b64_tr_b16 v[228:229], v159 offset:4096\n"
    "buffer_load_dwordx4 v134, s[80:83], s73 offen lds\n"
    "v_mfma_f32_16x16x32_bf16 v[40:43], v[232:235], a[80:83], v[40:43]\n"
    "s_waitcnt lgkmcnt(7)\n"
    "ds_read_b64_tr_b16 v[230:231], v159 offset:6144\n"
    "v_mfma_f32_16x16x32_bf16 v[16:19], v[208:211], a[80:83], v[16:19]\n"
    "s_add_u32 m0, s54, 150528\n"
    "ds_read_b64_tr_b16 v[232:233], v159 offset:8192\n"
    "v_mfma_f32_16x16x32_bf16 v[16:19], v[212:215], a[88:91], v[16:19]\n"
    "s_add_u32 s73, s73, 28672\n"
    "ds_read_b64_tr_b16 v[234:235], v159 offset:10240\n"
    "v_mfma_f32_16x16x32_bf16 v[20:23], v[212:215], a[92:95], v[20:23]\n"
    "s_waitcnt vmcnt(3)\n"
    "s_barrier\n"
    "v_mfma_f32_16x16x32_bf16 v[20:23], v[208:211], a[84:87], v[20:23]\n"
    "s_waitcnt lgkmcnt(6)\n"
    "ds_read_b64_tr_b16 v[236:237], v159 offset:12288\n"
    "ds_read_b64_tr_b16 v[238:239], v159 offset:14336\n"
    "v_mfma_f32_16x16x32_bf16 v[52:55], v[216:219], a[84:87], v[52:55]\n"
    "ds_read_b64_tr_b16 v[208:209], v144 offset:0\n"
    "buffer_load_dwordx4 v134, s[80:83], s73 offen lds\n"
    "v_mfma_f32_16x16x32_bf16 v[52:55], v[220:223], a[92:95], v[52:55]\n"
    "s_add_u32 m0, s54, 114688\n"
    "ds_read_b64_tr_b16 v[210:211], v144 offset:2048\n"
    "v_mfma_f32_16x16x32_bf16 v[48:51], v[220:223], a[88:91], v[48:51]\n"
    "s_add_u32 s73, s73, 143360\n"
    "ds_read_b64_tr_b16 v[212:213], v144 offset:4096\n"
    "v_mfma_f32_16x16x32_bf16 v[48:51], v[216:219], a[80:83], v[48:51]\n"
    "s_waitcnt lgkmcnt(7)\n"
    "ds_read_b64_tr_b16 v[214:215], v144 offset:6144\n"
    "buffer_load_dwordx4 v134, s[80:83], s73 offen lds\n"
    "v_mfma_f32_16x16x32_bf16 v[24:27], v[224:227], a[80:83], v[24:27]\n"
    "s_add_u32 m0, s54, 115712\n"
    "ds_read_b64_tr_b16 v[216:217], v144 offset:8192\n"
    "v_mfma_f32_16x16x32_bf16 v[24:27], v[228:231], a[88:91], v[24:27]\n"
    "s_add_u32 s73, s73, 28672\n"
    "ds_read_b64_tr_b16 v[218:219], v144 offset:10240\n"
    "v_mfma_f32_16x16x32_bf16 v[28:31], v[228:231], a[92:95], v[28:31]\n"
    "ds_read_b64_tr_b16 v[220:221], v144 offset:12288\n"
    "buffer_load_dwordx4 v134, s[80:83], s73 offen lds\n"
    "v_mfma_f32_16x16x32_bf16 v[28:31], v[224:227], a[84:87], v[28:31]\n"
    "s_waitcnt lgkmcnt(7)\n"
    "ds_read_b64_tr_b16 v[222:223], v144 offset:14336\n"
    "v_mfma_f32_16x16x32_bf16 v[60:63], v[232:235], a[84:87], v[60:63]\n"
    "s_add_u32 m0, s54, 116736\n"
    "ds_read_b64_tr_b16 v[224:225], v145 offset:0\n"
    "v_mfma_f32_16x16x32_bf16 v[60:63], v[236:239], a[92:95], v[60:63]\n"
    "s_add_u32 s73, s73, 28672\n"
    "ds_read_b64_tr_b16 v[226:227], v145 offset:2048\n"
    "v_mfma_f32_16x16x32_bf16 v[56:59], v[236:239], a[88:91], v[56:59]\n"
    "s_nop 0\n"
    "ds_read_b64_tr_b16 v[228:229], v145 offset:4096\n"
    "buffer_load_dwordx4 v134, s[80:83], s73 offen lds\n"
    "v_mfma_f32_16x16x32_bf16 v[56:59], v[232:235], a[80:83], v[56:59]\n"
    "s_waitcnt lgkmcnt(7)\n"
    "ds_read_b64_tr_b16 v[230:231], v145 offset:6144\n"
    "v_mfma_f32_16x16x32_bf16 v[0:3], v[208:211], a[96:99], v[0:3]\n"
    "s_add_u32 m0, s54, 117760\n"
    "ds_read_b64_tr_b16 v[232:233], v145 offset:8192\n"
    "v_mfma_f32_16x16x32_bf16 v[0:3], v[212:215], a[104:107], v[0:3]\n"
    "s_add_u32 s73, s73, 28672\n"
    "ds_read_b64_tr_b16 v[234:235], v145 offset:10240\n"
    "v_mfma_f32_16x16x32_bf16 v[4:7], v[212:215], a[108:111], v[4:7]\n"
    "ds_read_b64_tr_b16 v[236:237], v145 offset:12288\n"
    "buffer_load_dwordx4 v134, s[80:83], s73 offen lds\n"
    "v_mfma_f32_16x16x32_bf16 v[4:7], v[208:211], a[100:103], v[4:7]\n"
    "s_waitcnt lgkmcnt(7)\n"
    "ds_read_b64_tr_b16 v[238:239], v145 offset:14336\n"
    "v_mfma_f32_16x16x32_bf16 v[36:39], v[216:219], a[100:103], v[36:39]\n"
    "s_add_u32 m0, s54, 32768\n"
    "ds_read_b64_tr_b16 v[208:209], v146 offset:0\n"
    "v_mfma_f32_16x16x32_bf16 v[36:39], v[220:223], a[108:111], v[36:39]\n"
    "s_add_u32 s73, s73, 143360\n"
    "ds_read_b64_tr_b16 v[210:211], v146 offset:2048\n"
    "v_mfma_f32_16x16x32_bf16 v[32:35], v[220:223], a[104:107], v[32:35]\n"
    "ds_read_b64_tr_b16 v[212:213], v146 offset:4096\n"
    "buffer_load_dwordx4 v134, s[80:83], s73 offen lds\n"
    "v_mfma_f32_16x16x32_bf16 v[32:35], v[216:219], a[96:99], v[32:35]\n"
    "s_waitcnt lgkmcnt(7)\n"
    "ds_read_b64_tr_b16 v[214:215], v146 offset:6144\n"
    "v_mfma_f32_16x16x32_bf16 v[8:11], v[224:227], a[96:99], v[8:11]\n"
    "s_add_u32 m0, s54, 33792\n"
    "ds_read_b64_tr_b16 v[216:217], v146 offset:8192\n"
    "v_mfma_f32_16x16x32_bf16 v[8:11], v[228:231], a[104:107], v[8:11]\n"
    "s_add_u32 s73, s73, 28672\n"
    "ds_read_b64_tr_b16 v[218:219], v146 offset:10240\n"
    "v_mfma_f32_16x16x32_bf16 v[12:15], v[228:231], a[108:111], v[12:15]\n"
    "ds_read_b64_tr_b16 v[220:221], v146 offset:12288\n"
    "buffer_load_dwordx4 v134, s[80:83], s73 offen lds\n"
    "v_mfma_f32_16x16x32_bf16 v[12:15], v[224:227], a[100:103], v[12:15]\n"
    "s_waitcnt lgkmcnt(7)\n"
    "ds_read_b64_tr_b16 v[222:223], v146 offset:14336\n"
    "v_mfma_f32_16x16x32_bf16 v[44:47], v[232:235], a[100:103], v[44:47]\n"
    "s_add_u32 m0, s54, 34816\n"
    "ds_read_b64_tr_b16 v[224:225], v147 offset:0\n"
    "v_mfma_f32_16x16x32_bf16 v[44:47], v[236:239], a[108:111], v[44:47]\n"
    "s_add_u32 s73, s73, 28672\n"
    "ds_read_b64_tr_b16 v[226:227], v147 offset:2048\n"
    "v_mfma_f32_16x16x32_bf16 v[40:43], v[236:239], a[104:107], v[40:43]\n"
    "ds_read_b64_tr_b16 v[228:229], v147 offset:4096\n"
    "buffer_load_dwordx4 v134, s[80:83], s73 offen lds\n"
    "v_mfma_f32_16x16x32_bf16 v[40:43], v[232:235], a[96:99], v[40:43]\n"
    "s_waitcnt lgkmcnt(7)\n"
    "ds_read_b64_tr_b16 v[230:231], v147 offset:6144\n"
    "v_mfma_f32_16x16x32_bf16 v[16:19], v[208:211], a[96:99], v[16:19]\n"
    "s_add_u32 m0, s54, 35840\n"
    "ds_read_b64_tr_b16 v[232:233], v147 offset:8192\n"
    "v_mfma_f32_16x16x32_bf16 v[16:19], v[212:215], a[104:107], v[16:19]\n"
    "s_add_u32 s73, s73, 28672\n"
    "ds_read_b64_tr_b16 v[234:235], v147 offset:10240\n"
    "v_mfma_f32_16x16x32_bf16 v[20:23], v[212:215], a[108:111], v[20:23]\n"
    "ds_read_b64_tr_b16 v[236:237], v147 offset:12288\n"
    "buffer_load_dwordx4 v134, s[80:83], s73 offen lds\n"
    "v_mfma_f32_16x16x32_bf16 v[20:23], v[208:211], a[100:103], v[20:23]\n"
    "s_waitcnt lgkmcnt(7)\n"
    "ds_read_b64_tr_b16 v[238:239], v147 offset:14336\n"
    "v_mfma_f32_16x16x32_bf16 v[52:55], v[216:219], a[100:103], v[52:55]\n"
    "s_add_u32 m0, s54, 49152\n"
    "ds_read_b64_tr_b16 v[208:209], v144 offset:16384\n"
    "v_mfma_f32_16x16x32_bf16 v[52:55], v[220:223], a[108:111], v[52:55]\n"
    "s_add_u32 s73, s73, 143360\n"
    "ds_read_b64_tr_b16 v[210:211], v144 offset:18432\n"
    "v_mfma_f32_16x16x32_bf16 v[48:51], v[220:223], a[104:107], v[48:51]\n"
    "ds_read_b64_tr_b16 v[212:213], v144 offset:20480\n"
    "buffer_load_dwordx4 v134, s[80:83], s73 offen lds\n"
    "v_mfma_f32_16x16x32_bf16 v[48:51], v[216:219], a[96:99], v[48:51]\n"
    "s_waitcnt lgkmcnt(7)\n"
    "ds_read_b64_tr_b16 v[214:215], v144 offset:22528\n"
    "v_mfma_f32_16x16x32_bf16 v[24:27], v[224:227], a[96:99], v[24:27]\n"
    "s_add_u32 m0, s54, 50176\n"
    "ds_read_b64_tr_b16 v[216:217], v144 offset:24576\n"
    "v_mfma_f32_16x16x32_bf16 v[24:27], v[228:231], a[104:107], v[24:27]\n"
    "s_add_u32 s73, s73, 28672\n"
    "ds_read_b64_tr_b16 v[218:219], v144 offset:26624\n"
    "v_mfma_f32_16x16x32_bf16 v[28:31], v[228:231], a[108:111], v[28:31]\n"
    "ds_read_b64_tr_b16 v[220:221], v144 offset:28672\n"
    "buffer_load_dwordx4 v134, s[80:83], s73 offen lds\n"
    "v_mfma_f32_16x16x32_bf16 v[28:31], v[224:227], a[100:103], v[28:31]\n"
    "s_waitcnt lgkmcnt(7)\n"
    "ds_read_b64_tr_b16 v[222:223], v144 offset:30720\n"
    "v_mfma_f32_16x16x32_bf16 v[60:63], v[232:235], a[100:103], v[60:63]\n"
    "s_add_u32 m0, s54, 51200\n"
    "ds_read_b64_tr_b16 v[224:225], v145 offset:16384\n"
    "v_mfma_f32_16x16x32_bf16 v[60:63], v[236:239], a[108:111], v[60:63]\n"
    "s_add_u32 s73, s73, 28672\n"
    "ds_read_b64_tr_b16 v[226:227], v145 offset:18432\n"
    "v_mfma_f32_16x16x32_bf16 v[56:59], v[236:239], a[104:107], v[56:59]\n"
    "ds_read_b64_tr_b16 v[228:229], v145 offset:20480\n"
    "buffer_load_dwordx4 v134, s[80:83], s73 offen lds\n"
    "v_mfma_f32_16x16x32_bf16 v[56:59], v[232:235], a[96:99], v[56:59]\n"
    "s_waitcnt lgkmcnt(7)\n"
    "ds_read_b64_tr_b16 v[230:231], v145 offset:22528\n"
    "v_mfma_f32_16x16x32_bf16 v[64:67], v[208:211], a[0:3], 0\n"
    "s_add_u32 m0, s54, 52224\n"
    "ds_read_b64_tr_b16 v[232:233], v145 offset:24576\n"
    "v_mfma_f32_16x16x32_bf16 v[64:67], v[212:215], a[8:11], v[64:67]\n"
    "s_add_u32 s73, s73, 28672\n"
    "ds_read_b64_tr_b16 v[234:235], v145 offset:26624\n"
    "v_mfma_f32_16x16x32_bf16 v[68:71], v[212:215], a[12:15], 0\n"
    "ds_read_b64_tr_b16 v[236:237], v145 offset:28672\n"
    "buffer_load_dwordx4 v134, s[80:83], s73 offen lds\n"
    "v_mfma_f32_16x16x32_bf16 v[68:71], v[208:211], a[4:7], v[68:71]\n"
    "s_waitcnt lgkmcnt(7)\n"
    "ds_read_b64_tr_b16 v[238:239], v145 offset:30720\n"
    "v_mfma_f32_16x16x32_bf16 v[100:103], v[216:219], a[4:7], 0\n"
    "s_add_u32 m0, s54, 65536\n"
    "ds_read_b64_tr_b16 v[208:209], v146 offset:16384\n"
    "v_mfma_f32_16x16x32_bf16 v[100:103], v[220:223], a[12:15], v[100:103]\n"
    "s_add_u32 s73, s73, 143360\n"
    "ds_read_b64_tr_b16 v[210:211], v146 offset:18432\n"
    "v_mfma_f32_16x16x32_bf16 v[96:99], v[220:223], a[8:11], 0\n"
    "ds_read_b64_tr_b16 v[212:213], v146 offset:20480\n"
    "buffer_load_dwordx4 v134, s[80:83], s73 offen lds\n"
    "v_mfma_f32_16x16x32_bf16 v[96:99], v[216:219], a[0:3], v[96:99]\n"
    "s_waitcnt lgkmcnt(7)\n"
    "ds_read_b64_tr_b16 v[214:215], v146 offset:22528\n"
    "v_mfma_f32_16x16x32_bf16 v[72:75], v[224:227], a[0:3], 0\n"
    "s_add_u32 m0, s54, 66560\n"
    "ds_read_b64_tr_b16 v[216:217], v146 offset:24576\n"
    "ds_read_b64_tr_b16 v[218:219], v146 offset:26624\n"
    "v_mfma_f32_16x16x32_bf16 v[72:75], v[228:231], a[8:11], v[72:75]\n"
    "s_add_u32 s73, s73, 28672\n"
    "v_mul_f32 v160, v0, v0\n"
    "v_mul_f32 v161, v1, v1\n"
    "v_fma_f32 v160, v160, s50, v242\n"
    "v_fma_f32 v161, v161, s50, v242\n"
    "v_mul_f32 v160, v160, v0\n"
    "v_mul_f32 v161, v161, v1\n"
    "v_exp_f32 v160, v160\n"
    "v_exp_f32 v161, v161\n"
    "v_mfma_f32_16x16x32_bf16 v[76:79], v[228:231], a[12:15], 0\n"
    "ds_read_b64_tr_b16 v[220:221], v146 offset:28672\n"
    "ds_read_b64_tr_b16 v[222:223], v146 offset:30720\n"
    "v_mfma_f32_16x16x32_bf16 v[76:79], v[224:227], a[4:7], v[76:79]\n"
    "s_waitcnt lgkmcnt(8)\n"
    "buffer_load_dwordx4 v134, s[80:83], s73 offen lds\n"
    "v_mul_f32 v0, v0, v32\n"
    "v_mul_f32 v1, v1, v33\n"
    "v_add_f32 v160, 1.0, v160\n"
    "v_add_f32 v161, 1.0, v161\n"
    "v_rcp_f32 v160, v160\n"
    "v_rcp_f32 v161, v161\n"
    "v_mfma_f32_16x16x32_bf16 v[108:111], v[232:235], a[4:7], 0\n"
    "s_add_u32 m0, s54, 67584\n"
    "ds_read_b64_tr_b16 v[224:225], v147 offset:16384\n"
    "ds_read_b64_tr_b16 v[226:227], v147 offset:18432\n"
    "v_mfma_f32_16x16x32_bf16 v[108:111], v[236:239], a[12:15], v[108:111]\n"
    "s_add_u32 s73, s73, 28672\n"
    "v_mul_f32 v0, v0, v160\n"
    "v_mul_f32 v1, v1, v161\n"
    "v_mul_f32 v160, v2, v2\n"
    "v_mul_f32 v161, v3, v3\n"
    "v_fma_f32 v160, v160, s50, v242\n"
    "v_fma_f32 v161, v161, s50, v242\n"
    "v_mul_f32 v160, v160, v2\n"
    "v_mul_f32 v161, v161, v3\n"
    "v_exp_f32 v160, v160\n"
    "v_mfma_f32_16x16x32_bf16 v[104:107], v[236:239], a[8:11], 0\n"
    "ds_read_b64_tr_b16 v[228:229], v147 offset:20480\n"
    "ds_read_b64_tr_b16 v[230:231], v147 offset:22528\n"
    "v_mfma_f32_16x16x32_bf16 v[104:107], v[232:235], a[0:3], v[104:107]\n"
    "s_waitcnt lgkmcnt(8)\n"
    "buffer_load_dwordx4 v134, s[80:83], s73 offen lds\n"
    "v_exp_f32 v161, v161\n"
    "v_mul_f32 v2, v2, v34\n"
    "v_mul_f32 v3, v3, v35\n"
    "v_add_f32 v160, 1.0, v160\n"
    "v_add_f32 v161, 1.0, v161\n"
    "v_rcp_f32 v160, v160\n"
    "v_mfma_f32_16x16x32_bf16 v[80:83], v[208:211], a[0:3], 0\n"
    "s_add_u32 m0, s54, 68608\n"
    "ds_read_b64_tr_b16 v[232:233], v147 offset:24576\n"
    "ds_read_b64_tr_b16 v[234:235], v147 offset:26624\n"
    "v_mfma_f32_16x16x32_bf16 v[80:83], v[212:215], a[8:11], v[80:83]\n"
    "s_add_u32 s73, s73, 28672\n"
    "v_rcp_f32 v161, v161\n"
    "v_mfma_f32_16x16x32_bf16 v[84:87], v[212:215], a[12:15], 0\n"
    "s_waitcnt vmcnt(7)\n"
    "s_barrier\n"
    "v_mfma_f32_16x16x32_bf16 v[84:87], v[208:211], a[4:7], v[84:87]\n"
    "s_waitcnt lgkmcnt(6)\n"
    "ds_read_b64_tr_b16 v[236:237], v147 offset:28672\n"
    "ds_read_b64_tr_b16 v[238:239], v147 offset:30720\n"
    "v_mul_f32 v2, v2, v160\n"
    "v_mul_f32 v3, v3, v161\n"
    "v_mul_f32 v160, v8, v8\n"
    "v_mul_f32 v161, v9, v9\n"
    "v_fma_f32 v160, v160, s50, v242\n"
    "v_fma_f32 v161, v161, s50, v242\n"
    "v_mul_f32 v160, v160, v8\n"
    "v_mul_f32 v161, v161, v9\n"
    "v_mfma_f32_16x16x32_bf16 v[116:119], v[216:219], a[4:7], 0\n"
    "ds_read_b64_tr_b16 v[208:209], v156 offset:16384\n"
    "ds_read_b64_tr_b16 v[210:211], v156 offset:18432\n"
    "v_mfma_f32_16x16x32_bf16 v[116:119], v[220:223], a[12:15], v[116:119]\n"
    "buffer_load_dwordx4 v134, s[80:83], s73 offen lds\n"
    "v_exp_f32 v160, v160\n"
    "v_exp_f32 v161, v161\n"
    "v_mul_f32 v8, v8, v40\n"
    "v_mul_f32 v9, v9, v41\n"
    "v_add_f32 v160, 1.0, v160\n"
    "v_add_f32 v161, 1.0, v161\n"
    "v_mfma_f32_16x16x32_bf16 v[112:115], v[220:223], a[8:11], 0\n"
    "s_add_u32 m0, s54, 81920\n"
    "ds_read_b64_tr_b16 v[212:213], v156 offset:20480\n"
    "ds_read_b64_tr_b16 v[214:215], v156 offset:22528\n"
    "v_mfma_f32_16x16x32_bf16 v[112:115], v[216:219], a[0:3], v[112:115]\n"
    "s_waitcnt lgkmcnt(8)\n"
    "v_rcp_f32 v160, v160\n"
    "v_rcp_f32 v161, v161\n"
    "v_mfma_f32_16x16x32_bf16 v[88:91], v[224:227], a[0:3], 0\n"
    "s_add_u32 s73, s73, 143360\n"
    "ds_read_b64_tr_b16 v[216:217], v156 offset:24576\n"
    "ds_read_b64_tr_b16 v[218:219], v156 offset:26624\n"
    "v_mfma_f32_16x16x32_bf16 v[88:91], v[228:231], a[8:11], v[88:91]\n"
    "buffer_load_dwordx4 v134, s[80:83], s73 offen lds\n"
    "v_mul_f32 v8, v8, v160\n"
    "v_mul_f32 v9, v9, v161\n"
    "v_mul_f32 v160, v10, v10\n"
    "v_mul_f32 v161, v11, v11\n"
    "v_fma_f32 v160, v160, s50, v242\n"
    "v_fma_f32 v161, v161, s50, v242\n"
    "v_mul_f32 v160, v160, v10\n"
    "v_mul_f32 v161, v161, v11\n"
    "v_mfma_f32_16x16x32_bf16 v[92:95], v[228:231], a[12:15], 0\n"
    "s_add_u32 m0, s54, 82944\n"
    "ds_read_b64_tr_b16 v[220:221], v156 offset:28672\n"
    "ds_read_b64_tr_b16 v[222:223], v156 offset:30720\n"
    "v_mfma_f32_16x16x32_bf16 v[92:95], v[224:227], a[4:7], v[92:95]\n"
    "s_waitcnt lgkmcnt(8)\n"
    "v_exp_f32 v160, v160\n"
    "v_exp_f32 v161, v161\n"
    "v_mul_f32 v10, v10, v42\n"
    "v_mul_f32 v11, v11, v43\n"
    "v_add_f32 v160, 1.0, v160\n"
    "v_add_f32 v161, 1.0, v161\n"
    "v_rcp_f32 v160, v160\n"
    "v_mfma_f32_16x16x32_bf16 v[124:127], v[232:235], a[4:7], 0\n"
    "s_add_u32 s73, s73, 28672\n"
    "ds_read_b64_tr_b16 v[224:225], v157 offset:16384\n"
    "ds_read_b64_tr_b16 v[226:227], v157 offset:18432\n"
    "v_mfma_f32_16x16x32_bf16 v[124:127], v[236:239], a[12:15], v[124:127]\n"
    "buffer_load_dwordx4 v134, s[80:83], s73 offen lds\n"
    "v_rcp_f32 v161, v161\n"
    "v_mfma_f32_16x16x32_bf16 v[120:123], v[236:239], a[8:11], 0\n"
    "s_add_u32 m0, s54, 83968\n"
    "ds_read_b64_tr_b16 v[228:229], v157 offset:20480\n"
    "ds_read_b64_tr_b16 v[230:231], v157 offset:22528\n"
    "v_mfma_f32_16x16x32_bf16 v[120:123], v[232:235], a[0:3], v[120:123]\n"
    "s_waitcnt lgkmcnt(8)\n"
    "v_mul_f32 v10, v10, v160\n"
    "v_mul_f32 v11, v11, v161\n"
    "v_cvt_pk_bf16_f32 v192, v0, v1\n"
    "v_cvt_pk_bf16_f32 v193, v2, v3\n"
    "v_cvt_pk_bf16_f32 v194, v8, v9\n"
    "v_cvt_pk_bf16_f32 v195, v10, v11\n"
    "buffer_store_dwordx4 v[192:195], v254, s[76:79], s57 offen offset:0 sc1\n"
    "v_mul_f32 v160, v4, v4\n"
    "v_mul_f32 v161, v5, v5\n"
    "v_fma_f32 v160, v160, s50, v242\n"
    "v_mfma_f32_16x16x32_bf16 v[64:67], v[208:211], a[16:19], v[64:67]\n"
    "s_add_u32 s73, s73, 28672\n"
    "ds_read_b64_tr_b16 v[232:233], v157 offset:24576\n"
    "ds_read_b64_tr_b16 v[234:235], v157 offset:26624\n"
    "v_mfma_f32_16x16x32_bf16 v[64:67], v[212:215], a[24:27], v[64:67]\n"
    "buffer_load_dwordx4 v134, s[80:83], s73 offen lds\n"
    "v_fma_f32 v161, v161, s50, v242\n"
    "v_mul_f32 v160, v160, v4\n"
    "v_mul_f32 v161, v161, v5\n"
    "v_exp_f32 v160, v160\n"
    "v_exp_f32 v161, v161\n"
    "v_mul_f32 v4, v4, v36\n"
    "v_mul_f32 v5, v5, v37\n"
    "v_mfma_f32_16x16x32_bf16 v[68:71], v[212:215], a[28:31], v[68:71]\n"
    "s_add_u32 m0, s54, 84992\n"
    "ds_read_b64_tr_b16 v[236:237], v157 offset:28672\n"
    "ds_read_b64_tr_b16 v[238:239], v157 offset:30720\n"
    "v_mfma_f32_16x16x32_bf16 v[68:71], v[208:211], a[20:23], v[68:71]\n"
    "s_waitcnt lgkmcnt(8)\n"
    "v_add_f32 v160, 1.0, v160\n"
    "v_add_f32 v161, 1.0, v161\n"
    "v_rcp_f32 v160, v160\n"
    "v_rcp_f32 v161, v161\n"
    "v_mfma_f32_16x16x32_bf16 v[100:103], v[216:219], a[20:23], v[100:103]\n"
    "s_add_u32 s73, s73, 28672\n"
    "ds_read_b64_tr_b16 v[208:209], v158 offset:16384\n"
    "ds_read_b64_tr_b16 v[210:211], v158 offset:18432\n"
    "v_mfma_f32_16x16x32_bf16 v[100:103], v[220:223], a[28:31], v[100:103]\n"
    "buffer_load_dwordx4 v134, s[80:83], s73 offen lds\n"
    "v_mul_f32 v4, v4, v160\n"
    "v_mul_f32 v5, v5, v161\n"
    "v_mul_f32 v160, v6, v6\n"
    "v_mul_f32 v161, v7, v7\n"
    "v_fma_f32 v160, v160, s50, v242\n"
    "v_fma_f32 v161, v161, s50, v242\n"
    "v_mul_f32 v160, v160, v6\n"
    "v_mul_f32 v161, v161, v7\n"
    "v_mfma_f32_16x16x32_bf16 v[96:99], v[220:223], a[24:27], v[96:99]\n"
    "s_add_u32 m0, s64, 98304\n"
    "ds_read_b64_tr_b16 v[212:213], v158 offset:20480\n"
    "ds_read_b64_tr_b16 v[214:215], v158 offset:22528\n"
    "v_mfma_f32_16x16x32_bf16 v[96:99], v[216:219], a[16:19], v[96:99]\n"
    "s_waitcnt lgkmcnt(8)\n"
    "v_exp_f32 v160, v160\n"
    "v_exp_f32 v161, v161\n"
    "v_mul_f32 v6, v6, v38\n"
    "v_mul_f32 v7, v7, v39\n"
    "v_add_f32 v160, 1.0, v160\n"
    "v_add_f32 v161, 1.0, v161\n"
    "v_rcp_f32 v160, v160\n"
    "v_mfma_f32_16x16x32_bf16 v[72:75], v[224:227], a[16:19], v[72:75]\n"
    "s_add_u32 s52, s52, 78848\n"
    "ds_read_b64_tr_b16 v[216:217], v158 offset:24576\n"
    "ds_read_b64_tr_b16 v[218:219], v158 offset:26624\n"
    "v_mfma_f32_16x16x32_bf16 v[72:75], v[228:231], a[24:27], v[72:75]\n"
    "buffer_load_dwordx4 v255, s[84:87], s52 offen lds\n"
    "v_rcp_f32 v161, v161\n"
    "v_mfma_f32_16x16x32_bf16 v[76:79], v[228:231], a[28:31], v[76:79]\n"
    "s_add_u32 m0, s64, 99328\n"
    "ds_read_b64_tr_b16 v[220:221], v158 offset:28672\n"
    "ds_read_b64_tr_b16 v[222:223], v158 offset:30720\n"
    "v_mfma_f32_16x16x32_bf16 v[76:79], v[224:227], a[20:23], v[76:79]\n"
    "s_waitcnt lgkmcnt(8)\n"
    "v_mul_f32 v6, v6, v160\n"
    "v_mul_f32 v7, v7, v161\n"
    "v_mul_f32 v160, v12, v12\n"
    "v_mul_f32 v161, v13, v13\n"
    "v_fma_f32 v160, v160, s50, v242\n"
    "v_fma_f32 v161, v161, s50, v242\n"
    "v_mul_f32 v160, v160, v12\n"
    "v_mul_f32 v161, v161, v13\n"
    "v_exp_f32 v160, v160\n"
    "v_mfma_f32_16x16x32_bf16 v[108:111], v[232:235], a[20:23], v[108:111]\n"
    "s_add_u32 s52, s52, 7168\n"
    "ds_read_b64_tr_b16 v[224:225], v159 offset:16384\n"
    "ds_read_b64_tr_b16 v[226:227], v159 offset:18432\n"
    "v_mfma_f32_16x16x32_bf16 v[108:111], v[236:239], a[28:31], v[108:111]\n"
    "buffer_load_dwordx4 v255, s[84:87], s52 offen lds\n"
    "v_exp_f32 v161, v161\n"
    "v_mul_f32 v12, v12, v44\n"
    "v_mul_f32 v13, v13, v45\n"
    "v_add_f32 v160, 1.0, v160\n"
    "v_add_f32 v161, 1.0, v161\n"
    "v_rcp_f32 v160, v160\n"
    "v_mfma_f32_16x16x32_bf16 v[104:107], v[236:239], a[24:27], v[104:107]\n"
    "s_add_u32 m0, s64, 106496\n"
    "ds_read_b64_tr_b16 v[228:229], v159 offset:20480\n"
    "ds_read_b64_tr_b16 v[230:231], v159 offset:22528\n"
    "v_mfma_f32_16x16x32_bf16 v[104:107], v[232:235], a[16:19], v[104:107]\n"
    "s_waitcnt lgkmcnt(8)\n"
    "v_rcp_f32 v161, v161\n"
    "v_mfma_f32_16x16x32_bf16 v[80:83], v[208:211], a[16:19], v[80:83]\n"
    "s_add_u32 s52, s52, 21504\n"
    "ds_read_b64_tr_b16 v[232:233], v159 offset:24576\n"
    "ds_read_b64_tr_b16 v[234:235], v159 offset:26624\n"
    "v_mfma_f32_16x16x32_bf16 v[80:83], v[212:215], a[24:27], v[80:83]\n"
    "buffer_load_dwordx4 v255, s[84:87], s52 offen lds\n"
    "v_mul_f32 v12, v12, v160\n"
    "v_mul_f32 v13, v13, v161\n"
    "v_mul_f32 v160, v14, v14\n"
    "v_mul_f32 v161, v15, v15\n"
    "v_fma_f32 v160, v160, s50, v242\n"
    "v_fma_f32 v161, v161, s50, v242\n"
    "v_mul_f32 v160, v160, v14\n"
    "v_mul_f32 v161, v161, v15\n"
    "v_mfma_f32_16x16x32_bf16 v[84:87], v[212:215], a[28:31], v[84:87]\n"
    "s_add_u32 m0, s64, 107520\n"
    "ds_read_b64_tr_b16 v[236:237], v159 offset:28672\n"
    "ds_read_b64_tr_b16 v[238:239], v159 offset:30720\n"
    "v_mfma_f32_16x16x32_bf16 v[84:87], v[208:211], a[20:23], v[84:87]\n"
    "s_waitcnt lgkmcnt(8)\n"
    "v_exp_f32 v160, v160\n"
    "v_exp_f32 v161, v161\n"
    "v_mul_f32 v14, v14, v46\n"
    "v_mul_f32 v15, v15, v47\n"
    "v_add_f32 v160, 1.0, v160\n"
    "v_add_f32 v161, 1.0, v161\n"
    "v_rcp_f32 v160, v160\n"
    "v_mfma_f32_16x16x32_bf16 v[116:119], v[216:219], a[20:23], v[116:119]\n"
    "s_add_u32 s52, s52, 7168\n"
    "ds_read_b64_tr_b16 v[208:209], v148 offset:49152\n"
    "ds_read_b64_tr_b16 v[210:211], v148 offset:51200\n"
    "v_mfma_f32_16x16x32_bf16 v[116:119], v[220:223], a[28:31], v[116:119]\n"
    "buffer_load_dwordx4 v255, s[84:87], s52 offen lds\n"
    "v_rcp_f32 v161, v161\n"
    "v_mfma_f32_16x16x32_bf16 v[112:115], v[220:223], a[24:27], v[112:115]\n"
    "s_add_u32 m0, s64, 131072\n"
    "ds_read_b64_tr_b16 v[212:213], v148 offset:53248\n"
    "ds_read_b64_tr_b16 v[214:215], v148 offset:55296\n"
    "v_mfma_f32_16x16x32_bf16 v[112:115], v[216:219], a[16:19], v[112:115]\n"
    "s_waitcnt lgkmcnt(8)\n"
    "v_mul_f32 v14, v14, v160\n"
    "v_mul_f32 v15, v15, v161\n"
    "v_cvt_pk_bf16_f32 v196, v4, v5\n"
    "v_cvt_pk_bf16_f32 v197, v6, v7\n"
    "v_cvt_pk_bf16_f32 v198, v12, v13\n"
    "v_cvt_pk_bf16_f32 v199, v14, v15\n"
    "buffer_store_dwordx4 v[196:199], v254, s[76:79], s58 offen offset:0 sc1\n"
    "v_mul_f32 v160, v16, v16\n"
    "v_mul_f32 v161, v17, v17\n"
    "v_fma_f32 v160, v160, s50, v242\n"
    "v_mfma_f32_16x16x32_bf16 v[88:91], v[224:227], a[16:19], v[88:91]\n"
    "s_add_u32 s52, s52, 78848\n"
    "ds_read_b64_tr_b16 v[216:217], v148 offset:57344\n"
    "ds_read_b64_tr_b16 v[218:219], v148 offset:59392\n"
    "v_mfma_f32_16x16x32_bf16 v[88:91], v[228:231], a[24:27], v[88:91]\n"
    "buffer_load_dwordx4 v255, s[84:87], s52 offen lds\n"
    "v_fma_f32 v161, v161, s50, v242\n"
    "v_mul_f32 v160, v160, v16\n"
    "v_mul_f32 v161, v161, v17\n"
    "v_exp_f32 v160, v160\n"
    "v_exp_f32 v161, v161\n"
    "v_mul_f32 v16, v16, v48\n"
    "v_mul_f32 v17, v17, v49\n"
    "v_mfma_f32_16x16x32_bf16 v[92:95], v[228:231], a[28:31], v[92:95]\n"
    "s_add_u32 m0, s64, 132096\n"
    "ds_read_b64_tr_b16 v[220:221], v148 offset:61440\n"
    "ds_read_b64_tr_b16 v[222:223], v148 offset:63488\n"
    "v_mfma_f32_16x16x32_bf16 v[92:95], v[224:227], a[20:23], v[92:95]\n"
    "s_waitcnt lgkmcnt(8)\n"
    "v_add_f32 v160, 1.0, v160\n"
    "v_add_f32 v161, 1.0, v161\n"
    "v_rcp_f32 v160, v160\n"
    "v_rcp_f32 v161, v161\n"
    "v_mfma_f32_16x16x32_bf16 v[124:127], v[232:235], a[20:23], v[124:127]\n"
    "s_add_u32 s52, s52, 7168\n"
    "ds_read_b64_tr_b16 v[224:225], v149 offset:49152\n"
    "ds_read_b64_tr_b16 v[226:227], v149 offset:51200\n"
    "v_mfma_f32_16x16x32_bf16 v[124:127], v[236:239], a[28:31], v[124:127]\n"
    "buffer_load_dwordx4 v255, s[84:87], s52 offen lds\n"
    "v_mul_f32 v16, v16, v160\n"
    "v_mul_f32 v17, v17, v161\n"
    "v_mul_f32 v160, v18, v18\n"
    "v_mul_f32 v161, v19, v19\n"
    "v_fma_f32 v160, v160, s50, v242\n"
    "v_fma_f32 v161, v161, s50, v242\n"
    "v_mul_f32 v160, v160, v18\n"
    "v_mul_f32 v161, v161, v19\n"
    "v_mfma_f32_16x16x32_bf16 v[120:123], v[236:239], a[24:27], v[120:123]\n"
    "s_add_u32 m0, s64, 139264\n"
    "ds_read_b64_tr_b16 v[228:229], v149 offset:53248\n"
    "ds_read_b64_tr_b16 v[230:231], v149 offset:55296\n"
    "v_mfma_f32_16x16x32_bf16 v[120:123], v[232:235], a[16:19], v[120:123]\n"
    "s_waitcnt lgkmcnt(8)\n"
    "v_exp_f32 v160, v160\n"
    "v_exp_f32 v161, v161\n"
    "v_mul_f32 v18, v18, v50\n"
    "v_mul_f32 v19, v19, v51\n"
    "v_add_f32 v160, 1.0, v160\n"
    "v_add_f32 v161, 1.0, v161\n"
    "v_rcp_f32 v160, v160\n"
    "v_mfma_f32_16x16x32_bf16 v[64:67], v[208:211], a[32:35], v[64:67]\n"
    "s_add_u32 s52, s52, 21504\n"
    "ds_read_b64_tr_b16 v[232:233], v149 offset:57344\n"
    "ds_read_b64_tr_b16 v[234:235], v149 offset:59392\n"
    "v_mfma_f32_16x16x32_bf16 v[64:67], v[212:215], a[40:43], v[64:67]\n"
    "buffer_load_dwordx4 v255, s[84:87], s52 offen lds\n"
    "v_rcp_f32 v161, v161\n"
    "v_mfma_f32_16x16x32_bf16 v[68:71], v[212:215], a[44:47], v[68:71]\n"
    "s_add_u32 m0, s64, 140288\n"
    "ds_read_b64_tr_b16 v[236:237], v149 offset:61440\n"
    "ds_read_b64_tr_b16 v[238:239], v149 offset:63488\n"
    "v_mfma_f32_16x16x32_bf16 v[68:71], v[208:211], a[36:39], v[68:71]\n"
    "s_waitcnt lgkmcnt(8)\n"
    "v_mul_f32 v18, v18, v160\n"
    "v_mul_f32 v19, v19, v161\n"
    "v_mul_f32 v160, v24, v24\n"
    "v_mul_f32 v161, v25, v25\n"
    "v_fma_f32 v160, v160, s50, v242\n"
    "v_fma_f32 v161, v161, s50, v242\n"
    "v_mul_f32 v160, v160, v24\n"
    "v_mul_f32 v161, v161, v25\n"
    "v_exp_f32 v160, v160\n"
    "v_mfma_f32_16x16x32_bf16 v[100:103], v[216:219], a[36:39], v[100:103]\n"
    "s_add_u32 s52, s52, 7168\n"
    "ds_read_b64_tr_b16 v[208:209], v150 offset:49152\n"
    "ds_read_b64_tr_b16 v[210:211], v150 offset:51200\n"
    "v_mfma_f32_16x16x32_bf16 v[100:103], v[220:223], a[44:47], v[100:103]\n"
    "buffer_load_dwordx4 v255, s[84:87], s52 offen lds\n"
    "v_exp_f32 v161, v161\n"
    "v_mul_f32 v24, v24, v56\n"
    "v_mul_f32 v25, v25, v57\n"
    "v_add_f32 v160, 1.0, v160\n"
    "v_add_f32 v161, 1.0, v161\n"
    "v_rcp_f32 v160, v160\n"
    "v_mfma_f32_16x16x32_bf16 v[96:99], v[220:223], a[40:43], v[96:99]\n"
    "s_add_u32 m0, s64, 0\n"
    "ds_read_b64_tr_b16 v[212:213], v150 offset:53248\n"
    "ds_read_b64_tr_b16 v[214:215], v150 offset:55296\n"
    "v_mfma_f32_16x16x32_bf16 v[96:99], v[216:219], a[32:35], v[96:99]\n"
    "s_waitcnt lgkmcnt(8)\n"
    "v_rcp_f32 v161, v161\n"
    "v_mfma_f32_16x16x32_bf16 v[72:75], v[224:227], a[32:35], v[72:75]\n"
    "s_add_u32 s52, s52, 78848\n"
    "ds_read_b64_tr_b16 v[216:217], v150 offset:57344\n"
    "ds_read_b64_tr_b16 v[218:219], v150 offset:59392\n"
    "v_mfma_f32_16x16x32_bf16 v[72:75], v[228:231], a[40:43], v[72:75]\n"
    "buffer_load_dwordx4 v255, s[84:87], s52 offen lds\n"
    "v_mul_f32 v24, v24, v160\n"
    "v_mul_f32 v25, v25, v161\n"
    "v_mul_f32 v160, v26, v26\n"
    "v_mul_f32 v161, v27, v27\n"
    "v_fma_f32 v160, v160, s50, v242\n"
    "v_fma_f32 v161, v161, s50, v242\n"
    "v_mul_f32 v160, v160, v26\n"
    "v_mul_f32 v161, v161, v27\n"
    "v_mfma_f32_16x16x32_bf16 v[76:79], v[228:231], a[44:47], v[76:79]\n"
    "s_add_u32 m0, s64, 1024\n"
    "ds_read_b64_tr_b16 v[220:221], v150 offset:61440\n"
    "ds_read_b64_tr_b16 v[222:223], v150 offset:63488\n"
    "v_mfma_f32_16x16x32_bf16 v[76:79], v[224:227], a[36:39], v[76:79]\n"
    "s_waitcnt lgkmcnt(8)\n"
    "v_exp_f32 v160, v160\n"
    "v_exp_f32 v161, v161\n"
    "v_mul_f32 v26, v26, v58\n"
    "v_mul_f32 v27, v27, v59\n"
    "v_add_f32 v160, 1.0, v160\n"
    "v_add_f32 v161, 1.0, v161\n"
    "v_rcp_f32 v160, v160\n"
    "v_mfma_f32_16x16x32_bf16 v[108:111], v[232:235], a[36:39], v[108:111]\n"
    "s_add_u32 s52, s52, 7168\n"
    "ds_read_b64_tr_b16 v[224:225], v151 offset:49152\n"
    "ds_read_b64_tr_b16 v[226:227], v151 offset:51200\n"
    "v_mfma_f32_16x16x32_bf16 v[108:111], v[236:239], a[44:47], v[108:111]\n"
    "buffer_load_dwordx4 v255, s[84:87], s52 offen lds\n"
    "v_rcp_f32 v161, v161\n"
    "v_mfma_f32_16x16x32_bf16 v[104:107], v[236:239], a[40:43], v[104:107]\n"
    "s_add_u32 m0, s64, 8192\n"
    "ds_read_b64_tr_b16 v[228:229], v151 offset:53248\n"
    "ds_read_b64_tr_b16 v[230:231], v151 offset:55296\n"
    "v_mfma_f32_16x16x32_bf16 v[104:107], v[232:235], a[32:35], v[104:107]\n"
    "s_waitcnt lgkmcnt(8)\n"
    "v_mul_f32 v26, v26, v160\n"
    "v_mul_f32 v27, v27, v161\n"
    "v_cvt_pk_bf16_f32 v200, v16, v17\n"
    "v_cvt_pk_bf16_f32 v201, v18, v19\n"
    "v_cvt_pk_bf16_f32 v202, v24, v25\n"
    "v_cvt_pk_bf16_f32 v203, v26, v27\n"
    "buffer_store_dwordx4 v[200:203], v254, s[76:79], s57 offen offset:1024 sc1\n"
    "v_mul_f32 v160, v20, v20\n"
    "v_mul_f32 v161, v21, v21\n"
    "v_fma_f32 v160, v160, s50, v242\n"
    "v_mfma_f32_16x16x32_bf16 v[80:83], v[208:211], a[32:35], v[80:83]\n"
    "s_add_u32 s52, s52, 21504\n"
    "ds_read_b64_tr_b16 v[232:233], v151 offset:57344\n"
    "ds_read_b64_tr_b16 v[234:235], v151 offset:59392\n"
    "v_mfma_f32_16x16x32_bf16 v[80:83], v[212:215], a[40:43], v[80:83]\n"
    "buffer_load_dwordx4 v255, s[84:87], s52 offen lds\n"
    "v_fma_f32 v161, v161, s50, v242\n"
    "v_mul_f32 v160, v160, v20\n"
    "v_mul_f32 v161, v161, v21\n"
    "v_exp_f32 v160, v160\n"
    "v_exp_f32 v161, v161\n"
    "v_mul_f32 v20, v20, v52\n"
    "v_mul_f32 v21, v21, v53\n"
    "v_mfma_f32_16x16x32_bf16 v[84:87], v[212:215], a[44:47], v[84:87]\n"
    "s_add_u32 m0, s64, 9216\n"
    "ds_read_b64_tr_b16 v[236:237], v151 offset:61440\n"
    "ds_read_b64_tr_b16 v[238:239], v151 offset:63488\n"
    "v_mfma_f32_16x16x32_bf16 v[84:87], v[208:211], a[36:39], v[84:87]\n"
    "s_waitcnt lgkmcnt(8)\n"
    "v_add_f32 v160, 1.0, v160\n"
    "v_add_f32 v161, 1.0, v161\n"
    "v_rcp_f32 v160, v160\n"
    "v_rcp_f32 v161, v161\n"
    "v_mfma_f32_16x16x32_bf16 v[116:119], v[216:219], a[36:39], v[116:119]\n"
    "s_add_u32 s52, s52, 7168\n"
    "ds_read_b64_tr_b16 v[208:209], v144 offset:32768\n"
    "ds_read_b64_tr_b16 v[210:211], v144 offset:34816\n"
    "v_mfma_f32_16x16x32_bf16 v[116:119], v[220:223], a[44:47], v[116:119]\n"
    "buffer_load_dwordx4 v255, s[84:87], s52 offen lds\n"
    "v_mul_f32 v20, v20, v160\n"
    "v_mul_f32 v21, v21, v161\n"
    "v_mul_f32 v160, v22, v22\n"
    "v_mul_f32 v161, v23, v23\n"
    "v_fma_f32 v160, v160, s50, v242\n"
    "v_fma_f32 v161, v161, s50, v242\n"
    "v_mul_f32 v160, v160, v22\n"
    "v_mul_f32 v161, v161, v23\n"
    "v_mfma_f32_16x16x32_bf16 v[112:115], v[220:223], a[40:43], v[112:115]\n"
    "ds_read_b64_tr_b16 v[212:213], v144 offset:36864\n"
    "ds_read_b64_tr_b16 v[214:215], v144 offset:38912\n"
    "v_mfma_f32_16x16x32_bf16 v[112:115], v[216:219], a[32:35], v[112:115]\n"
    "s_waitcnt lgkmcnt(8)\n"
    "v_exp_f32 v160, v160\n"
    "v_exp_f32 v161, v161\n"
    "v_mul_f32 v22, v22, v54\n"
    "v_mul_f32 v23, v23, v55\n"
    "v_add_f32 v160, 1.0, v160\n"
    "v_add_f32 v161, 1.0, v161\n"
    "v_rcp_f32 v160, v160\n"
    "v_mfma_f32_16x16x32_bf16 v[88:91], v[224:227], a[32:35], v[88:91]\n"
    "ds_read_b64_tr_b16 v[216:217], v144 offset:40960\n"
    "ds_read_b64_tr_b16 v[218:219], v144 offset:43008\n"
    "v_mfma_f32_16x16x32_bf16 v[88:91], v[228:231], a[40:43], v[88:91]\n"
    "v_rcp_f32 v161, v161\n"
    "v_mfma_f32_16x16x32_bf16 v[92:95], v[228:231], a[44:47], v[92:95]\n"
    "ds_read_b64_tr_b16 v[220:221], v144 offset:45056\n"
    "ds_read_b64_tr_b16 v[222:223], v144 offset:47104\n"
    "v_mfma_f32_16x16x32_bf16 v[92:95], v[224:227], a[36:39], v[92:95]\n"
    "s_waitcnt lgkmcnt(8)\n"
    "v_mul_f32 v22, v22, v160\n"
    "v_mul_f32 v23, v23, v161\n"
    "v_mul_f32 v160, v28, v28\n"
    "v_mul_f32 v161, v29, v29\n"
    "v_fma_f32 v160, v160, s50, v242\n"
    "v_fma_f32 v161, v161, s50, v242\n"
    "v_mul_f32 v160, v160, v28\n"
    "v_mul_f32 v161, v161, v29\n"
    "v_exp_f32 v160, v160\n"
    "v_mfma_f32_16x16x32_bf16 v[124:127], v[232:235], a[36:39], v[124:127]\n"
    "ds_read_b64_tr_b16 v[224:225], v145 offset:32768\n"
    "ds_read_b64_tr_b16 v[226:227], v145 offset:34816\n"
    "v_mfma_f32_16x16x32_bf16 v[124:127], v[236:239], a[44:47], v[124:127]\n"
    "v_exp_f32 v161, v161\n"
    "v_mul_f32 v28, v28, v60\n"
    "v_mul_f32 v29, v29, v61\n"
    "v_add_f32 v160, 1.0, v160\n"
    "v_add_f32 v161, 1.0, v161\n"
    "v_rcp_f32 v160, v160\n"
    "v_rcp_f32 v161, v161\n"
    "v_mfma_f32_16x16x32_bf16 v[120:123], v[236:239], a[40:43], v[120:123]\n"
    "ds_read_b64_tr_b16 v[228:229], v145 offset:36864\n"
    "ds_read_b64_tr_b16 v[230:231], v145 offset:38912\n"
    "v_mfma_f32_16x16x32_bf16 v[120:123], v[232:235], a[32:35], v[120:123]\n"
    "s_waitcnt lgkmcnt(8)\n"
    "v_mul_f32 v28, v28, v160\n"
    "v_mul_f32 v29, v29, v161\n"
    "v_mul_f32 v160, v30, v30\n"
    "v_mul_f32 v161, v31, v31\n"
    "v_fma_f32 v160, v160, s50, v242\n"
    "v_fma_f32 v161, v161, s50, v242\n"
    "v_mul_f32 v160, v160, v30\n"
    "v_mul_f32 v161, v161, v31\n"
    "v_exp_f32 v160, v160\n"
    "v_mfma_f32_16x16x32_bf16 v[64:67], v[208:211], a[48:51], v[64:67]\n"
    "ds_read_b64_tr_b16 v[232:233], v145 offset:40960\n"
    "ds_read_b64_tr_b16 v[234:235], v145 offset:43008\n"
    "v_mfma_f32_16x16x32_bf16 v[64:67], v[212:215], a[56:59], v[64:67]\n"
    "s_waitcnt vmcnt(14)\n"
    "s_barrier\n"
    "v_mfma_f32_16x16x32_bf16 v[68:71], v[212:215], a[60:63], v[68:71]\n"
    "s_add_u32 m0, s64, 16384\n"
    "ds_read_b64_tr_b16 v[236:237], v145 offset:45056\n"
    "ds_read_b64_tr_b16 v[238:239], v145 offset:47104\n"
    "v_mfma_f32_16x16x32_bf16 v[68:71], v[208:211], a[52:55], v[68:71]\n"
    "s_waitcnt lgkmcnt(8)\n"
    "v_exp_f32 v161, v161\n"
    "v_mul_f32 v30, v30, v62\n"
    "v_mul_f32 v31, v31, v63\n"
    "v_add_f32 v160, 1.0, v160\n"
    "v_add_f32 v161, 1.0, v161\n"
    "v_rcp_f32 v160, v160\n"
    "v_rcp_f32 v161, v161\n"
    "v_mfma_f32_16x16x32_bf16 v[100:103], v[216:219], a[52:55], v[100:103]\n"
    "s_sub_u32 s52, s52, 666624\n"
    "ds_read_b64_tr_b16 v[208:209], v146 offset:32768\n"
    "v_mul_f32 v30, v30, v160\n"
    "v_mfma_f32_16x16x32_bf16 v[100:103], v[220:223], a[60:63], v[100:103]\n"
    "ds_read_b64_tr_b16 v[210:211], v146 offset:34816\n"
    "buffer_load_dwordx4 v255, s[84:87], s52 offen lds\n"
    "v_mul_f32 v31, v31, v161\n"
    "v_cvt_pk_bf16_f32 v204, v20, v21\n"
    "v_cvt_pk_bf16_f32 v205, v22, v23\n"
    "v_cvt_pk_bf16_f32 v206, v28, v29\n"
    "v_cvt_pk_bf16_f32 v207, v30, v31\n"
    "buffer_store_dwordx4 v[204:207], v254, s[76:79], s58 offen offset:1024 sc1\n"
    "v_mfma_f32_16x16x32_bf16 v[96:99], v[220:223], a[56:59], v[96:99]\n"
    "s_add_u32 m0, s64, 17408\n"
    "ds_read_b64_tr_b16 v[212:213], v146 offset:36864\n"
    "v_mfma_f32_16x16x32_bf16 v[96:99], v[216:219], a[48:51], v[96:99]\n"
    "s_waitcnt lgkmcnt(7)\n"
    "ds_read_b64_tr_b16 v[214:215], v146 offset:38912\n"
    "v_mfma_f32_16x16x32_bf16 v[72:75], v[224:227], a[48:51], v[72:75]\n"
    "s_add_u32 s52, s52, 7168\n"
    "ds_read_b64_tr_b16 v[216:217], v146 offset:40960\n"
    "v_mfma_f32_16x16x32_bf16 v[72:75], v[228:231], a[56:59], v[72:75]\n"
    "ds_read_b64_tr_b16 v[218:219], v146 offset:43008\n"
    "buffer_load_dwordx4 v255, s[84:87], s52 offen lds\n"
    "v_mfma_f32_16x16x32_bf16 v[76:79], v[228:231], a[60:63], v[76:79]\n"
    "s_add_u32 m0, s64, 24576\n"
    "ds_read_b64_tr_b16 v[220:221], v146 offset:45056\n"
    "v_mfma_f32_16x16x32_bf16 v[76:79], v[224:227], a[52:55], v[76:79]\n"
    "s_waitcnt lgkmcnt(7)\n"
    "ds_read_b64_tr_b16 v[222:223], v146 offset:47104\n"
    "v_mfma_f32_16x16x32_bf16 v[108:111], v[232:235], a[52:55], v[108:111]\n"
    "s_add_u32 s52, s52, 21504\n"
    "ds_read_b64_tr_b16 v[224:225], v147 offset:32768\n"
    "v_mfma_f32_16x16x32_bf16 v[108:111], v[236:239], a[60:63], v[108:111]\n"
    "ds_read_b64_tr_b16 v[226:227], v147 offset:34816\n"
    "buffer_load_dwordx4 v255, s[84:87], s52 offen lds\n"
    "v_mfma_f32_16x16x32_bf16 v[104:107], v[236:239], a[56:59], v[104:107]\n"
    "s_add_u32 m0, s64, 25600\n"
    "ds_read_b64_tr_b16 v[228:229], v147 offset:36864\n"
    "v_mfma_f32_16x16x32_bf16 v[104:107], v[232:235], a[48:51], v[104:107]\n"
    "s_waitcnt lgkmcnt(7)\n"
    "ds_read_b64_tr_b16 v[230:231], v147 offset:38912\n"
    "v_mfma_f32_16x16x32_bf16 v[80:83], v[208:211], a[48:51], v[80:83]\n"
    "s_add_u32 s52, s52, 7168\n"
    "ds_read_b64_tr_b16 v[232:233], v147 offset:40960\n"
    "v_mfma_f32_16x16x32_bf16 v[80:83], v[212:215], a[56:59], v[80:83]\n"
    "ds_read_b64_tr_b16 v[234:235], v147 offset:43008\n"
    "buffer_load_dwordx4 v255, s[84:87], s52 offen lds\n"
    "v_mfma_f32_16x16x32_bf16 v[84:87], v[212:215], a[60:63], v[84:87]\n"
    "s_add_u32 m0, s64, 147456\n"
    "ds_read_b64_tr_b16 v[236:237], v147 offset:45056\n"
    "v_mfma_f32_16x16x32_bf16 v[84:87], v[208:211], a[52:55], v[84:87]\n"
    "s_waitcnt lgkmcnt(7)\n"
    "ds_read_b64_tr_b16 v[238:239], v147 offset:47104\n"
    "v_mfma_f32_16x16x32_bf16 v[116:119], v[216:219], a[52:55], v[116:119]\n"
    "s_add_u32 s52, s52, 78848\n"
    "ds_read_b64_tr_b16 v[208:209], v144 offset:49152\n"
    "v_mfma_f32_16x16x32_bf16 v[116:119], v[220:223], a[60:63], v[116:119]\n"
    "ds_read_b64_tr_b16 v[210:211], v144 offset:51200\n"
    "buffer_load_dwordx4 v255, s[84:87], s52 offen lds\n"
    "v_mfma_f32_16x16x32_bf16 v[112:115], v[220:223], a[56:59], v[112:115]\n"
    "s_add_u32 m0, s64, 148480\n"
    "ds_read_b64_tr_b16 v[212:213], v144 offset:53248\n"
    "v_mfma_f32_16x16x32_bf16 v[112:115], v[216:219], a[48:51], v[112:115]\n"
    "s_waitcnt lgkmcnt(7)\n"
    "ds_read_b64_tr_b16 v[214:215], v144 offset:55296\n"
    "v_mfma_f32_16x16x32_bf16 v[88:91], v[224:227], a[48:51], v[88:91]\n"
    "s_add_u32 s52, s52, 7168\n"
    "ds_read_b64_tr_b16 v[216:217], v144 offset:57344\n"
    "v_mfma_f32_16x16x32_bf16 v[88:91], v[228:231], a[56:59], v[88:91]\n"
    "ds_read_b64_tr_b16 v[218:219], v144 offset:59392\n"
    "buffer_load_dwordx4 v255, s[84:87], s52 offen lds\n"
    "v_mfma_f32_16x16x32_bf16 v[92:95], v[228:231], a[60:63], v[92:95]\n"
    "s_add_u32 m0, s64, 155648\n"
    "ds_read_b64_tr_b16 v[220:221], v144 offset:61440\n"
    "v_mfma_f32_16x16x32_bf16 v[92:95], v[224:227], a[52:55], v[92:95]\n"
    "s_waitcnt lgkmcnt(7)\n"
    "ds_read_b64_tr_b16 v[222:223], v144 offset:63488\n"
    "v_mfma_f32_16x16x32_bf16 v[124:127], v[232:235], a[52:55], v[124:127]\n"
    "s_add_u32 s52, s52, 21504\n"
    "ds_read_b64_tr_b16 v[224:225], v145 offset:49152\n"
    "v_mfma_f32_16x16x32_bf16 v[124:127], v[236:239], a[60:63], v[124:127]\n"
    "ds_read_b64_tr_b16 v[226:227], v145 offset:51200\n"
    "buffer_load_dwordx4 v255, s[84:87], s52 offen lds\n"
    "v_mfma_f32_16x16x32_bf16 v[120:123], v[236:239], a[56:59], v[120:123]\n"
    "s_add_u32 m0, s64, 156672\n"
    "ds_read_b64_tr_b16 v[228:229], v145 offset:53248\n"
    "v_mfma_f32_16x16x32_bf16 v[120:123], v[232:235], a[48:51], v[120:123]\n"
    "s_waitcnt lgkmcnt(7)\n"
    "ds_read_b64_tr_b16 v[230:231], v145 offset:55296\n"
    "v_mfma_f32_16x16x32_bf16 v[64:67], v[208:211], a[64:67], v[64:67]\n"
    "s_add_u32 s52, s52, 7168\n"
    "ds_read_b64_tr_b16 v[232:233], v145 offset:57344\n"
    "v_mfma_f32_16x16x32_bf16 v[64:67], v[212:215], a[72:75], v[64:67]\n"
    "ds_read_b64_tr_b16 v[234:235], v145 offset:59392\n"
    "buffer_load_dwordx4 v255, s[84:87], s52 offen lds\n"
    "v_mfma_f32_16x16x32_bf16 v[68:71], v[212:215], a[76:79], v[68:71]\n"
    "s_add_u32 m0, s64, 114688\n"
    "ds_read_b64_tr_b16 v[236:237], v145 offset:61440\n"
    "v_mfma_f32_16x16x32_bf16 v[68:71], v[208:211], a[68:71], v[68:71]\n"
    "s_waitcnt lgkmcnt(7)\n"
    "ds_read_b64_tr_b16 v[238:239], v145 offset:63488\n"
    "v_mfma_f32_16x16x32_bf16 v[100:103], v[216:219], a[68:71], v[100:103]\n"
    "s_add_u32 s52, s52, 78848\n"
    "ds_read_b64_tr_b16 v[208:209], v146 offset:49152\n"
    "v_mfma_f32_16x16x32_bf16 v[100:103], v[220:223], a[76:79], v[100:103]\n"
    "ds_read_b64_tr_b16 v[210:211], v146 offset:51200\n"
    "buffer_load_dwordx4 v255, s[84:87], s52 offen lds\n"
    "v_mfma_f32_16x16x32_bf16 v[96:99], v[220:223], a[72:75], v[96:99]\n"
    "s_add_u32 m0, s64, 115712\n"
    "ds_read_b64_tr_b16 v[212:213], v146 offset:53248\n"
    "v_mfma_f32_16x16x32_bf16 v[96:99], v[216:219], a[64:67], v[96:99]\n"
    "s_waitcnt lgkmcnt(7)\n"
    "ds_read_b64_tr_b16 v[214:215], v146 offset:55296\n"
    "v_mfma_f32_16x16x32_bf16 v[72:75], v[224:227], a[64:67], v[72:75]\n"
    "s_add_u32 s52, s52, 7168\n"
    "ds_read_b64_tr_b16 v[216:217], v146 offset:57344\n"
    "v_mfma_f32_16x16x32_bf16 v[72:75], v[228:231], a[72:75], v[72:75]\n"
    "ds_read_b64_tr_b16 v[218:219], v146 offset:59392\n"
    "buffer_load_dwordx4 v255, s[84:87], s52 offen lds\n"
    "v_mfma_f32_16x16x32_bf16 v[76:79], v[228:231], a[76:79], v[76:79]\n"
    "s_add_u32 m0, s64, 122880\n"
    "ds_read_b64_tr_b16 v[220:221], v146 offset:61440\n"
    "v_mfma_f32_16x16x32_bf16 v[76:79], v[224:227], a[68:71], v[76:79]\n"
    "s_waitcnt lgkmcnt(7)\n"
    "ds_read_b64_tr_b16 v[222:223], v146 offset:63488\n"
    "v_mfma_f32_16x16x32_bf16 v[108:111], v[232:235], a[68:71], v[108:111]\n"
    "s_add_u32 s52, s52, 21504\n"
    "ds_read_b64_tr_b16 v[224:225], v147 offset:49152\n"
    "v_mfma_f32_16x16x32_bf16 v[108:111], v[236:239], a[76:79], v[108:111]\n"
    "ds_read_b64_tr_b16 v[226:227], v147 offset:51200\n"
    "buffer_load_dwordx4 v255, s[84:87], s52 offen lds\n"
    "v_mfma_f32_16x16x32_bf16 v[104:107], v[236:239], a[72:75], v[104:107]\n"
    "s_add_u32 m0, s64, 123904\n"
    "ds_read_b64_tr_b16 v[228:229], v147 offset:53248\n"
    "v_mfma_f32_16x16x32_bf16 v[104:107], v[232:235], a[64:67], v[104:107]\n"
    "s_waitcnt lgkmcnt(7)\n"
    "ds_read_b64_tr_b16 v[230:231], v147 offset:55296\n"
    "v_mfma_f32_16x16x32_bf16 v[80:83], v[208:211], a[64:67], v[80:83]\n"
    "s_add_u32 s52, s52, 7168\n"
    "ds_read_b64_tr_b16 v[232:233], v147 offset:57344\n"
    "v_mfma_f32_16x16x32_bf16 v[80:83], v[212:215], a[72:75], v[80:83]\n"
    "ds_read_b64_tr_b16 v[234:235], v147 offset:59392\n"
    "buffer_load_dwordx4 v255, s[84:87], s52 offen lds\n"
    "v_mfma_f32_16x16x32_bf16 v[84:87], v[212:215], a[76:79], v[84:87]\n"
    "ds_read_b64_tr_b16 v[236:237], v147 offset:61440\n"
    "v_mfma_f32_16x16x32_bf16 v[84:87], v[208:211], a[68:71], v[84:87]\n"
    "s_waitcnt lgkmcnt(7)\n"
    "ds_read_b64_tr_b16 v[238:239], v147 offset:63488\n"
    "v_mfma_f32_16x16x32_bf16 v[116:119], v[216:219], a[68:71], v[116:119]\n"
    "ds_read_b64_tr_b16 v[208:209], v148 offset:0\n"
    "v_mfma_f32_16x16x32_bf16 v[116:119], v[220:223], a[76:79], v[116:119]\n"
    "ds_read_b64_tr_b16 v[210:211], v148 offset:2048\n"
    "v_mfma_f32_16x16x32_bf16 v[112:115], v[220:223], a[72:75], v[112:115]\n"
    "ds_read_b64_tr_b16 v[212:213], v148 offset:4096\n"
    "v_mfma_f32_16x16x32_bf16 v[112:115], v[216:219], a[64:67], v[112:115]\n"
    "s_waitcnt lgkmcnt(7)\n"
    "ds_read_b64_tr_b16 v[214:215], v148 offset:6144\n"
    "v_mfma_f32_16x16x32_bf16 v[88:91], v[224:227], a[64:67], v[88:91]\n"
    "ds_read_b64_tr_b16 v[216:217], v148 offset:8192\n"
    "v_mfma_f32_16x16x32_bf16 v[88:91], v[228:231], a[72:75], v[88:91]\n"
    "ds_read_b64_tr_b16 v[218:219], v148 offset:10240\n"
    "v_mfma_f32_16x16x32_bf16 v[92:95], v[228:231], a[76:79], v[92:95]\n"
    "ds_read_b64_tr_b16 v[220:221], v148 offset:12288\n"
    "v_mfma_f32_16x16x32_bf16 v[92:95], v[224:227], a[68:71], v[92:95]\n"
    "s_waitcnt lgkmcnt(7)\n"
    "ds_read_b64_tr_b16 v[222:223], v148 offset:14336\n"
    "v_mfma_f32_16x16x32_bf16 v[124:127], v[232:235], a[68:71], v[124:127]\n"
    "s_waitcnt vmcnt(11)\n"
    "ds_read_b64_tr_b16 v[224:225], v149 offset:0\n"
    "v_mfma_f32_16x16x32_bf16 v[124:127], v[236:239], a[76:79], v[124:127]\n"
    "ds_read_b64_tr_b16 v[226:227], v149 offset:2048\n"
    "buffer_store_dwordx2 v[248:249], v250, s[76:79], s65 offen sc1\n"
    "v_mfma_f32_16x16x32_bf16 v[120:123], v[236:239], a[72:75], v[120:123]\n"
    "ds_read_b64_tr_b16 v[228:229], v149 offset:4096\n"
    "v_mfma_f32_16x16x32_bf16 v[120:123], v[232:235], a[64:67], v[120:123]\n"
    "s_waitcnt lgkmcnt(7)\n"
    "ds_read_b64_tr_b16 v[230:231], v149 offset:6144\n"
    "v_mfma_f32_16x16x32_bf16 v[64:67], v[208:211], a[80:83], v[64:67]\n"
    "ds_read_b64_tr_b16 v[232:233], v149 offset:8192\n"
    "v_mfma_f32_16x16x32_bf16 v[64:67], v[212:215], a[88:91], v[64:67]\n"
    "s_nop 0\n"
    "s_barrier\n"
    "v_mfma_f32_16x16x32_bf16 v[68:71], v[212:215], a[92:95], v[68:71]\n"
    "s_add_u32 m0, s64, 32768\n"
    "ds_read_b64_tr_b16 v[234:235], v149 offset:10240\n"
    "ds_read_b64_tr_b16 v[236:237], v149 offset:12288\n"
    "v_mfma_f32_16x16x32_bf16 v[68:71], v[208:211], a[84:87], v[68:71]\n"
    "s_waitcnt lgkmcnt(7)\n"
    "ds_read_b64_tr_b16 v[238:239], v149 offset:14336\n"
    "v_mfma_f32_16x16x32_bf16 v[100:103], v[216:219], a[84:87], v[100:103]\n"
    "s_add_u32 s52, s52, 78848\n"
    "ds_read_b64_tr_b16 v[208:209], v150 offset:0\n"
    "v_mfma_f32_16x16x32_bf16 v[100:103], v[220:223], a[92:95], v[100:103]\n"
    "ds_read_b64_tr_b16 v[210:211], v150 offset:2048\n"
    "buffer_load_dwordx4 v255, s[84:87], s52 offen lds\n"
    "v_mfma_f32_16x16x32_bf16 v[96:99], v[220:223], a[88:91], v[96:99]\n"
    "s_add_u32 m0, s64, 33792\n"
    "ds_read_b64_tr_b16 v[212:213], v150 offset:4096\n"
    "v_mfma_f32_16x16x32_bf16 v[96:99], v[216:219], a[80:83], v[96:99]\n"
    "s_waitcnt lgkmcnt(7)\n"
    "ds_read_b64_tr_b16 v[214:215], v150 offset:6144\n"
    "v_mfma_f32_16x16x32_bf16 v[72:75], v[224:227], a[80:83], v[72:75]\n"
    "s_add_u32 s52, s52, 7168\n"
    "ds_read_b64_tr_b16 v[216:217], v150 offset:8192\n"
    "v_mfma_f32_16x16x32_bf16 v[72:75], v[228:231], a[88:91], v[72:75]\n"
    "ds_read_b64_tr_b16 v[218:219], v150 offset:10240\n"
    "buffer_load_dwordx4 v255, s[84:87], s52 offen lds\n"
    "v_mfma_f32_16x16x32_bf16 v[76:79], v[228:231], a[92:95], v[76:79]\n"
    "s_add_u32 m0, s64, 40960\n"
    "ds_read_b64_tr_b16 v[220:221], v150 offset:12288\n"
    "v_mfma_f32_16x16x32_bf16 v[76:79], v[224:227], a[84:87], v[76:79]\n"
    "s_waitcnt lgkmcnt(7)\n"
    "ds_read_b64_tr_b16 v[222:223], v150 offset:14336\n"
    "v_mfma_f32_16x16x32_bf16 v[108:111], v[232:235], a[84:87], v[108:111]\n"
    "s_add_u32 s52, s52, 21504\n"
    "ds_read_b64_tr_b16 v[224:225], v151 offset:0\n"
    "v_mfma_f32_16x16x32_bf16 v[108:111], v[236:239], a[92:95], v[108:111]\n"
    "ds_read_b64_tr_b16 v[226:227], v151 offset:2048\n"
    "buffer_load_dwordx4 v255, s[84:87], s52 offen lds\n"
    "v_mfma_f32_16x16x32_bf16 v[104:107], v[236:239], a[88:91], v[104:107]\n"
    "s_add_u32 m0, s64, 41984\n"
    "ds_read_b64_tr_b16 v[228:229], v151 offset:4096\n"
    "v_mfma_f32_16x16x32_bf16 v[104:107], v[232:235], a[80:83], v[104:107]\n"
    "s_waitcnt lgkmcnt(7)\n"
    "ds_read_b64_tr_b16 v[230:231], v151 offset:6144\n"
    "v_mfma_f32_16x16x32_bf16 v[80:83], v[208:211], a[80:83], v[80:83]\n"
    "s_add_u32 s52, s52, 7168\n"
    "ds_read_b64_tr_b16 v[232:233], v151 offset:8192\n"
    "v_mfma_f32_16x16x32_bf16 v[80:83], v[212:215], a[88:91], v[80:83]\n"
    "ds_read_b64_tr_b16 v[234:235], v151 offset:10240\n"
    "buffer_load_dwordx4 v255, s[84:87], s52 offen lds\n"
    "v_mfma_f32_16x16x32_bf16 v[84:87], v[212:215], a[92:95], v[84:87]\n"
    "s_add_u32 m0, s64, 49152\n"
    "ds_read_b64_tr_b16 v[236:237], v151 offset:12288\n"
    "v_mfma_f32_16x16x32_bf16 v[84:87], v[208:211], a[84:87], v[84:87]\n"
    "s_waitcnt lgkmcnt(7)\n"
    "ds_read_b64_tr_b16 v[238:239], v151 offset:14336\n"
    "v_mfma_f32_16x16x32_bf16 v[116:119], v[216:219], a[84:87], v[116:119]\n"
    "s_add_u32 s52, s52, 78848\n"
    "ds_read_b64_tr_b16 v[208:209], v148 offset:16384\n"
    "v_mfma_f32_16x16x32_bf16 v[116:119], v[220:223], a[92:95], v[116:119]\n"
    "ds_read_b64_tr_b16 v[210:211], v148 offset:18432\n"
    "buffer_load_dwordx4 v255, s[84:87], s52 offen lds\n"
    "v_mfma_f32_16x16x32_bf16 v[112:115], v[220:223], a[88:91], v[112:115]\n"
    "s_add_u32 m0, s64, 50176\n"
    "ds_read_b64_tr_b16 v[212:213], v148 offset:20480\n"
    "v_mfma_f32_16x16x32_bf16 v[112:115], v[216:219], a[80:83], v[112:115]\n"
    "s_waitcnt lgkmcnt(7)\n"
    "ds_read_b64_tr_b16 v[214:215], v148 offset:22528\n"
    "v_mfma_f32_16x16x32_bf16 v[88:91], v[224:227], a[80:83], v[88:91]\n"
    "s_add_u32 s52, s52, 7168\n"
    "ds_read_b64_tr_b16 v[216:217], v148 offset:24576\n"
    "v_mfma_f32_16x16x32_bf16 v[88:91], v[228:231], a[88:91], v[88:91]\n"
    "ds_read_b64_tr_b16 v[218:219], v148 offset:26624\n"
    "buffer_load_dwordx4 v255, s[84:87], s52 offen lds\n"
    "v_mfma_f32_16x16x32_bf16 v[92:95], v[228:231], a[92:95], v[92:95]\n"
    "s_add_u32 m0, s64, 57344\n"
    "ds_read_b64_tr_b16 v[220:221], v148 offset:28672\n"
    "v_mfma_f32_16x16x32_bf16 v[92:95], v[224:227], a[84:87], v[92:95]\n"
    "s_waitcnt lgkmcnt(7)\n"
    "ds_read_b64_tr_b16 v[222:223], v148 offset:30720\n"
    "v_mfma_f32_16x16x32_bf16 v[124:127], v[232:235], a[84:87], v[124:127]\n"
    "s_add_u32 s52, s52, 21504\n"
    "ds_read_b64_tr_b16 v[224:225], v149 offset:16384\n"
    "v_mfma_f32_16x16x32_bf16 v[124:127], v[236:239], a[92:95], v[124:127]\n"
    "ds_read_b64_tr_b16 v[226:227], v149 offset:18432\n"
    "buffer_load_dwordx4 v255, s[84:87], s52 offen lds\n"
    "v_mfma_f32_16x16x32_bf16 v[120:123], v[236:239], a[88:91], v[120:123]\n"
    "s_add_u32 m0, s64, 58368\n"
    "ds_read_b64_tr_b16 v[228:229], v149 offset:20480\n"
    "v_mfma_f32_16x16x32_bf16 v[120:123], v[232:235], a[80:83], v[120:123]\n"
    "s_waitcnt lgkmcnt(7)\n"
    "ds_read_b64_tr_b16 v[230:231], v149 offset:22528\n"
    "v_mfma_f32_16x16x32_bf16 v[64:67], v[208:211], a[96:99], v[64:67]\n"
    "s_add_u32 s52, s52, 7168\n"
    "ds_read_b64_tr_b16 v[232:233], v149 offset:24576\n"
    "v_mfma_f32_16x16x32_bf16 v[64:67], v[212:215], a[104:107], v[64:67]\n"
    "ds_read_b64_tr_b16 v[234:235], v149 offset:26624\n"
    "buffer_load_dwordx4 v255, s[84:87], s52 offen lds\n"
    "v_mfma_f32_16x16x32_bf16 v[68:71], v[212:215], a[108:111], v[68:71]\n"
    "ds_read_b64_tr_b16 v[236:237], v149 offset:28672\n"
    "v_mfma_f32_16x16x32_bf16 v[68:71], v[208:211], a[100:103], v[68:71]\n"
    "s_waitcnt lgkmcnt(7)\n"
    "ds_read_b64_tr_b16 v[238:239], v149 offset:30720\n"
    "v_mfma_f32_16x16x32_bf16 v[100:103], v[216:219], a[100:103], v[100:103]\n"
    "ds_read_b64_tr_b16 v[208:209], v150 offset:16384\n"
    "v_mfma_f32_16x16x32_bf16 v[100:103], v[220:223], a[108:111], v[100:103]\n"
    "ds_read_b64_tr_b16 v[210:211], v150 offset:18432\n"
    "v_mfma_f32_16x16x32_bf16 v[96:99], v[220:223], a[104:107], v[96:99]\n"
    "ds_read_b64_tr_b16 v[212:213], v150 offset:20480\n"
    "v_mfma_f32_16x16x32_bf16 v[96:99], v[216:219], a[96:99], v[96:99]\n"
    "s_waitcnt lgkmcnt(7)\n"
    "ds_read_b64_tr_b16 v[214:215], v150 offset:22528\n"
    "v_mfma_f32_16x16x32_bf16 v[72:75], v[224:227], a[96:99], v[72:75]\n"
    "ds_read_b64_tr_b16 v[216:217], v150 offset:24576\n"
    "v_mfma_f32_16x16x32_bf16 v[72:75], v[228:231], a[104:107], v[72:75]\n"
    "ds_read_b64_tr_b16 v[218:219], v150 offset:26624\n"
    "v_mfma_f32_16x16x32_bf16 v[76:79], v[228:231], a[108:111], v[76:79]\n"
    "ds_read_b64_tr_b16 v[220:221], v150 offset:28672\n"
    "v_mfma_f32_16x16x32_bf16 v[76:79], v[224:227], a[100:103], v[76:79]\n"
    "s_waitcnt lgkmcnt(7)\n"
    "ds_read_b64_tr_b16 v[222:223], v150 offset:30720\n"
    "v_mfma_f32_16x16x32_bf16 v[108:111], v[232:235], a[100:103], v[108:111]\n"
    "ds_read_b64_tr_b16 v[224:225], v151 offset:16384\n"
    "v_mfma_f32_16x16x32_bf16 v[108:111], v[236:239], a[108:111], v[108:111]\n"
    "ds_read_b64_tr_b16 v[226:227], v151 offset:18432\n"
    "v_mfma_f32_16x16x32_bf16 v[104:107], v[236:239], a[104:107], v[104:107]\n"
    "ds_read_b64_tr_b16 v[228:229], v151 offset:20480\n"
    "v_mfma_f32_16x16x32_bf16 v[104:107], v[232:235], a[96:99], v[104:107]\n"
    "s_waitcnt lgkmcnt(7)\n"
    "ds_read_b64_tr_b16 v[230:231], v151 offset:22528\n"
    "v_mfma_f32_16x16x32_bf16 v[80:83], v[208:211], a[96:99], v[80:83]\n"
    "ds_read_b64_tr_b16 v[232:233], v151 offset:24576\n"
    "v_mfma_f32_16x16x32_bf16 v[80:83], v[212:215], a[104:107], v[80:83]\n"
    "ds_read_b64_tr_b16 v[234:235], v151 offset:26624\n"
    "v_mfma_f32_16x16x32_bf16 v[84:87], v[212:215], a[108:111], v[84:87]\n"
    "ds_read_b64_tr_b16 v[236:237], v151 offset:28672\n"
    "v_mfma_f32_16x16x32_bf16 v[84:87], v[208:211], a[100:103], v[84:87]\n"
    "s_waitcnt lgkmcnt(7)\n"
    "ds_read_b64_tr_b16 v[238:239], v151 offset:30720\n"
    "v_mfma_f32_16x16x32_bf16 v[116:119], v[216:219], a[100:103], v[116:119]\n"
    "v_mfma_f32_16x16x32_bf16 v[116:119], v[220:223], a[108:111], v[116:119]\n"
    "v_mfma_f32_16x16x32_bf16 v[112:115], v[220:223], a[104:107], v[112:115]\n"
    "v_mfma_f32_16x16x32_bf16 v[112:115], v[216:219], a[96:99], v[112:115]\n"
    "s_waitcnt lgkmcnt(4)\n"
    "v_mfma_f32_16x16x32_bf16 v[88:91], v[224:227], a[96:99], v[88:91]\n"
    "v_mfma_f32_16x16x32_bf16 v[88:91], v[228:231], a[104:107], v[88:91]\n"
    "v_mfma_f32_16x16x32_bf16 v[92:95], v[228:231], a[108:111], v[92:95]\n"
    "v_mfma_f32_16x16x32_bf16 v[92:95], v[224:227], a[100:103], v[92:95]\n"
    "s_waitcnt lgkmcnt(0)\n"
    "v_mfma_f32_16x16x32_bf16 v[124:127], v[232:235], a[100:103], v[124:127]\n"
    "v_mfma_f32_16x16x32_bf16 v[124:127], v[236:239], a[108:111], v[124:127]\n"
    "v_mfma_f32_16x16x32_bf16 v[120:123], v[236:239], a[104:107], v[120:123]\n"
    "v_mfma_f32_16x16x32_bf16 v[120:123], v[232:235], a[96:99], v[120:123]\n"
    "s_waitcnt vmcnt(9)\n"
    "s_barrier\n"
    "s_add_u32 m0, s64, 65536\n"
    "s_add_u32 s52, s52, 78848\n"
    "v_pk_mul_f32 v[160:161], v[64:65], v[64:65]\n"
    "buffer_load_dwordx4 v255, s[84:87], s52 offen lds\n"
    "v_pk_mul_f32 v[162:163], v[66:67], v[66:67]\n"
    "v_pk_mul_f32 v[164:165], v[72:73], v[72:73]\n"
    "v_pk_mul_f32 v[166:167], v[74:75], v[74:75]\n"
    "v_pk_fma_f32 v[160:161], v[160:161], v[244:245], v[242:243]\n"
    "v_pk_fma_f32 v[162:163], v[162:163], v[244:245], v[242:243]\n"
    "v_pk_fma_f32 v[164:165], v[164:165], v[244:245], v[242:243]\n"
    "v_pk_fma_f32 v[166:167], v[166:167], v[244:245], v[242:243]\n"
    "s_add_u32 m0, s64, 66560\n"
    "s_add_u32 s52, s52, 7168\n"
    "v_pk_mul_f32 v[160:161], v[160:161], v[64:65]\n"
    "buffer_load_dwordx4 v255, s[84:87], s52 offen lds\n"
    "v_pk_mul_f32 v[162:163], v[162:163], v[66:67]\n"
    "v_pk_mul_f32 v[164:165], v[164:165], v[72:73]\n"
    "v_pk_mul_f32 v[166:167], v[166:167], v[74:75]\n"
    "v_exp_f32 v160, v160\n"
    "v_exp_f32 v161, v161\n"
    "v_pk_mul_f32 v[64:65], v[64:65], v[96:97]\n"
    "v_exp_f32 v162, v162\n"
    "s_add_u32 m0, s64, 73728\n"
    "s_add_u32 s52, s52, 21504\n"
    "v_exp_f32 v163, v163\n"
    "buffer_load_dwordx4 v255, s[84:87], s52 offen lds\n"
    "v_pk_mul_f32 v[66:67], v[66:67], v[98:99]\n"
    "v_exp_f32 v164, v164\n"
    "v_exp_f32 v165, v165\n"
    "v_pk_mul_f32 v[72:73], v[72:73], v[104:105]\n"
    "v_exp_f32 v166, v166\n"
    "v_exp_f32 v167, v167\n"
    "v_pk_mul_f32 v[74:75], v[74:75], v[106:107]\n"
    "s_add_u32 m0, s64, 74752\n"
    "s_add_u32 s52, s52, 7168\n"
    "v_pk_add_f32 v[160:161], v[160:161], v[246:247]\n"
    "buffer_load_dwordx4 v255, s[84:87], s52 offen lds\n"
    "v_pk_add_f32 v[162:163], v[162:163], v[246:247]\n"
    "v_pk_add_f32 v[164:165], v[164:165], v[246:247]\n"
    "v_pk_add_f32 v[166:167], v[166:167], v[246:247]\n"
    "v_rcp_f32 v160, v160\n"
    "v_rcp_f32 v161, v161\n"
    "v_rcp_f32 v162, v162\n"
    "v_rcp_f32 v163, v163\n"
    "v_rcp_f32 v164, v164\n"
    "v_rcp_f32 v165, v165\n"
    "v_rcp_f32 v166, v166\n"
    "v_rcp_f32 v167, v167\n"
    "v_pk_mul_f32 v[64:65], v[64:65], v[160:161]\n"
    "v_pk_mul_f32 v[66:67], v[66:67], v[162:163]\n"
    "v_pk_mul_f32 v[72:73], v[72:73], v[164:165]\n"
    "v_pk_mul_f32 v[74:75], v[74:75], v[166:167]\n"
    "v_cvt_pk_bf16_f32 v192, v64, v65\n"
    "v_cvt_pk_bf16_f32 v193, v66, v67\n"
    "v_cvt_pk_bf16_f32 v194, v72, v73\n"
    "v_cvt_pk_bf16_f32 v195, v74, v75\n"
    "buffer_load_dwordx2 v[240:241], v251, s[76:79], s67 offen sc1\n"
    "buffer_store_dwordx4 v[192:195], v254, s[76:79], s57 offen offset:2048 sc1\n"
    "s_add_u32 m0, s64, 81920\n"
    "s_add_u32 s52, s52, 78848\n"
    "v_pk_mul_f32 v[160:161], v[68:69], v[68:69]\n"
    "buffer_load_dwordx4 v255, s[84:87], s52 offen lds\n"
    "v_pk_mul_f32 v[162:163], v[70:71], v[70:71]\n"
    "v_pk_mul_f32 v[164:165], v[76:77], v[76:77]\n"
    "v_pk_mul_f32 v[166:167], v[78:79], v[78:79]\n"
    "v_pk_fma_f32 v[160:161], v[160:161], v[244:245], v[242:243]\n"
    "v_pk_fma_f32 v[162:163], v[162:163], v[244:245], v[242:243]\n"
    "v_pk_fma_f32 v[164:165], v[164:165], v[244:245], v[242:243]\n"
    "v_pk_fma_f32 v[166:167], v[166:167], v[244:245], v[242:243]\n"
    "s_add_u32 m0, s64, 82944\n"
    "s_add_u32 s52, s52, 7168\n"
    "v_pk_mul_f32 v[160:161], v[160:161], v[68:69]\n"
    "buffer_load_dwordx4 v255, s[84:87], s52 offen lds\n"
    "v_pk_mul_f32 v[162:163], v[162:163], v[70:71]\n"
    "v_pk_mul_f32 v[164:165], v[164:165], v[76:77]\n"
    "v_pk_mul_f32 v[166:167], v[166:167], v[78:79]\n"
    "v_exp_f32 v160, v160\n"
    "v_exp_f32 v161, v161\n"
    "v_pk_mul_f32 v[68:69], v[68:69], v[100:101]\n"
    "v_exp_f32 v162, v162\n"
    "s_add_u32 m0, s64, 90112\n"
    "s_add_u32 s52, s52, 21504\n"
    "v_exp_f32 v163, v163\n"
    "buffer_load_dwordx4 v255, s[84:87], s52 offen lds\n"
    "v_pk_mul_f32 v[70:71], v[70:71], v[102:103]\n"
    "v_exp_f32 v164, v164\n"
    "v_exp_f32 v165, v165\n"
    "v_pk_mul_f32 v[76:77], v[76:77], v[108:109]\n"
    "v_exp_f32 v166, v166\n"
    "v_exp_f32 v167, v167\n"
    "v_pk_mul_f32 v[78:79], v[78:79], v[110:111]\n"
    "s_add_u32 m0, s64, 91136\n"
    "s_add_u32 s52, s52, 7168\n"
    "v_pk_add_f32 v[160:161], v[160:161], v[246:247]\n"
    "buffer_load_dwordx4 v255, s[84:87], s52 offen lds\n"
    "v_pk_add_f32 v[162:163], v[162:163], v[246:247]\n"
    "v_pk_add_f32 v[164:165], v[164:165], v[246:247]\n"
    "v_pk_add_f32 v[166:167], v[166:167], v[246:247]\n"
    "v_rcp_f32 v160, v160\n"
    "v_rcp_f32 v161, v161\n"
    "v_rcp_f32 v162, v162\n"
    "v_rcp_f32 v163, v163\n"
    "v_rcp_f32 v164, v164\n"
    "v_rcp_f32 v165, v165\n"
    "v_rcp_f32 v166, v166\n"
    "v_rcp_f32 v167, v167\n"
    "v_pk_mul_f32 v[68:69], v[68:69], v[160:161]\n"
    "v_pk_mul_f32 v[70:71], v[70:71], v[162:163]\n"
    "v_pk_mul_f32 v[76:77], v[76:77], v[164:165]\n"
    "v_pk_mul_f32 v[78:79], v[78:79], v[166:167]\n"
    "v_cvt_pk_bf16_f32 v196, v68, v69\n"
    "v_cvt_pk_bf16_f32 v197, v70, v71\n"
    "v_cvt_pk_bf16_f32 v198, v76, v77\n"
    "v_cvt_pk_bf16_f32 v199, v78, v79\n"
    "buffer_store_dwordx4 v[196:199], v254, s[76:79], s58 offen offset:2048 sc1\n"
    "v_pk_mul_f32 v[160:161], v[80:81], v[80:81]\n"
    "v_pk_mul_f32 v[162:163], v[82:83], v[82:83]\n"
    "v_pk_mul_f32 v[164:165], v[88:89], v[88:89]\n"
    "v_pk_mul_f32 v[166:167], v[90:91], v[90:91]\n"
    "v_pk_fma_f32 v[160:161], v[160:161], v[244:245], v[242:243]\n"
    "v_pk_fma_f32 v[162:163], v[162:163], v[244:245], v[242:243]\n"
    "v_pk_fma_f32 v[164:165], v[164:165], v[244:245], v[242:243]\n"
    "v_pk_fma_f32 v[166:167], v[166:167], v[244:245], v[242:243]\n"
    "v_pk_mul_f32 v[160:161], v[160:161], v[80:81]\n"
    "v_pk_mul_f32 v[162:163], v[162:163], v[82:83]\n"
    "v_pk_mul_f32 v[164:165], v[164:165], v[88:89]\n"
    "v_pk_mul_f32 v[166:167], v[166:167], v[90:91]\n"
    "v_exp_f32 v160, v160\n"
    "v_exp_f32 v161, v161\n"
    "v_pk_mul_f32 v[80:81], v[80:81], v[112:113]\n"
    "v_exp_f32 v162, v162\n"
    "v_exp_f32 v163, v163\n"
    "v_pk_mul_f32 v[82:83], v[82:83], v[114:115]\n"
    "v_exp_f32 v164, v164\n"
    "v_exp_f32 v165, v165\n"
    "v_pk_mul_f32 v[88:89], v[88:89], v[120:121]\n"
    "v_exp_f32 v166, v166\n"
    "v_exp_f32 v167, v167\n"
    "v_pk_mul_f32 v[90:91], v[90:91], v[122:123]\n"
    "v_pk_add_f32 v[160:161], v[160:161], v[246:247]\n"
    "v_pk_add_f32 v[162:163], v[162:163], v[246:247]\n"
    "v_pk_add_f32 v[164:165], v[164:165], v[246:247]\n"
    "v_pk_add_f32 v[166:167], v[166:167], v[246:247]\n"
    "v_rcp_f32 v160, v160\n"
    "v_rcp_f32 v161, v161\n"
    "v_rcp_f32 v162, v162\n"
    "v_rcp_f32 v163, v163\n"
    "v_rcp_f32 v164, v164\n"
    "v_rcp_f32 v165, v165\n"
    "v_rcp_f32 v166, v166\n"
    "v_rcp_f32 v167, v167\n"
    "v_pk_mul_f32 v[80:81], v[80:81], v[160:161]\n"
    "v_pk_mul_f32 v[82:83], v[82:83], v[162:163]\n"
    "v_pk_mul_f32 v[88:89], v[88:89], v[164:165]\n"
    "v_pk_mul_f32 v[90:91], v[90:91], v[166:167]\n"
    "v_cvt_pk_bf16_f32 v200, v80, v81\n"
    "v_cvt_pk_bf16_f32 v201, v82, v83\n"
    "v_cvt_pk_bf16_f32 v202, v88, v89\n"
    "v_cvt_pk_bf16_f32 v203, v90, v91\n"
    "buffer_store_dwordx4 v[200:203], v254, s[76:79], s57 offen offset:3072 sc1\n"
    "v_pk_mul_f32 v[160:161], v[84:85], v[84:85]\n"
    "v_pk_mul_f32 v[162:163], v[86:87], v[86:87]\n"
    "v_pk_mul_f32 v[164:165], v[92:93], v[92:93]\n"
    "v_pk_mul_f32 v[166:167], v[94:95], v[94:95]\n"
    "v_pk_fma_f32 v[160:161], v[160:161], v[244:245], v[242:243]\n"
    "v_pk_fma_f32 v[162:163], v[162:163], v[244:245], v[242:243]\n"
    "v_pk_fma_f32 v[164:165], v[164:165], v[244:245], v[242:243]\n"
    "v_pk_fma_f32 v[166:167], v[166:167], v[244:245], v[242:243]\n"
    "v_pk_mul_f32 v[160:161], v[160:161], v[84:85]\n"
    "v_pk_mul_f32 v[162:163], v[162:163], v[86:87]\n"
    "v_pk_mul_f32 v[164:165], v[164:165], v[92:93]\n"
    "v_pk_mul_f32 v[166:167], v[166:167], v[94:95]\n"
    "v_exp_f32 v160, v160\n"
    "v_exp_f32 v161, v161\n"
    "v_pk_mul_f32 v[84:85], v[84:85], v[116:117]\n"
    "v_exp_f32 v162, v162\n"
    "v_exp_f32 v163, v163\n"
    "v_pk_mul_f32 v[86:87], v[86:87], v[118:119]\n"
    "v_exp_f32 v164, v164\n"
    "v_exp_f32 v165, v165\n"
    "v_pk_mul_f32 v[92:93], v[92:93], v[124:125]\n"
    "v_exp_f32 v166, v166\n"
    "v_exp_f32 v167, v167\n"
    "v_pk_mul_f32 v[94:95], v[94:95], v[126:127]\n"
    "v_pk_add_f32 v[160:161], v[160:161], v[246:247]\n"
    "v_pk_add_f32 v[162:163], v[162:163], v[246:247]\n"
    "v_pk_add_f32 v[164:165], v[164:165], v[246:247]\n"
    "v_pk_add_f32 v[166:167], v[166:167], v[246:247]\n"
    "v_rcp_f32 v160, v160\n"
    "v_rcp_f32 v161, v161\n"
    "v_rcp_f32 v162, v162\n"
    "v_rcp_f32 v163, v163\n"
    "v_rcp_f32 v164, v164\n"
    "v_rcp_f32 v165, v165\n"
    "v_rcp_f32 v166, v166\n"
    "v_rcp_f32 v167, v167\n"
    "v_pk_mul_f32 v[84:85], v[84:85], v[160:161]\n"
    "v_pk_mul_f32 v[86:87], v[86:87], v[162:163]\n"
    "v_pk_mul_f32 v[92:93], v[92:93], v[164:165]\n"
    "v_pk_mul_f32 v[94:95], v[94:95], v[166:167]\n"
    "v_cvt_pk_bf16_f32 v204, v84, v85\n"
    "v_cvt_pk_bf16_f32 v205, v86, v87\n"
    "v_cvt_pk_bf16_f32 v206, v92, v93\n"
    "v_cvt_pk_bf16_f32 v207, v94, v95\n"
    "buffer_store_dwordx4 v[204:207], v254, s[76:79], s58 offen offset:3072 sc1\n"
    "s_waitcnt vmcnt(8)\n"
    "v_cmp_ne_u64 vcc, s[74:75], v[240:241]\n"
    "s_mov_b32 s49, s59\n"
    "s_cbranch_vccnz SLOW3_%=\n"
    "RET3_%=:\n"
    "buffer_load_dwordx4 a[0:3], v128, s[76:79], s49 offen offset:0 sc1\n"
    "buffer_load_dwordx4 a[4:7], v129, s[76:79], s49 offen offset:0 sc1\n"
    "buffer_load_dwordx4 a[8:11], v128, s[76:79], s49 offen offset:1024 sc1\n"
    "buffer_load_dwordx4 a[12:15], v129, s[76:79], s49 offen offset:1024 sc1\n"
    "s_add_u32 s49, s49, 4096\n"
    "buffer_load_dwordx4 a[16:19], v128, s[76:79], s49 offen offset:0 sc1\n"
    "buffer_load_dwordx4 a[20:23], v129, s[76:79], s49 offen offset:0 sc1\n"
    "buffer_load_dwordx4 a[24:27], v128, s[76:79], s49 offen offset:1024 sc1\n"
    "buffer_load_dwordx4 a[28:31], v129, s[76:79], s49 offen offset:1024 sc1\n"
    "s_add_u32 s49, s49, 4096\n"
    "s_waitcnt vmcnt(8)\n"
    "buffer_store_dwordx2 v[248:249], v250, s[76:79], s66 offen sc1\n"
    "ds_read_b64_tr_b16 v[208:209], v140 offset:32768\n"
    "ds_read_b64_tr_b16 v[210:211], v140 offset:34816\n"
    "ds_read_b64_tr_b16 v[212:213], v140 offset:40960\n"
    "ds_read_b64_tr_b16 v[214:215], v140 offset:43008\n"
    "ds_read_b64_tr_b16 v[216:217], v141 offset:32768\n"
    "ds_read_b64_tr_b16 v[218:219], v141 offset:34816\n"
    "ds_read_b64_tr_b16 v[220:221], v141 offset:40960\n"
    "ds_read_b64_tr_b16 v[222:223], v141 offset:43008\n"
    "ds_read_b64_tr_b16 v[224:225], v142 offset:32768\n"
    "ds_read_b64_tr_b16 v[226:227], v142 offset:34816\n"
    "ds_read_b64_tr_b16 v[228:229], v142 offset:40960\n"
    "ds_read_b64_tr_b16 v[230:231], v142 offset:43008\n"
    "s_waitcnt vmcnt(5)\n"
    "v_mfma_f32_16x16x32_bf16 v[112:115], a[128:131], a[0:3], 0\n"
    "v_mfma_f32_16x16x32_bf16 v[112:115], a[132:135], a[8:11], v[112:115]\n"
    "buffer_load_dwordx4 a[32:35], v128, s[76:79], s49 offen offset:0 sc1\n"
    "v_mfma_f32_16x16x32_bf16 a[112:115], a[132:135], a[12:15], 0\n"
    "v_mfma_f32_16x16x32_bf16 a[112:115], a[128:131], a[4:7], a[112:115]\n"
    "buffer_load_dwordx4 a[36:39], v129, s[76:79], s49 offen offset:0 sc1\n"
    "v_mfma_f32_16x16x32_bf16 a[116:119], a[136:139], a[4:7], 0\n"
    "v_mfma_f32_16x16x32_bf16 a[116:119], a[140:143], a[12:15], a[116:119]\n"
    "buffer_load_dwordx4 a[40:43], v128, s[76:79], s49 offen offset:1024 sc1\n"
    "v_mfma_f32_16x16x32_bf16 v[116:119], a[140:143], a[8:11], 0\n"
    "v_mfma_f32_16x16x32_bf16 v[116:119], a[136:139], a[0:3], v[116:119]\n"
    "buffer_load_dwordx4 a[44:47], v129, s[76:79], s49 offen offset:1024 sc1\n"
    "v_mfma_f32_16x16x32_bf16 v[120:123], a[144:147], a[0:3], 0\n"
    "s_add_u32 s49, s49, 4096\n"
    "v_mfma_f32_16x16x32_bf16 v[120:123], a[148:151], a[8:11], v[120:123]\n"
    "v_mfma_f32_16x16x32_bf16 a[120:123], a[148:151], a[12:15], 0\n"
    "buffer_load_dwordx4 a[48:51], v128, s[76:79], s49 offen offset:0 sc1\n"
    "v_mfma_f32_16x16x32_bf16 a[120:123], a[144:147], a[4:7], a[120:123]\n"
    "v_mfma_f32_16x16x32_bf16 a[124:127], a[152:155], a[4:7], 0\n"
    "buffer_load_dwordx4 a[52:55], v129, s[76:79], s49 offen offset:0 sc1\n"
    "v_mfma_f32_16x16x32_bf16 a[124:127], a[156:159], a[12:15], a[124:127]\n"
    "v_mfma_f32_16x16x32_bf16 v[124:127], a[156:159], a[8:11], 0\n"
    "s_waitcnt vmcnt(7)\n"
    "buffer_load_dwordx4 a[56:59], v128, s[76:79], s49 offen offset:1024 sc1\n"
    "v_mfma_f32_16x16x32_bf16 v[124:127], a[152:155], a[0:3], v[124:127]\n"
    "v_mfma_f32_16x16x32_bf16 v[112:115], a[160:163], a[16:19], v[112:115]\n"
    "buffer_load_dwordx4 a[60:63], v129, s[76:79], s49 offen offset:1024 sc1\n"
    "v_mfma_f32_16x16x32_bf16 v[112:115], a[164:167], a[24:27], v[112:115]\n"
    "s_add_u32 s49, s49, 4096\n"
    "v_mfma_f32_16x16x32_bf16 a[112:115], a[164:167], a[28:31], a[112:115]\n"
    "v_mfma_f32_16x16x32_bf16 a[112:115], a[160:163], a[20:23], a[112:115]\n"
    "buffer_load_dwordx4 a[64:67], v128, s[76:79], s49 offen offset:0 sc1\n"
    "v_mfma_f32_16x16x32_bf16 a[116:119], a[168:171], a[20:23], a[116:119]\n"
    "v_mfma_f32_16x16x32_bf16 a[116:119], a[172:175], a[28:31], a[116:119]\n"
    "buffer_load_dwordx4 a[68:71], v129, s[76:79], s49 offen offset:0 sc1\n"
    "v_mfma_f32_16x16x32_bf16 v[116:119], a[172:175], a[24:27], v[116:119]\n"
    "v_mfma_f32_16x16x32_bf16 v[116:119], a[168:171], a[16:19], v[116:119]\n"
    "buffer_load_dwordx4 a[72:75], v128, s[76:79], s49 offen offset:1024 sc1\n"
    "v_mfma_f32_16x16x32_bf16 v[120:123], a[176:179], a[16:19], v[120:123]\n"
    "v_mfma_f32_16x16x32_bf16 v[120:123], a[180:183], a[24:27], v[120:123]\n"
    "buffer_load_dwordx4 a[76:79], v129, s[76:79], s49 offen offset:1024 sc1\n"
    "v_mfma_f32_16x16x32_bf16 a[120:123], a[180:183], a[28:31], a[120:123]\n"
    "s_add_u32 s49, s49, 4096\n"
    "v_mfma_f32_16x16x32_bf16 a[120:123], a[176:179], a[20:23], a[120:123]\n"
    "v_mfma_f32_16x16x32_bf16 a[124:127], a[184:187], a[20:23], a[124:127]\n"
    "buffer_load_dwordx4 a[80:83], v128, s[76:79], s49 offen offset:0 sc1\n"
    "v_mfma_f32_16x16x32_bf16 a[124:127], a[188:191], a[28:31], a[124:127]\n"
    "v_mfma_f32_16x16x32_bf16 v[124:127], a[188:191], a[24:27], v[124:127]\n"
    "s_waitcnt vmcnt(9)\n"
    "buffer_load_dwordx4 a[84:87], v129, s[76:79], s49 offen offset:0 sc1\n"
    "v_mfma_f32_16x16x32_bf16 v[124:127], a[184:187], a[16:19], v[124:127]\n"
    "v_mfma_f32_16x16x32_bf16 v[112:115], a[192:195], a[32:35], v[112:115]\n"
    "buffer_load_dwordx4 a[88:91], v128, s[76:79], s49 offen offset:1024 sc1\n"
    "v_mfma_f32_16x16x32_bf16 v[112:115], a[196:199], a[40:43], v[112:115]\n"
    "v_mfma_f32_16x16x32_bf16 a[112:115], a[196:199], a[44:47], a[112:115]\n"
    "buffer_load_dwordx4 a[92:95], v129, s[76:79], s49 offen offset:1024 sc1\n"
    "v_mfma_f32_16x16x32_bf16 a[112:115], a[192:195], a[36:39], a[112:115]\n"
    "s_add_u32 s49, s49, 4096\n"
    "v_mfma_f32_16x16x32_bf16 a[116:119], a[200:203], a[36:39], a[116:119]\n"
    "buffer_load_dwordx2 v[172:173], v251, s[76:79], s56 offen sc1\n"
    "v_mfma_f32_16x16x32_bf16 a[116:119], a[204:207], a[44:47], a[116:119]\n"
    "buffer_load_dwordx4 a[96:99], v128, s[76:79], s49 offen offset:0 sc1\n"
    "v_mfma_f32_16x16x32_bf16 v[116:119], a[204:207], a[40:43], v[116:119]\n"
    "v_mfma_f32_16x16x32_bf16 v[116:119], a[200:203], a[32:35], v[116:119]\n"
    "buffer_load_dwordx4 a[100:103], v129, s[76:79], s49 offen offset:0 sc1\n"
    "v_mfma_f32_16x16x32_bf16 v[120:123], a[208:211], a[32:35], v[120:123]\n"
    "v_mfma_f32_16x16x32_bf16 v[120:123], a[212:215], a[40:43], v[120:123]\n"
    "buffer_load_dwordx4 a[104:107], v128, s[76:79], s49 offen offset:1024 sc1\n"
    "v_mfma_f32_16x16x32_bf16 a[120:123], a[212:215], a[44:47], a[120:123]\n"
    "v_mfma_f32_16x16x32_bf16 a[120:123], a[208:211], a[36:39], a[120:123]\n"
    "buffer_load_dwordx4 a[108:111], v129, s[76:79], s49 offen offset:1024 sc1\n"
    "v_mfma_f32_16x16x32_bf16 a[124:127], a[216:219], a[36:39], a[124:127]\n"
    "s_mov_b32 s49, s59\n"
    "v_mfma_f32_16x16x32_bf16 a[124:127], a[220:223], a[44:47], a[124:127]\n"
    "v_mfma_f32_16x16x32_bf16 v[124:127], a[220:223], a[40:43], v[124:127]\n"
    "s_waitcnt vmcnt(13)\n"
    "buffer_load_dwordx4 v[240:243], v254, s[76:79], s48 offen sc1\n"
    "v_mfma_f32_16x16x32_bf16 v[124:127], a[216:219], a[32:35], v[124:127]\n"
    "buffer_load_dwordx4 v[244:247], v254, s[76:79], s48 offen offset:2048 sc1\n"
    "v_mfma_f32_16x16x32_bf16 v[112:115], a[224:227], a[48:51], v[112:115]\n"
    "v_mfma_f32_16x16x32_bf16 v[112:115], a[228:231], a[56:59], v[112:115]\n"
    "v_mfma_f32_16x16x32_bf16 a[112:115], a[228:231], a[60:63], a[112:115]\n"
    "v_mfma_f32_16x16x32_bf16 a[112:115], a[224:227], a[52:55], a[112:115]\n"
    "v_mfma_f32_16x16x32_bf16 a[116:119], a[232:235], a[52:55], a[116:119]\n"
    "v_mfma_f32_16x16x32_bf16 a[116:119], a[236:239], a[60:63], a[116:119]\n"
    "v_mfma_f32_16x16x32_bf16 v[116:119], a[236:239], a[56:59], v[116:119]\n"
    "v_mfma_f32_16x16x32_bf16 v[116:119], a[232:235], a[48:51], v[116:119]\n"
    "v_mfma_f32_16x16x32_bf16 v[120:123], a[240:243], a[48:51], v[120:123]\n"
    "v_mfma_f32_16x16x32_bf16 v[120:123], a[244:247], a[56:59], v[120:123]\n"
    "v_mfma_f32_16x16x32_bf16 a[120:123], a[244:247], a[60:63], a[120:123]\n"
    "v_mfma_f32_16x16x32_bf16 a[120:123], a[240:243], a[52:55], a[120:123]\n"
    "v_mfma_f32_16x16x32_bf16 a[124:127], a[248:251], a[52:55], a[124:127]\n"
    "v_mfma_f32_16x16x32_bf16 a[124:127], a[252:255], a[60:63], a[124:127]\n"
    "v_mfma_f32_16x16x32_bf16 v[124:127], a[252:255], a[56:59], v[124:127]\n"
    "s_waitcnt vmcnt(11)\n"
    "v_mfma_f32_16x16x32_bf16 v[124:127], a[248:251], a[48:51], v[124:127]\n"
    "s_waitcnt lgkmcnt(8)\n"
    "v_mfma_f32_16x16x32_bf16 v[112:115], v[208:211], a[64:67], v[112:115]\n"
    "ds_read_b64_tr_b16 v[232:233], v143 offset:32768\n"
    "v_mfma_f32_16x16x32_bf16 v[112:115], v[212:215], a[72:75], v[112:115]\n"
    "ds_read_b64_tr_b16 v[234:235], v143 offset:34816\n"
    "v_mfma_f32_16x16x32_bf16 a[112:115], v[212:215], a[76:79], a[112:115]\n"
    "ds_read_b64_tr_b16 v[236:237], v143 offset:40960\n"
    "v_mfma_f32_16x16x32_bf16 a[112:115], v[208:211], a[68:71], a[112:115]\n"
    "s_waitcnt lgkmcnt(7)\n"
    "ds_read_b64_tr_b16 v[238:239], v143 offset:43008\n"
    "v_mfma_f32_16x16x32_bf16 a[116:119], v[216:219], a[68:71], a[116:119]\n"
    "ds_read_b64_tr_b16 v[208:209], v168 offset:0\n"
    "v_mfma_f32_16x16x32_bf16 a[116:119], v[220:223], a[76:79], a[116:119]\n"
    "ds_read_b64_tr_b16 v[210:211], v168 offset:2048\n"
    "v_mfma_f32_16x16x32_bf16 v[116:119], v[220:223], a[72:75], v[116:119]\n"
    "ds_read_b64_tr_b16 v[212:213], v168 offset:8192\n"
    "v_mfma_f32_16x16x32_bf16 v[116:119], v[216:219], a[64:67], v[116:119]\n"
    "s_waitcnt lgkmcnt(7)\n"
    "ds_read_b64_tr_b16 v[214:215], v168 offset:10240\n"
    "v_mfma_f32_16x16x32_bf16 v[120:123], v[224:227], a[64:67], v[120:123]\n"
    "ds_read_b64_tr_b16 v[216:217], v169 offset:0\n"
    "v_mfma_f32_16x16x32_bf16 v[120:123], v[228:231], a[72:75], v[120:123]\n"
    "ds_read_b64_tr_b16 v[218:219], v169 offset:2048\n"
    "v_mfma_f32_16x16x32_bf16 a[120:123], v[228:231], a[76:79], a[120:123]\n"
    "ds_read_b64_tr_b16 v[220:221], v169 offset:8192\n"
    "v_mfma_f32_16x16x32_bf16 a[120:123], v[224:227], a[68:71], a[120:123]\n"
    "s_waitcnt lgkmcnt(7)\n"
    "ds_read_b64_tr_b16 v[222:223], v169 offset:10240\n"
    "v_mfma_f32_16x16x32_bf16 a[124:127], v[232:235], a[68:71], a[124:127]\n"
    "ds_read_b64_tr_b16 v[224:225], v170 offset:0\n"
    "v_mfma_f32_16x16x32_bf16 a[124:127], v[236:239], a[76:79], a[124:127]\n"
    "ds_read_b64_tr_b16 v[226:227], v170 offset:2048\n"
    "v_mfma_f32_16x16x32_bf16 v[124:127], v[236:239], a[72:75], v[124:127]\n"
    "s_waitcnt vmcnt(7)\n"
    "ds_read_b64_tr_b16 v[228:229], v170 offset:8192\n"
    "v_mfma_f32_16x16x32_bf16 v[124:127], v[232:235], a[64:67], v[124:127]\n"
    "s_waitcnt lgkmcnt(7)\n"
    "ds_read_b64_tr_b16 v[230:231], v170 offset:10240\n"
    "v_mfma_f32_16x16x32_bf16 v[112:115], v[208:211], a[80:83], v[112:115]\n"
    "s_waitcnt vmcnt(6)\n"
    "v_cmp_ne_u64 vcc, s[74:75], v[172:173]\n"
    "s_mov_b32 s53, 1048576\n"
    "s_cbranch_vccz PD3B_%=\n"
    "PL3B_%=:\n"
    "buffer_load_dwordx2 v[172:173], v251, s[76:79], s56 offen sc1\n"
    "s_sub_u32 s53, s53, 1\n"
    "s_waitcnt vmcnt(0)\n"
    "v_cmp_ne_u64 vcc, s[74:75], v[172:173]\n"
    "s_cbranch_scc1 PD3B_%=\n"
    "s_cbranch_vccnz PL3B_%=\n"
    "PD3B_%=:\n"
    "ds_read_b64_tr_b16 v[232:233], v171 offset:0\n"
    "v_mfma_f32_16x16x32_bf16 v[112:115], v[212:215], a[88:91], v[112:115]\n"
    "ds_read_b64_tr_b16 v[234:235], v171 offset:2048\n"
    "buffer_load_dwordx4 v[0:3], v128, s[76:79], s49 offen offset:2048 sc1\n"
    "v_mfma_f32_16x16x32_bf16 a[112:115], v[212:215], a[92:95], a[112:115]\n"
    "ds_read_b64_tr_b16 v[236:237], v171 offset:8192\n"
    "buffer_load_dwordx4 v[4:7], v129, s[76:79], s49 offen offset:2048 sc1\n"
    "v_mfma_f32_16x16x32_bf16 a[112:115], v[208:211], a[84:87], a[112:115]\n"
    "s_waitcnt lgkmcnt(7)\n"
    "ds_read_b64_tr_b16 v[238:239], v171 offset:10240\n"
    "buffer_load_dwordx4 v[8:11], v128, s[76:79], s49 offen offset:3072 sc1\n"
    "v_mfma_f32_16x16x32_bf16 a[116:119], v[216:219], a[84:87], a[116:119]\n"
    "ds_read_b64_tr_b16 v[208:209], v136 offset:0\n"
    "buffer_load_dwordx4 v[12:15], v129, s[76:79], s49 offen offset:3072 sc1\n"
    "v_mfma_f32_16x16x32_bf16 a[116:119], v[220:223], a[92:95], a[116:119]\n"
    "s_add_u32 s49, s49, 4096\n"
    "ds_read_b64_tr_b16 v[210:211], v136 offset:2048\n"
    "v_mfma_f32_16x16x32_bf16 v[116:119], v[220:223], a[88:91], v[116:119]\n"
    "ds_read_b64_tr_b16 v[212:213], v136 offset:8192\n"
    "buffer_load_dwordx4 v[16:19], v128, s[76:79], s49 offen offset:2048 sc1\n"
    "v_mfma_f32_16x16x32_bf16 v[116:119], v[216:219], a[80:83], v[116:119]\n"
    "s_waitcnt lgkmcnt(7)\n"
    "ds_read_b64_tr_b16 v[214:215], v136 offset:10240\n"
    "buffer_load_dwordx4 v[20:23], v129, s[76:79], s49 offen offset:2048 sc1\n"
    "v_mfma_f32_16x16x32_bf16 v[120:123], v[224:227], a[80:83], v[120:123]\n"
    "ds_read_b64_tr_b16 v[216:217], v137 offset:0\n"
    "buffer_load_dwordx4 v[24:27], v128, s[76:79], s49 offen offset:3072 sc1\n"
    "v_mfma_f32_16x16x32_bf16 v[120:123], v[228:231], a[88:91], v[120:123]\n"
    "ds_read_b64_tr_b16 v[218:219], v137 offset:2048\n"
    "buffer_load_dwordx4 v[28:31], v129, s[76:79], s49 offen offset:3072 sc1\n"
    "v_mfma_f32_16x16x32_bf16 a[120:123], v[228:231], a[92:95], a[120:123]\n"
    "s_add_u32 s49, s49, 4096\n"
    "ds_read_b64_tr_b16 v[220:221], v137 offset:8192\n"
    "v_mfma_f32_16x16x32_bf16 a[120:123], v[224:227], a[84:87], a[120:123]\n"
    "s_waitcnt lgkmcnt(7)\n"
    "ds_read_b64_tr_b16 v[222:223], v137 offset:10240\n"
    "buffer_load_dwordx4 v[32:35], v128, s[76:79], s49 offen offset:2048 sc1\n"
    "v_mfma_f32_16x16x32_bf16 a[124:127], v[232:235], a[84:87], a[124:127]\n"
    "ds_read_b64_tr_b16 v[224:225], v138 offset:0\n"
    "v_mfma_f32_16x16x32_bf16 a[124:127], v[236:239], a[92:95], a[124:127]\n"
    "ds_read_b64_tr_b16 v[226:227], v138 offset:2048\n"
    "buffer_load_dwordx4 v[36:39], v129, s[76:79], s49 offen offset:2048 sc1\n"
    "v_mfma_f32_16x16x32_bf16 v[124:127], v[236:239], a[88:91], v[124:127]\n"
    "s_waitcnt vmcnt(12)\n"
    "ds_read_b64_tr_b16 v[228:229], v138 offset:8192\n"
    "v_mfma_f32_16x16x32_bf16 v[124:127], v[232:235], a[80:83], v[124:127]\n"
    "s_waitcnt lgkmcnt(7)\n"
    "ds_read_b64_tr_b16 v[230:231], v138 offset:10240\n"
    "buffer_load_dwordx4 v[40:43], v128, s[76:79], s49 offen offset:3072 sc1\n"
    "v_mfma_f32_16x16x32_bf16 v[112:115], v[208:211], a[96:99], v[112:115]\n"
    "ds_read_b64_tr_b16 v[232:233], v139 offset:0\n"
    "v_mfma_f32_16x16x32_bf16 v[112:115], v[212:215], a[104:107], v[112:115]\n"
    "ds_read_b64_tr_b16 v[234:235], v139 offset:2048\n"
    "buffer_load_dwordx4 v[44:47], v129, s[76:79], s49 offen offset:3072 sc1\n"
    "v_mfma_f32_16x16x32_bf16 a[112:115], v[212:215], a[108:111], a[112:115]\n"
    "ds_read_b64_tr_b16 v[236:237], v139 offset:8192\n"
    "v_mfma_f32_16x16x32_bf16 a[112:115], v[208:211], a[100:103], a[112:115]\n"
    "s_waitcnt lgkmcnt(7)\n"
    "ds_read_b64_tr_b16 v[238:239], v139 offset:10240\n"
    "v_mfma_f32_16x16x32_bf16 a[116:119], v[216:219], a[100:103], a[116:119]\n"
    "s_add_u32 s49, s49, 4096\n"
    "ds_read_b64_tr_b16 v[208:209], v136 offset:16384\n"
    "v_mfma_f32_16x16x32_bf16 a[116:119], v[220:223], a[108:111], a[116:119]\n"
    "ds_read_b64_tr_b16 v[210:211], v136 offset:18432\n"
    "buffer_load_dwordx4 v[48:51], v128, s[76:79], s49 offen offset:2048 sc1\n"
    "v_mfma_f32_16x16x32_bf16 v[116:119], v[220:223], a[104:107], v[116:119]\n"
    "ds_read_b64_tr_b16 v[212:213], v136 offset:24576\n"
    "v_mfma_f32_16x16x32_bf16 v[116:119], v[216:219], a[96:99], v[116:119]\n"
    "s_waitcnt lgkmcnt(7)\n"
    "ds_read_b64_tr_b16 v[214:215], v136 offset:26624\n"
    "buffer_load_dwordx4 v[52:55], v129, s[76:79], s49 offen offset:2048 sc1\n"
    "v_mfma_f32_16x16x32_bf16 v[120:123], v[224:227], a[96:99], v[120:123]\n"
    "ds_read_b64_tr_b16 v[216:217], v137 offset:16384\n"
    "v_mfma_f32_16x16x32_bf16 v[120:123], v[228:231], a[104:107], v[120:123]\n"
    "ds_read_b64_tr_b16 v[218:219], v137 offset:18432\n"
    "buffer_load_dwordx4 v[56:59], v128, s[76:79], s49 offen offset:3072 sc1\n"
    "v_mfma_f32_16x16x32_bf16 a[120:123], v[228:231], a[108:111], a[120:123]\n"
    "ds_read_b64_tr_b16 v[220:221], v137 offset:24576\n"
    "v_mfma_f32_16x16x32_bf16 a[120:123], v[224:227], a[100:103], a[120:123]\n"
    "s_waitcnt lgkmcnt(7)\n"
    "ds_read_b64_tr_b16 v[222:223], v137 offset:26624\n"
    "buffer_load_dwordx4 v[60:63], v129, s[76:79], s49 offen offset:3072 sc1\n"
    "v_mfma_f32_16x16x32_bf16 a[124:127], v[232:235], a[100:103], a[124:127]\n"
    "ds_read_b64_tr_b16 v[224:225], v138 offset:16384\n"
    "v_mfma_f32_16x16x32_bf16 a[124:127], v[236:239], a[108:111], a[124:127]\n"
    "s_add_u32 s49, s49, 4096\n"
    "ds_read_b64_tr_b16 v[226:227], v138 offset:18432\n"
    "v_mfma_f32_16x16x32_bf16 v[124:127], v[236:239], a[104:107], v[124:127]\n"
    "s_waitcnt vmcnt(12)\n"
    "ds_read_b64_tr_b16 v[228:229], v138 offset:24576\n"
    "buffer_load_dwordx4 v[64:67], v128, s[76:79], s49 offen offset:2048 sc1\n"
    "v_mfma_f32_16x16x32_bf16 v[124:127], v[232:235], a[96:99], v[124:127]\n"
    "s_waitcnt lgkmcnt(7)\n"
    "ds_read_b64_tr_b16 v[230:231], v138 offset:26624\n"
    "v_mfma_f32_16x16x32_bf16 v[112:115], v[208:211], v[0:3], v[112:115]\n"
    "ds_read_b64_tr_b16 v[232:233], v139 offset:16384\n"
    "buffer_load_dwordx4 v[68:71], v129, s[76:79], s49 offen offset:2048 sc1\n"
    "v_mfma_f32_16x16x32_bf16 v[112:115], v[212:215], v[8:11], v[112:115]\n"
    "ds_read_b64_tr_b16 v[234:235], v139 offset:18432\n"
    "v_mfma_f32_16x16x32_bf16 a[112:115], v[212:215], v[12:15], a[112:115]\n"
    "ds_read_b64_tr_b16 v[236:237], v139 offset:24576\n"
    "buffer_load_dwordx4 v[72:75], v128, s[76:79], s49 offen offset:3072 sc1\n"
    "v_mfma_f32_16x16x32_bf16 a[112:115], v[208:211], v[4:7], a[112:115]\n"
    "s_waitcnt lgkmcnt(7)\n"
    "ds_read_b64_tr_b16 v[238:239], v139 offset:26624\n"
    "v_mfma_f32_16x16x32_bf16 a[116:119], v[216:219], v[4:7], a[116:119]\n"
    "ds_read_b64_tr_b16 v[208:209], v168 offset:16384\n"
    "buffer_load_dwordx4 v[76:79], v129, s[76:79], s49 offen offset:3072 sc1\n"
    "v_mfma_f32_16x16x32_bf16 a[116:119], v[220:223], v[12:15], a[116:119]\n"
    "ds_read_b64_tr_b16 v[210:211], v168 offset:18432\n"
    "v_mfma_f32_16x16x32_bf16 v[116:119], v[220:223], v[8:11], v[116:119]\n"
    "s_add_u32 s49, s49, 4096\n"
    "ds_read_b64_tr_b16 v[212:213], v168 offset:24576\n"
    "v_mfma_f32_16x16x32_bf16 v[116:119], v[216:219], v[0:3], v[116:119]\n"
    "s_waitcnt lgkmcnt(7)\n"
    "ds_read_b64_tr_b16 v[214:215], v168 offset:26624\n"
    "buffer_load_dwordx4 v[80:83], v128, s[76:79], s49 offen offset:2048 sc1\n"
    "v_mfma_f32_16x16x32_bf16 v[120:123], v[224:227], v[0:3], v[120:123]\n"
    "ds_read_b64_tr_b16 v[216:217], v169 offset:16384\n"
    "v_mfma_f32_16x16x32_bf16 v[120:123], v[228:231], v[8:11], v[120:123]\n"
    "ds_read_b64_tr_b16 v[218:219], v169 offset:18432\n"
    "buffer_load_dwordx4 v[84:87], v129, s[76:79], s49 offen offset:2048 sc1\n"
    "v_mfma_f32_16x16x32_bf16 a[120:123], v[228:231], v[12:15], a[120:123]\n"
    "ds_read_b64_tr_b16 v[220:221], v169 offset:24576\n"
    "v_mfma_f32_16x16x32_bf16 a[120:123], v[224:227], v[4:7], a[120:123]\n"
    "s_waitcnt lgkmcnt(7)\n"
    "ds_read_b64_tr_b16 v[222:223], v169 offset:26624\n"
    "buffer_load_dwordx4 v[88:91], v128, s[76:79], s49 offen offset:3072 sc1\n"
    "v_mfma_f32_16x16x32_bf16 a[124:127], v[232:235], v[4:7], a[124:127]\n"
    "ds_read_b64_tr_b16 v[224:225], v170 offset:16384\n"
    "v_mfma_f32_16x16x32_bf16 a[124:127], v[236:239], v[12:15], a[124:127]\n"
    "ds_read_b64_tr_b16 v[226:227], v170 offset:18432\n"
    "buffer_load_dwordx4 v[92:95], v129, s[76:79], s49 offen offset:3072 sc1\n"
    "v_mfma_f32_16x16x32_bf16 v[124:127], v[236:239], v[8:11], v[124:127]\n"
    "s_waitcnt vmcnt(16)\n"
    "ds_read_b64_tr_b16 v[228:229], v170 offset:24576\n"
    "v_mfma_f32_16x16x32_bf16 v[124:127], v[232:235], v[0:3], v[124:127]\n"
    "s_waitcnt lgkmcnt(7)\n"
    "ds_read_b64_tr_b16 v[230:231], v170 offset:26624\n"
    "v_mfma_f32_16x16x32_bf16 v[112:115], v[208:211], v[16:19], v[112:115]\n"
    "s_add_u32 s49, s49, 4096\n"
    "ds_read_b64_tr_b16 v[232:233], v171 offset:16384\n"
    "v_mfma_f32_16x16x32_bf16 v[112:115], v[212:215], v[24:27], v[112:115]\n"
    "ds_read_b64_tr_b16 v[234:235], v171 offset:18432\n"
    "buffer_load_dwordx4 v[96:99], v128, s[76:79], s49 offen offset:2048 sc1\n"
    "v_mfma_f32_16x16x32_bf16 a[112:115], v[212:215], v[28:31], a[112:115]\n"
    "ds_read_b64_tr_b16 v[236:237], v171 offset:24576\n"
    "v_mfma_f32_16x16x32_bf16 a[112:115], v[208:211], v[20:23], a[112:115]\n"
    "s_waitcnt lgkmcnt(7)\n"
    "ds_read_b64_tr_b16 v[238:239], v171 offset:26624\n"
    "buffer_load_dwordx4 v[100:103], v129, s[76:79], s49 offen offset:2048 sc1\n"
    "v_mfma_f32_16x16x32_bf16 a[116:119], v[216:219], v[20:23], a[116:119]\n"
    "ds_read_b64_tr_b16 v[208:209], v140 offset:49152\n"
    "v_mfma_f32_16x16x32_bf16 a[116:119], v[220:223], v[28:31], a[116:119]\n"
    "ds_read_b64_tr_b16 v[210:211], v140 offset:51200\n"
    "buffer_load_dwordx4 v[104:107], v128, s[76:79], s49 offen offset:3072 sc1\n"
    "v_mfma_f32_16x16x32_bf16 v[116:119], v[220:223], v[24:27], v[116:119]\n"
    "ds_read_b64_tr_b16 v[212:213], v140 offset:57344\n"
    "v_mfma_f32_16x16x32_bf16 v[116:119], v[216:219], v[16:19], v[116:119]\n"
    "s_waitcnt lgkmcnt(7)\n"
    "ds_read_b64_tr_b16 v[214:215], v140 offset:59392\n"
    "buffer_load_dwordx4 v[108:111], v129, s[76:79], s49 offen offset:3072 sc1\n"
    "v_mfma_f32_16x16x32_bf16 v[120:123], v[224:227], v[16:19], v[120:123]\n"
    "ds_read_b64_tr_b16 v[216:217], v141 offset:49152\n"
    "v_mfma_f32_16x16x32_bf16 v[120:123], v[228:231], v[24:27], v[120:123]\n"
    "s_add_u32 s49, s49, 4096\n"
    "ds_read_b64_tr_b16 v[218:219], v141 offset:51200\n"
    "v_mfma_f32_16x16x32_bf16 a[120:123], v[228:231], v[28:31], a[120:123]\n"
    "ds_read_b64_tr_b16 v[220:221], v141 offset:57344\n"
    "v_mfma_f32_16x16x32_bf16 a[120:123], v[224:227], v[20:23], a[120:123]\n"
    "s_waitcnt lgkmcnt(7)\n"
    "ds_read_b64_tr_b16 v[222:223], v141 offset:59392\n"
    "v_mfma_f32_16x16x32_bf16 a[124:127], v[232:235], v[20:23], a[124:127]\n"
    "ds_read_b64_tr_b16 v[224:225], v142 offset:49152\n"
    "v_mfma_f32_16x16x32_bf16 a[124:127], v[236:239], v[28:31], a[124:127]\n"
    "ds_read_b64_tr_b16 v[226:227], v142 offset:51200\n"
    "v_mfma_f32_16x16x32_bf16 v[124:127], v[236:239], v[24:27], v[124:127]\n"
    "s_waitcnt vmcnt(16)\n"
    "ds_read_b64_tr_b16 v[228:229], v142 offset:57344\n"
    "v_mfma_f32_16x16x32_bf16 v[124:127], v[232:235], v[16:19], v[124:127]\n"
    "s_waitcnt lgkmcnt(7)\n"
    "ds_read_b64_tr_b16 v[230:231], v142 offset:59392\n"
    "v_mfma_f32_16x16x32_bf16 v[112:115], v[208:211], v[32:35], v[112:115]\n"
    "ds_read_b64_tr_b16 v[232:233], v143 offset:49152\n"
    "v_mfma_f32_16x16x32_bf16 v[112:115], v[212:215], v[40:43], v[112:115]\n"
    "s_nop 0\n"
    "s_barrier\n"
    "v_mfma_f32_16x16x32_bf16 a[112:115], v[212:215], v[44:47], a[112:115]\n"
    "ds_read_b64_tr_b16 v[234:235], v143 offset:51200\n"
    "ds_read_b64_tr_b16 v[236:237], v143 offset:57344\n"
    "v_mfma_f32_16x16x32_bf16 a[112:115], v[208:211], v[36:39], a[112:115]\n"
    "s_waitcnt lgkmcnt(7)\n"
    "ds_read_b64_tr_b16 v[238:239], v143 offset:59392\n"
    "v_mfma_f32_16x16x32_bf16 a[116:119], v[216:219], v[36:39], a[116:119]\n"
    "ds_read_b64_tr_b16 v[208:209], v136 offset:32768\n"
    "v_mfma_f32_16x16x32_bf16 a[116:119], v[220:223], v[44:47], a[116:119]\n"
    "ds_read_b64_tr_b16 v[210:211], v136 offset:34816\n"
    "v_mfma_f32_16x16x32_bf16 v[116:119], v[220:223], v[40:43], v[116:119]\n"
    "ds_read_b64_tr_b16 v[212:213], v136 offset:40960\n"
    "v_mfma_f32_16x16x32_bf16 v[116:119], v[216:219], v[32:35], v[116:119]\n"
    "s_waitcnt lgkmcnt(7)\n"
    "ds_read_b64_tr_b16 v[214:215], v136 offset:43008\n"
    "v_mfma_f32_16x16x32_bf16 v[120:123], v[224:227], v[32:35], v[120:123]\n"
    "ds_read_b64_tr_b16 v[216:217], v137 offset:32768\n"
    "v_mfma_f32_16x16x32_bf16 v[120:123], v[228:231], v[40:43], v[120:123]\n"
    "ds_read_b64_tr_b16 v[218:219], v137 offset:34816\n"
    "v_mfma_f32_16x16x32_bf16 a[120:123], v[228:231], v[44:47], a[120:123]\n"
    "ds_read_b64_tr_b16 v[220:221], v137 offset:40960\n"
    "v_mfma_f32_16x16x32_bf16 a[120:123], v[224:227], v[36:39], a[120:123]\n"
    "s_waitcnt lgkmcnt(7)\n"
    "ds_read_b64_tr_b16 v[222:223], v137 offset:43008\n"
    "v_mfma_f32_16x16x32_bf16 a[124:127], v[232:235], v[36:39], a[124:127]\n"
    "ds_read_b64_tr_b16 v[224:225], v138 offset:32768\n"
    "v_mfma_f32_16x16x32_bf16 a[124:127], v[236:239], v[44:47], a[124:127]\n"
    "ds_read_b64_tr_b16 v[226:227], v138 offset:34816\n"
    "v_mfma_f32_16x16x32_bf16 v[124:127], v[236:239], v[40:43], v[124:127]\n"
    "s_waitcnt vmcnt(12)\n"
    "ds_read_b64_tr_b16 v[228:229], v138 offset:40960\n"
    "v_mfma_f32_16x16x32_bf16 v[124:127], v[232:235], v[32:35], v[124:127]\n"
    "s_waitcnt lgkmcnt(7)\n"
    "ds_read_b64_tr_b16 v[230:231], v138 offset:43008\n"
    "v_mfma_f32_16x16x32_bf16 v[112:115], v[208:211], v[48:51], v[112:115]\n"
    "s_nop 0\n"
    "ds_read_b64_tr_b16 v[232:233], v139 offset:32768\n"
    "v_mfma_f32_16x16x32_bf16 v[112:115], v[212:215], v[56:59], v[112:115]\n"
    "ds_read_b64_tr_b16 v[234:235], v139 offset:34816\n"
    "v_lshlrev_b32 v192, 16, v240\n"
    "v_mfma_f32_16x16x32_bf16 a[112:115], v[212:215], v[60:63], a[112:115]\n"
    "ds_read_b64_tr_b16 v[236:237], v139 offset:40960\n"
    "v_and_b32 v193, 0xffff0000, v240\n"
    "v_mfma_f32_16x16x32_bf16 a[112:115], v[208:211], v[52:55], a[112:115]\n"
    "s_waitcnt lgkmcnt(7)\n"
    "ds_read_b64_tr_b16 v[238:239], v139 offset:43008\n"
    "v_lshlrev_b32 v194, 16, v241\n"
    "v_mfma_f32_16x16x32_bf16 a[116:119], v[216:219], v[52:55], a[116:119]\n"
    "ds_read_b64_tr_b16 v[208:209], v136 offset:49152\n"
    "v_and_b32 v195, 0xffff0000, v241\n"
    "v_mfma_f32_16x16x32_bf16 a[116:119], v[220:223], v[60:63], a[116:119]\n"
    "ds_read_b64_tr_b16 v[210:211], v136 offset:51200\n"
    "v_lshlrev_b32 v196, 16, v242\n"
    "v_mfma_f32_16x16x32_bf16 v[116:119], v[220:223], v[56:59], v[116:119]\n"
    "ds_read_b64_tr_b16 v[212:213], v136 offset:57344\n"
    "v_and_b32 v197, 0xffff0000, v242\n"
    "v_mfma_f32_16x16x32_bf16 v[116:119], v[216:219], v[48:51], v[116:119]\n"
    "s_waitcnt lgkmcnt(7)\n"
    "ds_read_b64_tr_b16 v[214:215], v136 offset:59392\n"
    "v_lshlrev_b32 v198, 16, v243\n"
    "v_mfma_f32_16x16x32_bf16 v[120:123], v[224:227], v[48:51], v[120:123]\n"
    "ds_read_b64_tr_b16 v[216:217], v137 offset:49152\n"
    "v_and_b32 v199, 0xffff0000, v243\n"
    "v_mfma_f32_16x16x32_bf16 v[120:123], v[228:231], v[56:59], v[120:123]\n"
    "ds_read_b64_tr_b16 v[218:219], v137 offset:51200\n"
    "v_lshlrev_b32 v200, 16, v244\n"
    "v_mfma_f32_16x16x32_bf16 a[120:123], v[228:231], v[60:63], a[120:123]\n"
    "ds_read_b64_tr_b16 v[220:221], v137 offset:57344\n"
    "v_and_b32 v201, 0xffff0000, v244\n"
    "v_mfma_f32_16x16x32_bf16 a[120:123], v[224:227], v[52:55], a[120:123]\n"
    "s_waitcnt lgkmcnt(7)\n"
    "ds_read_b64_tr_b16 v[222:223], v137 offset:59392\n"
    "v_lshlrev_b32 v202, 16, v245\n"
    "v_mfma_f32_16x16x32_bf16 a[124:127], v[232:235], v[52:55], a[124:127]\n"
    "ds_read_b64_tr_b16 v[224:225], v138 offset:49152\n"
    "v_and_b32 v203, 0xffff0000, v245\n"
    "v_mfma_f32_16x16x32_bf16 a[124:127], v[236:239], v[60:63], a[124:127]\n"
    "ds_read_b64_tr_b16 v[226:227], v138 offset:51200\n"
    "v_lshlrev_b32 v204, 16, v246\n"
    "v_mfma_f32_16x16x32_bf16 v[124:127], v[236:239], v[56:59], v[124:127]\n"
    "s_waitcnt vmcnt(8)\n"
    "ds_read_b64_tr_b16 v[228:229], v138 offset:57344\n"
    "v_and_b32 v205, 0xffff0000, v246\n"
    "v_mfma_f32_16x16x32_bf16 v[124:127], v[232:235], v[48:51], v[124:127]\n"
    "s_waitcnt lgkmcnt(7)\n"
    "ds_read_b64_tr_b16 v[230:231], v138 offset:59392\n"
    "v_lshlrev_b32 v206, 16, v247\n"
    "v_mfma_f32_16x16x32_bf16 v[112:115], v[208:211], v[64:67], v[112:115]\n"
    "ds_read_b64_tr_b16 v[232:233], v139 offset:49152\n"
    "v_and_b32 v207, 0xffff0000, v247\n"
    "v_mfma_f32_16x16x32_bf16 v[112:115], v[212:215], v[72:75], v[112:115]\n"
    "ds_read_b64_tr_b16 v[234:235], v139 offset:51200\n"
    "v_mfma_f32_16x16x32_bf16 a[112:115], v[212:215], v[76:79], a[112:115]\n"
    "ds_read_b64_tr_b16 v[236:237], v139 offset:57344\n"
    "v_mfma_f32_16x16x32_bf16 a[112:115], v[208:211], v[68:71], a[112:115]\n"
    "s_waitcnt lgkmcnt(7)\n"
    "ds_read_b64_tr_b16 v[238:239], v139 offset:59392\n"
    "v_mfma_f32_16x16x32_bf16 a[116:119], v[216:219], v[68:71], a[116:119]\n"
    "ds_read_b64_tr_b16 v[208:209], v140 offset:0\n"
    "v_mfma_f32_16x16x32_bf16 a[116:119], v[220:223], v[76:79], a[116:119]\n"
    "ds_read_b64_tr_b16 v[210:211], v140 offset:2048\n"
    "v_mfma_f32_16x16x32_bf16 v[116:119], v[220:223], v[72:75], v[116:119]\n"
    "ds_read_b64_tr_b16 v[212:213], v140 offset:8192\n"
    "v_mfma_f32_16x16x32_bf16 v[116:119], v[216:219], v[64:67], v[116:119]\n"
    "s_waitcnt lgkmcnt(7)\n"
    "ds_read_b64_tr_b16 v[214:215], v140 offset:10240\n"
    "v_mfma_f32_16x16x32_bf16 v[120:123], v[224:227], v[64:67], v[120:123]\n"
    "ds_read_b64_tr_b16 v[216:217], v141 offset:0\n"
    "v_mfma_f32_16x16x32_bf16 v[120:123], v[228:231], v[72:75], v[120:123]\n"
    "ds_read_b64_tr_b16 v[218:219], v141 offset:2048\n"
    "v_mfma_f32_16x16x32_bf16 a[120:123], v[228:231], v[76:79], a[120:123]\n"
    "ds_read_b64_tr_b16 v[220:221], v141 offset:8192\n"
    "v_mfma_f32_16x16x32_bf16 a[120:123], v[224:227], v[68:71], a[120:123]\n"
    "s_waitcnt lgkmcnt(7)\n"
    "ds_read_b64_tr_b16 v[222:223], v141 offset:10240\n"
    "v_mfma_f32_16x16x32_bf16 a[124:127], v[232:235], v[68:71], a[124:127]\n"
    "ds_read_b64_tr_b16 v[224:225], v142 offset:0\n"
    "v_mfma_f32_16x16x32_bf16 a[124:127], v[236:239], v[76:79], a[124:127]\n"
    "ds_read_b64_tr_b16 v[226:227], v142 offset:2048\n"
    "v_mfma_f32_16x16x32_bf16 v[124:127], v[236:239], v[72:75], v[124:127]\n"
    "s_waitcnt vmcnt(4)\n"
    "ds_read_b64_tr_b16 v[228:229], v142 offset:8192\n"
    "v_mfma_f32_16x16x32_bf16 v[124:127], v[232:235], v[64:67], v[124:127]\n"
    "s_waitcnt lgkmcnt(7)\n"
    "ds_read_b64_tr_b16 v[230:231], v142 offset:10240\n"
    "v_mfma_f32_16x16x32_bf16 v[112:115], v[208:211], v[80:83], v[112:115]\n"
    "ds_read_b64_tr_b16 v[232:233], v143 offset:0\n"
    "v_mfma_f32_16x16x32_bf16 v[112:115], v[212:215], v[88:91], v[112:115]\n"
    "ds_read_b64_tr_b16 v[234:235], v143 offset:2048\n"
    "v_mfma_f32_16x16x32_bf16 a[112:115], v[212:215], v[92:95], a[112:115]\n"
    "ds_read_b64_tr_b16 v[236:237], v143 offset:8192\n"
    "v_mfma_f32_16x16x32_bf16 a[112:115], v[208:211], v[84:87], a[112:115]\n"
    "s_waitcnt lgkmcnt(7)\n"
    "ds_read_b64_tr_b16 v[238:239], v143 offset:10240\n"
    "v_mfma_f32_16x16x32_bf16 a[116:119], v[216:219], v[84:87], a[116:119]\n"
    "ds_read_b64_tr_b16 v[208:209], v140 offset:16384\n"
    "v_mfma_f32_16x16x32_bf16 a[116:119], v[220:223], v[92:95], a[116:119]\n"
    "ds_read_b64_tr_b16 v[210:211], v140 offset:18432\n"
    "v_mfma_f32_16x16x32_bf16 v[116:119], v[220:223], v[88:91], v[116:119]\n"
    "ds_read_b64_tr_b16 v[212:213], v140 offset:24576\n"
    "v_mfma_f32_16x16x32_bf16 v[116:119], v[216:219], v[80:83], v[116:119]\n"
    "s_waitcnt lgkmcnt(7)\n"
    "ds_read_b64_tr_b16 v[214:215], v140 offset:26624\n"
    "v_mfma_f32_16x16x32_bf16 v[120:123], v[224:227], v[80:83], v[120:123]\n"
    "ds_read_b64_tr_b16 v[216:217], v141 offset:16384\n"
    "v_mfma_f32_16x16x32_bf16 v[120:123], v[228:231], v[88:91], v[120:123]\n"
    "ds_read_b64_tr_b16 v[218:219], v141 offset:18432\n"
    "v_mfma_f32_16x16x32_bf16 a[120:123], v[228:231], v[92:95], a[120:123]\n"
    "ds_read_b64_tr_b16 v[220:221], v141 offset:24576\n"
    "v_mfma_f32_16x16x32_bf16 a[120:123], v[224:227], v[84:87], a[120:123]\n"
    "s_waitcnt lgkmcnt(7)\n"
    "ds_read_b64_tr_b16 v[222:223], v141 offset:26624\n"
    "v_mfma_f32_16x16x32_bf16 a[124:127], v[232:235], v[84:87], a[124:127]\n"
    "ds_read_b64_tr_b16 v[224:225], v142 offset:16384\n"
    "v_mfma_f32_16x16x32_bf16 a[124:127], v[236:239], v[92:95], a[124:127]\n"
    "ds_read_b64_tr_b16 v[226:227], v142 offset:18432\n"
    "v_mfma_f32_16x16x32_bf16 v[124:127], v[236:239], v[88:91], v[124:127]\n"
    "s_waitcnt vmcnt(0)\n"
    "ds_read_b64_tr_b16 v[228:229], v142 offset:24576\n"
    "v_mfma_f32_16x16x32_bf16 v[124:127], v[232:235], v[80:83], v[124:127]\n"
    "s_waitcnt lgkmcnt(7)\n"
    "ds_read_b64_tr_b16 v[230:231], v142 offset:26624\n"
    "ds_read_b64_tr_b16 v[232:233], v143 offset:16384\n"
    "ds_read_b64_tr_b16 v[234:235], v143 offset:18432\n"
    "v_mfma_f32_16x16x32_bf16 a[112:115], v[208:211], v[100:103], a[112:115]\n"
    "ds_read_b64_tr_b16 v[236:237], v143 offset:24576\n"
    "ds_read_b64_tr_b16 v[238:239], v143 offset:26624\n"
    "v_mfma_f32_16x16x32_bf16 a[112:115], v[212:215], v[108:111], a[112:115]\n"
    "s_waitcnt lgkmcnt(8)\n"
    "v_mfma_f32_16x16x32_bf16 a[116:119], v[216:219], v[100:103], a[116:119]\n"
    "v_mfma_f32_16x16x32_bf16 a[116:119], v[220:223], v[108:111], a[116:119]\n"
    "s_waitcnt lgkmcnt(4)\n"
    "v_mfma_f32_16x16x32_bf16 a[120:123], v[224:227], v[100:103], a[120:123]\n"
    "v_mfma_f32_16x16x32_bf16 a[120:123], v[228:231], v[108:111], a[120:123]\n"
    "s_waitcnt lgkmcnt(0)\n"
    "v_mfma_f32_16x16x32_bf16 a[124:127], v[232:235], v[100:103], a[124:127]\n"
    "v_mfma_f32_16x16x32_bf16 a[124:127], v[236:239], v[108:111], a[124:127]\n"
    "v_mfma_f32_16x16x32_bf16 v[112:115], v[208:211], v[96:99], v[112:115]\n"
    "v_mfma_f32_16x16x32_bf16 v[112:115], v[212:215], v[104:107], v[112:115]\n"
    "ds_write_b128 v131, a[112:115] offset:0\n"
    "v_mfma_f32_16x16x32_bf16 v[116:119], v[216:219], v[96:99], v[116:119]\n"
    "v_mfma_f32_16x16x32_bf16 v[116:119], v[220:223], v[104:107], v[116:119]\n"
    "ds_write_b128 v131, a[116:119] offset:1024\n"
    "v_mfma_f32_16x16x32_bf16 v[120:123], v[224:227], v[96:99], v[120:123]\n"
    "v_mfma_f32_16x16x32_bf16 v[120:123], v[228:231], v[104:107], v[120:123]\n"
    "ds_write_b128 v131, a[120:123] offset:2048\n"
    "v_mfma_f32_16x16x32_bf16 v[124:127], v[232:235], v[96:99], v[124:127]\n"
    "v_mfma_f32_16x16x32_bf16 v[124:127], v[236:239], v[104:107], v[124:127]\n"
    "ds_write_b128 v131, a[124:127] offset:3072\n"
    "s_nop 2\n"
    "v_pk_fma_f32 v[192:193], v[176:177], v[112:113], v[192:193]\n"
    "v_pk_fma_f32 v[194:195], v[178:179], v[114:115], v[194:195]\n"
    "v_pk_fma_f32 v[196:197], v[180:181], v[116:117], v[196:197]\n"
    "v_pk_fma_f32 v[198:199], v[182:183], v[118:119], v[198:199]\n"
    "v_pk_fma_f32 v[200:201], v[184:185], v[120:121], v[200:201]\n"
    "v_pk_fma_f32 v[202:203], v[186:187], v[122:123], v[202:203]\n"
    "v_pk_fma_f32 v[204:205], v[188:189], v[124:125], v[204:205]\n"
    "v_pk_fma_f32 v[206:207], v[190:191], v[126:127], v[206:207]\n"
    "s_waitcnt lgkmcnt(0)\n"
    "s_barrier\n"
    "ds_read_b128 v[16:19], v132 offset:0\n"
    "ds_read_b128 v[20:23], v132 offset:1024\n"
    "ds_read_b128 v[24:27], v132 offset:2048\n"
    "ds_read_b128 v[28:31], v132 offset:3072\n"
    "s_waitcnt lgkmcnt(3)\n"
    "v_pk_fma_f32 v[192:193], v[176:177], v[16:17], v[192:193]\n"
    "v_pk_fma_f32 v[194:195], v[178:179], v[18:19], v[194:195]\n"
    "v_cvt_pk_bf16_f32 v0, v192, v193\n"
    "v_cvt_pk_bf16_f32 v1, v194, v195\n"
    "s_waitcnt lgkmcnt(2)\n"
    "v_pk_fma_f32 v[196:197], v[180:181], v[20:21], v[196:197]\n"
    "v_pk_fma_f32 v[198:199], v[182:183], v[22:23], v[198:199]\n"
    "v_cvt_pk_bf16_f32 v2, v196, v197\n"
    "v_cvt_pk_bf16_f32 v3, v198, v199\n"
    "s_nop 1\n"
    "v_permlane32_swap_b32 v0, v2\n"
    "v_permlane32_swap_b32 v1, v3\n"
    "s_nop 0\n"
    "v_permlane16_swap_b32 v0, v2\n"
    "v_permlane16_swap_b32 v1, v3\n"
    "buffer_store_dwordx4 v[0:3], v133, s[88:91], s68 offen offset:0 sc1\n"
    "s_waitcnt lgkmcnt(1)\n"
    "v_pk_fma_f32 v[200:201], v[184:185], v[24:25], v[200:201]\n"
    "v_pk_fma_f32 v[202:203], v[186:187], v[26:27], v[202:203]\n"
    "v_cvt_pk_bf16_f32 v4, v200, v201\n"
    "v_cvt_pk_bf16_f32 v5, v202, v203\n"
    "s_waitcnt lgkmcnt(0)\n"
    "v_pk_fma_f32 v[204:205], v[188:189], v[28:29], v[204:205]\n"
    "v_pk_fma_f32 v[206:207], v[190:191], v[30:31], v[206:207]\n"
    "v_cvt_pk_bf16_f32 v6, v204, v205\n"
    "v_cvt_pk_bf16_f32 v7, v206, v207\n"
    "s_nop 1\n"
    "v_permlane32_swap_b32 v4, v6\n"
    "v_permlane32_swap_b32 v5, v7\n"
    "s_nop 0\n"
    "v_permlane16_swap_b32 v4, v6\n"
    "v_permlane16_swap_b32 v5, v7\n"
    "buffer_store_dwordx4 v[4:7], v133, s[88:91], s68 offen offset:64 sc1\n"
    "s_branch FIN_%=\n"
    "EPO_%=:\n"
    "s_lshl_b32 s70, %1, 2\n"
    "s_add_u32 s70, s70, s72\n"
    "s_cmp_lg_u32 s70, 1023\n"
    "s_cbranch_scc1 FIN_%=\n"
    "s_mov_b32 s53, 1048576\n"
    "v_lshrrev_b32 v245, 4, v254\n"
    "v_lshlrev_b32 v245, 5, v245\n"
    "EPOL_%=:\n"
    "s_mov_b32 s70, 339968\n"
    "buffer_load_dwordx2 v[0:1], v245, s[76:79], s70 offen sc1\n"
    "s_add_u32 s70, s70, 2048\n"
    "buffer_load_dwordx2 v[2:3], v245, s[76:79], s70 offen sc1\n"
    "s_add_u32 s70, s70, 2048\n"
    "buffer_load_dwordx2 v[4:5], v245, s[76:79], s70 offen sc1\n"
    "s_add_u32 s70, s70, 2048\n"
    "buffer_load_dwordx2 v[6:7], v245, s[76:79], s70 offen sc1\n"
    "s_add_u32 s70, s70, 2048\n"
    "buffer_load_dwordx2 v[8:9], v245, s[76:79], s70 offen sc1\n"
    "s_add_u32 s70, s70, 2048\n"
    "buffer_load_dwordx2 v[10:11], v245, s[76:79], s70 offen sc1\n"
    "s_add_u32 s70, s70, 2048\n"
    "buffer_load_dwordx2 v[12:13], v245, s[76:79], s70 offen sc1\n"
    "s_add_u32 s70, s70, 2048\n"
    "buffer_load_dwordx2 v[14:15], v245, s[76:79], s70 offen sc1\n"
    "s_add_u32 s70, s70, 2048\n"
    "buffer_load_dwordx2 v[16:17], v245, s[76:79], s70 offen sc1\n"
    "s_add_u32 s70, s70, 2048\n"
    "buffer_load_dwordx2 v[18:19], v245, s[76:79], s70 offen sc1\n"
    "s_add_u32 s70, s70, 2048\n"
    "buffer_load_dwordx2 v[20:21], v245, s[76:79], s70 offen sc1\n"
    "s_add_u32 s70, s70, 2048\n"
    "buffer_load_dwordx2 v[22:23], v245, s[76:79], s70 offen sc1\n"
    "s_add_u32 s70, s70, 2048\n"
    "buffer_load_dwordx2 v[24:25], v245, s[76:79], s70 offen sc1\n"
    "s_add_u32 s70, s70, 2048\n"
    "buffer_load_dwordx2 v[26:27], v245, s[76:79], s70 offen sc1\n"
    "s_add_u32 s70, s70, 2048\n"
    "buffer_load_dwordx2 v[28:29], v245, s[76:79], s70 offen sc1\n"
    "s_add_u32 s70, s70, 2048\n"
    "buffer_load_dwordx2 v[30:31], v245, s[76:79], s70 offen sc1\n"
    "s_add_u32 s70, s70, 2048\n"
    "s_waitcnt vmcnt(0)\n"
    "v_mov_b32 v242, 0\n"
    "v_xor_b32 v243, s74, v0\n"
    "v_xor_b32 v244, s75, v1\n"
    "v_or3_b32 v242, v242, v243, v244\n"
    "v_xor_b32 v243, s74, v2\n"
    "v_xor_b32 v244, s75, v3\n"
    "v_or3_b32 v242, v242, v243, v244\n"
    "v_xor_b32 v243, s74, v4\n"
    "v_xor_b32 v244, s75, v5\n"
    "v_or3_b32 v242, v242, v243, v244\n"
    "v_xor_b32 v243, s74, v6\n"
    "v_xor_b32 v244, s75, v7\n"
    "v_or3_b32 v242, v242, v243, v244\n"
    "v_xor_b32 v243, s74, v8\n"
    "v_xor_b32 v244, s75, v9\n"
    "v_or3_b32 v242, v242, v243, v244\n"
    "v_xor_b32 v243, s74, v10\n"
    "v_xor_b32 v244, s75, v11\n"
    "v_or3_b32 v242, v242, v243, v244\n"
    "v_xor_b32 v243, s74, v12\n"
    "v_xor_b32 v244, s75, v13\n"
    "v_or3_b32 v242, v242, v243, v244\n"
    "v_xor_b32 v243, s74, v14\n"
    "v_xor_b32 v244, s75, v15\n"
    "v_or3_b32 v242, v242, v243, v244\n"
    "v_xor_b32 v243, s74, v16\n"
    "v_xor_b32 v244, s75, v17\n"
    "v_or3_b32 v242, v242, v243, v244\n"
    "v_xor_b32 v243, s74, v18\n"
    "v_xor_b32 v244, s75, v19\n"
    "v_or3_b32 v242, v242, v243, v244\n"
    "v_xor_b32 v243, s74, v20\n"
    "v_xor_b32 v244, s75, v21\n"
    "v_or3_b32 v242, v242, v243, v244\n"
    "v_xor_b32 v243, s74, v22\n"
    "v_xor_b32 v244, s75, v23\n"
    "v_or3_b32 v242, v242, v243, v244\n"
    "v_xor_b32 v243, s74, v24\n"
    "v_xor_b32 v244, s75, v25\n"
    "v_or3_b32 v242, v242, v243, v244\n"
    "v_xor_b32 v243, s74, v26\n"
    "v_xor_b32 v244, s75, v27\n"
    "v_or3_b32 v242, v242, v243, v244\n"
    "v_xor_b32 v243, s74, v28\n"
    "v_xor_b32 v244, s75, v29\n"
    "v_or3_b32 v242, v242, v243, v244\n"
    "v_xor_b32 v243, s74, v30\n"
    "v_xor_b32 v244, s75, v31\n"
    "v_or3_b32 v242, v242, v243, v244\n"
    "s_sub_u32 s53, s53, 1\n"
    "v_cmp_ne_u32 vcc, 0, v242\n"
    "s_cbranch_scc1 EPOD_%=\n"
    "s_cbranch_vccnz EPOL_%=\n"
    "EPOD_%=:\n"
    "s_mov_b32 s70, 0\n"
    "buffer_store_dwordx2 v[248:249], v250, s[76:79], s70 offen sc1\n"
    "s_branch FIN_%=\n"
    "GRE0_%=:\n"
    "s_mov_b32 s53, 1048576\n"
    "GRE1_%=:\n"
    "buffer_load_dwordx4 v[8:11], v254, s[76:79], s49 offen sc1\n"
    "buffer_load_dwordx4 v[12:15], v254, s[76:79], s52 offen sc1\n"
    "s_waitcnt vmcnt(0)\n"
    "v_mov_b32 v246, 0\n"
    "v_xor_b32 v244, s74, v9\n"
    "v_xor_b32 v245, s74, v11\n"
    "v_or3_b32 v246, v246, v244, v245\n"
    "v_and_b32 v9, 0xffff0000, v8\n"
    "v_lshlrev_b32 v8, 16, v8\n"
    "v_and_b32 v11, 0xffff0000, v10\n"
    "v_lshlrev_b32 v10, 16, v10\n"
    "v_add_f32 v8, v242, v8\n"
    "v_add_f32 v9, v242, v9\n"
    "v_add_f32 v10, v242, v10\n"
    "v_add_f32 v11, v242, v11\n"
    "v_cvt_pk_bf16_f32 v8, 0, v8\n"
    "v_cvt_pk_bf16_f32 v9, 0, v9\n"
    "v_cvt_pk_bf16_f32 v10, 0, v10\n"
    "v_cvt_pk_bf16_f32 v11, 0, v11\n"
    "v_xor_b32 v244, s74, v13\n"
    "v_xor_b32 v245, s74, v15\n"
    "v_or3_b32 v246, v246, v244, v245\n"
    "v_and_b32 v13, 0xffff0000, v12\n"
    "v_lshlrev_b32 v12, 16, v12\n"
    "v_and_b32 v15, 0xffff0000, v14\n"
    "v_lshlrev_b32 v14, 16, v14\n"
    "v_add_f32 v12, v243, v12\n"
    "v_add_f32 v13, v243, v13\n"
    "v_add_f32 v14, v243, v14\n"
    "v_add_f32 v15, v243, v15\n"
    "v_cvt_pk_bf16_f32 v12, 0, v12\n"
    "v_cvt_pk_bf16_f32 v13, 0, v13\n"
    "v_cvt_pk_bf16_f32 v14, 0, v14\n"
    "v_cvt_pk_bf16_f32 v15, 0, v15\n"
    "ds_write_b128 v40, v[8:11] offset:0\n"
    "ds_write_b128 v41, v[12:15] offset:0\n"
    "s_sub_u32 s53, s53, 1\n"
    "v_cmp_ne_u32 vcc, 0, v246\n"
    "s_waitcnt lgkmcnt(0)\n"
    "s_cbranch_scc1 GOK1_%=\n"
    "s_cbranch_vccnz GRE1_%=\n"
    "s_branch GOK1_%=\n"
    "SLOWA_%=:\n"
    "s_waitcnt vmcnt(0)\n"
    "buffer_store_dwordx2 v[248:249], v250, s[76:79], s41 offen sc1\n"
    "v_cmp_ne_u64 vcc, s[74:75], v[240:241]\n"
    "s_mov_b32 s53, 1048576\n"
    "s_cbranch_vccz PDA_%=\n"
    "PLA_%=:\n"
    "buffer_load_dwordx2 v[240:241], v251, s[76:79], s42 offen sc1\n"
    "s_sub_u32 s53, s53, 1\n"
    "s_waitcnt vmcnt(0)\n"
    "v_cmp_ne_u64 vcc, s[74:75], v[240:241]\n"
    "s_cbranch_scc1 PDA_%=\n"
    "s_cbranch_vccnz PLA_%=\n"
    "PDA_%=:\n"
    "s_branch RETA_%=\n"
    "SLOW3_%=:\n"
    "s_waitcnt vmcnt(0)\n"
    "buffer_store_dwordx2 v[248:249], v250, s[76:79], s66 offen sc1\n"
    "v_cmp_ne_u64 vcc, s[74:75], v[240:241]\n"
    "s_mov_b32 s53, 1048576\n"
    "s_cbranch_vccz PD3A_%=\n"
    "PL3A_%=:\n"
    "buffer_load_dwordx2 v[240:241], v251, s[76:79], s67 offen sc1\n"
    "s_sub_u32 s53, s53, 1\n"
    "s_waitcnt vmcnt(0)\n"
    "v_cmp_ne_u64 vcc, s[74:75], v[240:241]\n"
    "s_cbranch_scc1 PD3A_%=\n"
    "s_cbranch_vccnz PL3A_%=\n"
    "PD3A_%=:\n"
    "s_branch RET3_%=\n"
    "FIN_%=:\n"
    "s_endpgm\n"
    "CEND_%=:\n"
    "RARE0_%=:\n"
    "s_nop 7\n"
    "s_nop 7\n"
    "v_max3_f32 v240, v128, v129, v130\n"
    "v_max3_f32 v240, v240, v131, v144\n"
    "v_max3_f32 v240, v240, v145, v146\n"
    "v_max_f32 v240, v240, v147\n"
    "v_mov_b32 v241, v240\n"
    "s_nop 1\n"
    "v_permlane16_swap_b32 v240, v241\n"
    "s_nop 1\n"
    "v_max_f32 v240, v240, v241\n"
    "v_mov_b32 v241, v240\n"
    "s_nop 1\n"
    "v_permlane32_swap_b32 v240, v241\n"
    "s_nop 1\n"
    "v_max_f32 v240, v240, v241\n"
    "v_add_f32 v240, 102.0, v240\n"
    "v_max_f32 v240, 0, v240\n"
    "v_sub_f32 v192, v192, v240\n"
    "v_sub_f32 v193, v193, v240\n"
    "v_sub_f32 v194, v194, v240\n"
    "v_sub_f32 v195, v195, v240\n"
    "v_sub_f32 v128, v128, v240\n"
    "v_sub_f32 v129, v129, v240\n"
    "v_sub_f32 v130, v130, v240\n"
    "v_sub_f32 v131, v131, v240\n"
    "v_sub_f32 v144, v144, v240\n"
    "v_sub_f32 v145, v145, v240\n"
    "v_sub_f32 v146, v146, v240\n"
    "v_sub_f32 v147, v147, v240\n"
    "v_exp_f32 v241, -v240\n"
    "s_nop 0\n"
    "v_mul_f32 v248, v248, v241\n"
    "v_mul_f32 v0, v0, v241\n"
    "v_mul_f32 v1, v1, v241\n"
    "v_mul_f32 v2, v2, v241\n"
    "v_mul_f32 v3, v3, v241\n"
    "v_mul_f32 v8, v8, v241\n"
    "v_mul_f32 v9, v9, v241\n"
    "v_mul_f32 v10, v10, v241\n"
    "v_mul_f32 v11, v11, v241\n"
    "v_mul_f32 v16, v16, v241\n"
    "v_mul_f32 v17, v17, v241\n"
    "v_mul_f32 v18, v18, v241\n"
    "v_mul_f32 v19, v19, v241\n"
    "v_mul_f32 v24, v24, v241\n"
    "v_mul_f32 v25, v25, v241\n"
    "v_mul_f32 v26, v26, v241\n"
    "v_mul_f32 v27, v27, v241\n"
    "v_mul_f32 v32, v32, v241\n"
    "v_mul_f32 v33, v33, v241\n"
    "v_mul_f32 v34, v34, v241\n"
    "v_mul_f32 v35, v35, v241\n"
    "v_mul_f32 v40, v40, v241\n"
    "v_mul_f32 v41, v41, v241\n"
    "v_mul_f32 v42, v42, v241\n"
    "v_mul_f32 v43, v43, v241\n"
    "v_mul_f32 v48, v48, v241\n"
    "v_mul_f32 v49, v49, v241\n"
    "v_mul_f32 v50, v50, v241\n"
    "v_mul_f32 v51, v51, v241\n"
    "v_mul_f32 v56, v56, v241\n"
    "v_mul_f32 v57, v57, v241\n"
    "v_mul_f32 v58, v58, v241\n"
    "v_mul_f32 v59, v59, v241\n"
    "v_mul_f32 v64, v64, v241\n"
    "v_mul_f32 v65, v65, v241\n"
    "v_mul_f32 v66, v66, v241\n"
    "v_mul_f32 v67, v67, v241\n"
    "v_mul_f32 v72, v72, v241\n"
    "v_mul_f32 v73, v73, v241\n"
    "v_mul_f32 v74, v74, v241\n"
    "v_mul_f32 v75, v75, v241\n"
    "v_mul_f32 v80, v80, v241\n"
    "v_mul_f32 v81, v81, v241\n"
    "v_mul_f32 v82, v82, v241\n"
    "v_mul_f32 v83, v83, v241\n"
    "v_mul_f32 v88, v88, v241\n"
    "v_mul_f32 v89, v89, v241\n"
    "v_mul_f32 v90, v90, v241\n"
    "v_mul_f32 v91, v91, v241\n"
    "v_mul_f32 v96, v96, v241\n"
    "v_mul_f32 v97, v97, v241\n"
    "v_mul_f32 v98, v98, v241\n"
    "v_mul_f32 v99, v99, v241\n"
    "v_mul_f32 v104, v104, v241\n"
    "v_mul_f32 v105, v105, v241\n"
    "v_mul_f32 v106, v106, v241\n"
    "v_mul_f32 v107, v107, v241\n"
    "v_mul_f32 v112, v112, v241\n"
    "v_mul_f32 v113, v113, v241\n"
    "v_mul_f32 v114, v114, v241\n"
    "v_mul_f32 v115, v115, v241\n"
    "v_mul_f32 v120, v120, v241\n"
    "v_mul_f32 v121, v121, v241\n"
    "v_mul_f32 v122, v122, v241\n"
    "v_mul_f32 v123, v123, v241\n"
    "v_max3_f32 v240, v132, v133, v134\n"
    "v_max3_f32 v240, v240, v135, v148\n"
    "v_max3_f32 v240, v240, v149, v150\n"
    "v_max_f32 v240, v240, v151\n"
    "v_mov_b32 v241, v240\n"
    "s_nop 1\n"
    "v_permlane16_swap_b32 v240, v241\n"
    "s_nop 1\n"
    "v_max_f32 v240, v240, v241\n"
    "v_mov_b32 v241, v240\n"
    "s_nop 1\n"
    "v_permlane32_swap_b32 v240, v241\n"
    "s_nop 1\n"
    "v_max_f32 v240, v240, v241\n"
    "v_add_f32 v240, 102.0, v240\n"
    "v_max_f32 v240, 0, v240\n"
    "v_sub_f32 v196, v196, v240\n"
    "v_sub_f32 v197, v197, v240\n"
    "v_sub_f32 v198, v198, v240\n"
    "v_sub_f32 v199, v199, v240\n"
    "v_sub_f32 v132, v132, v240\n"
    "v_sub_f32 v133, v133, v240\n"
    "v_sub_f32 v134, v134, v240\n"
    "v_sub_f32 v135, v135, v240\n"
    "v_sub_f32 v148, v148, v240\n"
    "v_sub_f32 v149, v149, v240\n"
    "v_sub_f32 v150, v150, v240\n"
    "v_sub_f32 v151, v151, v240\n"
    "v_exp_f32 v241, -v240\n"
    "s_nop 0\n"
    "v_mul_f32 v249, v249, v241\n"
    "v_mul_f32 v4, v4, v241\n"
    "v_mul_f32 v5, v5, v241\n"
    "v_mul_f32 v6, v6, v241\n"
    "v_mul_f32 v7, v7, v241\n"
    "v_mul_f32 v12, v12, v241\n"
    "v_mul_f32 v13, v13, v241\n"
    "v_mul_f32 v14, v14, v241\n"
    "v_mul_f32 v15, v15, v241\n"
    "v_mul_f32 v20, v20, v241\n"
    "v_mul_f32 v21, v21, v241\n"
    "v_mul_f32 v22, v22, v241\n"
    "v_mul_f32 v23, v23, v241\n"
    "v_mul_f32 v28, v28, v241\n"
    "v_mul_f32 v29, v29, v241\n"
    "v_mul_f32 v30, v30, v241\n"
    "v_mul_f32 v31, v31, v241\n"
    "v_mul_f32 v36, v36, v241\n"
    "v_mul_f32 v37, v37, v241\n"
    "v_mul_f32 v38, v38, v241\n"
    "v_mul_f32 v39, v39, v241\n"
    "v_mul_f32 v44, v44, v241\n"
    "v_mul_f32 v45, v45, v241\n"
    "v_mul_f32 v46, v46, v241\n"
    "v_mul_f32 v47, v47, v241\n"
    "v_mul_f32 v52, v52, v241\n"
    "v_mul_f32 v53, v53, v241\n"
    "v_mul_f32 v54, v54, v241\n"
    "v_mul_f32 v55, v55, v241\n"
    "v_mul_f32 v60, v60, v241\n"
    "v_mul_f32 v61, v61, v241\n"
    "v_mul_f32 v62, v62, v241\n"
    "v_mul_f32 v63, v63, v241\n"
    "v_mul_f32 v68, v68, v241\n"
    "v_mul_f32 v69, v69, v241\n"
    "v_mul_f32 v70, v70, v241\n"
    "v_mul_f32 v71, v71, v241\n"
    "v_mul_f32 v76, v76, v241\n"
    "v_mul_f32 v77, v77, v241\n"
    "v_mul_f32 v78, v78, v241\n"
    "v_mul_f32 v79, v79, v241\n"
    "v_mul_f32 v84, v84, v241\n"
    "v_mul_f32 v85, v85, v241\n"
    "v_mul_f32 v86, v86, v241\n"
    "v_mul_f32 v87, v87, v241\n"
    "v_mul_f32 v92, v92, v241\n"
    "v_mul_f32 v93, v93, v241\n"
    "v_mul_f32 v94, v94, v241\n"
    "v_mul_f32 v95, v95, v241\n"
    "v_mul_f32 v100, v100, v241\n"
    "v_mul_f32 v101, v101, v241\n"
    "v_mul_f32 v102, v102, v241\n"
    "v_mul_f32 v103, v103, v241\n"
    "v_mul_f32 v108, v108, v241\n"
    "v_mul_f32 v109, v109, v241\n"
    "v_mul_f32 v110, v110, v241\n"
    "v_mul_f32 v111, v111, v241\n"
    "v_mul_f32 v116, v116, v241\n"
    "v_mul_f32 v117, v117, v241\n"
    "v_mul_f32 v118, v118, v241\n"
    "v_mul_f32 v119, v119, v241\n"
    "v_mul_f32 v124, v124, v241\n"
    "v_mul_f32 v125, v125, v241\n"
    "v_mul_f32 v126, v126, v241\n"
    "v_mul_f32 v127, v127, v241\n"
    "v_max3_f32 v240, v136, v137, v138\n"
    "v_max3_f32 v240, v240, v139, v152\n"
    "v_max3_f32 v240, v240, v153, v154\n"
    "v_max_f32 v240, v240, v155\n"
    "v_mov_b32 v241, v240\n"
    "s_nop 1\n"
    "v_permlane16_swap_b32 v240, v241\n"
    "s_nop 1\n"
    "v_max_f32 v240, v240, v241\n"
    "v_mov_b32 v241, v240\n"
    "s_nop 1\n"
    "v_permlane32_swap_b32 v240, v241\n"
    "s_nop 1\n"
    "v_max_f32 v240, v240, v241\n"
    "v_add_f32 v240, 102.0, v240\n"
    "v_max_f32 v240, 0, v240\n"
    "v_sub_f32 v200, v200, v240\n"
    "v_sub_f32 v201, v201, v240\n"
    "v_sub_f32 v202, v202, v240\n"
    "v_sub_f32 v203, v203, v240\n"
    "v_sub_f32 v136, v136, v240\n"
    "v_sub_f32 v137, v137, v240\n"
    "v_sub_f32 v138, v138, v240\n"
    "v_sub_f32 v139, v139, v240\n"
    "v_sub_f32 v152, v152, v240\n"
    "v_sub_f32 v153, v153, v240\n"
    "v_sub_f32 v154, v154, v240\n"
    "v_sub_f32 v155, v155, v240\n"
    "v_exp_f32 v241, -v240\n"
    "s_nop 0\n"
    "v_mul_f32 v250, v250, v241\n"
    "v_accvgpr_read_b32 v242, a128\n"
    "v_mul_f32 v242, v242, v241\n"
    "v_accvgpr_write_b32 a128, v242\n"
    "v_accvgpr_read_b32 v243, a129\n"
    "v_mul_f32 v243, v243, v241\n"
    "v_accvgpr_write_b32 a129, v243\n"
    "v_accvgpr_read_b32 v242, a130\n"
    "v_mul_f32 v242, v242, v241\n"
    "v_accvgpr_write_b32 a130, v242\n"
    "v_accvgpr_read_b32 v243, a131\n"
    "v_mul_f32 v243, v243, v241\n"
    "v_accvgpr_write_b32 a131, v243\n"
    "v_accvgpr_read_b32 v242, a136\n"
    "v_mul_f32 v242, v242, v241\n"
    "v_accvgpr_write_b32 a136, v242\n"
    "v_accvgpr_read_b32 v243, a137\n"
    "v_mul_f32 v243, v243, v241\n"
    "v_accvgpr_write_b32 a137, v243\n"
    "v_accvgpr_read_b32 v242, a138\n"
    "v_mul_f32 v242, v242, v241\n"
    "v_accvgpr_write_b32 a138, v242\n"
    "v_accvgpr_read_b32 v243, a139\n"
    "v_mul_f32 v243, v243, v241\n"
    "v_accvgpr_write_b32 a139, v243\n"
    "v_accvgpr_read_b32 v242, a144\n"
    "v_mul_f32 v242, v242, v241\n"
    "v_accvgpr_write_b32 a144, v242\n"
    "v_accvgpr_read_b32 v243, a145\n"
    "v_mul_f32 v243, v243, v241\n"
    "v_accvgpr_write_b32 a145, v243\n"
    "v_accvgpr_read_b32 v242, a146\n"
    "v_mul_f32 v242, v242, v241\n"
    "v_accvgpr_write_b32 a146, v242\n"
    "v_accvgpr_read_b32 v243, a147\n"
    "v_mul_f32 v243, v243, v241\n"
    "v_accvgpr_write_b32 a147, v243\n"
    "v_accvgpr_read_b32 v242, a152\n"
    "v_mul_f32 v242, v242, v241\n"
    "v_accvgpr_write_b32 a152, v242\n"
    "v_accvgpr_read_b32 v243, a153\n"
    "v_mul_f32 v243, v243, v241\n"
    "v_accvgpr_write_b32 a153, v243\n"
    "v_accvgpr_read_b32 v242, a154\n"
    "v_mul_f32 v242, v242, v241\n"
    "v_accvgpr_write_b32 a154, v242\n"
    "v_accvgpr_read_b32 v243, a155\n"
    "v_mul_f32 v243, v243, v241\n"
    "v_accvgpr_write_b32 a155, v243\n"
    "v_accvgpr_read_b32 v242, a160\n"
    "v_mul_f32 v242, v242, v241\n"
    "v_accvgpr_write_b32 a160, v242\n"
    "v_accvgpr_read_b32 v243, a161\n"
    "v_mul_f32 v243, v243, v241\n"
    "v_accvgpr_write_b32 a161, v243\n"
    "v_accvgpr_read_b32 v242, a162\n"
    "v_mul_f32 v242, v242, v241\n"
    "v_accvgpr_write_b32 a162, v242\n"
    "v_accvgpr_read_b32 v243, a163\n"
    "v_mul_f32 v243, v243, v241\n"
    "v_accvgpr_write_b32 a163, v243\n"
    "v_accvgpr_read_b32 v242, a168\n"
    "v_mul_f32 v242, v242, v241\n"
    "v_accvgpr_write_b32 a168, v242\n"
    "v_accvgpr_read_b32 v243, a169\n"
    "v_mul_f32 v243, v243, v241\n"
    "v_accvgpr_write_b32 a169, v243\n"
    "v_accvgpr_read_b32 v242, a170\n"
    "v_mul_f32 v242, v242, v241\n"
    "v_accvgpr_write_b32 a170, v242\n"
    "v_accvgpr_read_b32 v243, a171\n"
    "v_mul_f32 v243, v243, v241\n"
    "v_accvgpr_write_b32 a171, v243\n"
    "v_accvgpr_read_b32 v242, a176\n"
    "v_mul_f32 v242, v242, v241\n"
    "v_accvgpr_write_b32 a176, v242\n"
    "v_accvgpr_read_b32 v243, a177\n"
    "v_mul_f32 v243, v243, v241\n"
    "v_accvgpr_write_b32 a177, v243\n"
    "v_accvgpr_read_b32 v242, a178\n"
    "v_mul_f32 v242, v242, v241\n"
    "v_accvgpr_write_b32 a178, v242\n"
    "v_accvgpr_read_b32 v243, a179\n"
    "v_mul_f32 v243, v243, v241\n"
    "v_accvgpr_write_b32 a179, v243\n"
    "v_accvgpr_read_b32 v242, a184\n"
    "v_mul_f32 v242, v242, v241\n"
    "v_accvgpr_write_b32 a184, v242\n"
    "v_accvgpr_read_b32 v243, a185\n"
    "v_mul_f32 v243, v243, v241\n"
    "v_accvgpr_write_b32 a185, v243\n"
    "v_accvgpr_read_b32 v242, a186\n"
    "v_mul_f32 v242, v242, v241\n"
    "v_accvgpr_write_b32 a186, v242\n"
    "v_accvgpr_read_b32 v243, a187\n"
    "v_mul_f32 v243, v243, v241\n"
    "v_accvgpr_write_b32 a187, v243\n"
    "v_accvgpr_read_b32 v242, a192\n"
    "v_mul_f32 v242, v242, v241\n"
    "v_accvgpr_write_b32 a192, v242\n"
    "v_accvgpr_read_b32 v243, a193\n"
    "v_mul_f32 v243, v243, v241\n"
    "v_accvgpr_write_b32 a193, v243\n"
    "v_accvgpr_read_b32 v242, a194\n"
    "v_mul_f32 v242, v242, v241\n"
    "v_accvgpr_write_b32 a194, v242\n"
    "v_accvgpr_read_b32 v243, a195\n"
    "v_mul_f32 v243, v243, v241\n"
    "v_accvgpr_write_b32 a195, v243\n"
    "v_accvgpr_read_b32 v242, a200\n"
    "v_mul_f32 v242, v242, v241\n"
    "v_accvgpr_write_b32 a200, v242\n"
    "v_accvgpr_read_b32 v243, a201\n"
    "v_mul_f32 v243, v243, v241\n"
    "v_accvgpr_write_b32 a201, v243\n"
    "v_accvgpr_read_b32 v242, a202\n"
    "v_mul_f32 v242, v242, v241\n"
    "v_accvgpr_write_b32 a202, v242\n"
    "v_accvgpr_read_b32 v243, a203\n"
    "v_mul_f32 v243, v243, v241\n"
    "v_accvgpr_write_b32 a203, v243\n"
    "v_accvgpr_read_b32 v242, a208\n"
    "v_mul_f32 v242, v242, v241\n"
    "v_accvgpr_write_b32 a208, v242\n"
    "v_accvgpr_read_b32 v243, a209\n"
    "v_mul_f32 v243, v243, v241\n"
    "v_accvgpr_write_b32 a209, v243\n"
    "v_accvgpr_read_b32 v242, a210\n"
    "v_mul_f32 v242, v242, v241\n"
    "v_accvgpr_write_b32 a210, v242\n"
    "v_accvgpr_read_b32 v243, a211\n"
    "v_mul_f32 v243, v243, v241\n"
    "v_accvgpr_write_b32 a211, v243\n"
    "v_accvgpr_read_b32 v242, a216\n"
    "v_mul_f32 v242, v242, v241\n"
    "v_accvgpr_write_b32 a216, v242\n"
    "v_accvgpr_read_b32 v243, a217\n"
    "v_mul_f32 v243, v243, v241\n"
    "v_accvgpr_write_b32 a217, v243\n"
    "v_accvgpr_read_b32 v242, a218\n"
    "v_mul_f32 v242, v242, v241\n"
    "v_accvgpr_write_b32 a218, v242\n"
    "v_accvgpr_read_b32 v243, a219\n"
    "v_mul_f32 v243, v243, v241\n"
    "v_accvgpr_write_b32 a219, v243\n"
    "v_accvgpr_read_b32 v242, a224\n"
    "v_mul_f32 v242, v242, v241\n"
    "v_accvgpr_write_b32 a224, v242\n"
    "v_accvgpr_read_b32 v243, a225\n"
    "v_mul_f32 v243, v243, v241\n"
    "v_accvgpr_write_b32 a225, v243\n"
    "v_accvgpr_read_b32 v242, a226\n"
    "v_mul_f32 v242, v242, v241\n"
    "v_accvgpr_write_b32 a226, v242\n"
    "v_accvgpr_read_b32 v243, a227\n"
    "v_mul_f32 v243, v243, v241\n"
    "v_accvgpr_write_b32 a227, v243\n"
    "v_accvgpr_read_b32 v242, a232\n"
    "v_mul_f32 v242, v242, v241\n"
    "v_accvgpr_write_b32 a232, v242\n"
    "v_accvgpr_read_b32 v243, a233\n"
    "v_mul_f32 v243, v243, v241\n"
    "v_accvgpr_write_b32 a233, v243\n"
    "v_accvgpr_read_b32 v242, a234\n"
    "v_mul_f32 v242, v242, v241\n"
    "v_accvgpr_write_b32 a234, v242\n"
    "v_accvgpr_read_b32 v243, a235\n"
    "v_mul_f32 v243, v243, v241\n"
    "v_accvgpr_write_b32 a235, v243\n"
    "v_accvgpr_read_b32 v242, a240\n"
    "v_mul_f32 v242, v242, v241\n"
    "v_accvgpr_write_b32 a240, v242\n"
    "v_accvgpr_read_b32 v243, a241\n"
    "v_mul_f32 v243, v243, v241\n"
    "v_accvgpr_write_b32 a241, v243\n"
    "v_accvgpr_read_b32 v242, a242\n"
    "v_mul_f32 v242, v242, v241\n"
    "v_accvgpr_write_b32 a242, v242\n"
    "v_accvgpr_read_b32 v243, a243\n"
    "v_mul_f32 v243, v243, v241\n"
    "v_accvgpr_write_b32 a243, v243\n"
    "v_accvgpr_read_b32 v242, a248\n"
    "v_mul_f32 v242, v242, v241\n"
    "v_accvgpr_write_b32 a248, v242\n"
    "v_accvgpr_read_b32 v243, a249\n"
    "v_mul_f32 v243, v243, v241\n"
    "v_accvgpr_write_b32 a249, v243\n"
    "v_accvgpr_read_b32 v242, a250\n"
    "v_mul_f32 v242, v242, v241\n"
    "v_accvgpr_write_b32 a250, v242\n"
    "v_accvgpr_read_b32 v243, a251\n"
    "v_mul_f32 v243, v243, v241\n"
    "v_accvgpr_write_b32 a251, v243\n"
    "v_max3_f32 v240, v140, v141, v142\n"
    "v_max3_f32 v240, v240, v143, v156\n"
    "v_max3_f32 v240, v240, v157, v158\n"
    "v_max_f32 v240, v240, v159\n"
    "v_mov_b32 v241, v240\n"
    "s_nop 1\n"
    "v_permlane16_swap_b32 v240, v241\n"
    "s_nop 1\n"
    "v_max_f32 v240, v240, v241\n"
    "v_mov_b32 v241, v240\n"
    "s_nop 1\n"
    "v_permlane32_swap_b32 v240, v241\n"
    "s_nop 1\n"
    "v_max_f32 v240, v240, v241\n"
    "v_add_f32 v240, 102.0, v240\n"
    "v_max_f32 v240, 0, v240\n"
    "v_sub_f32 v204, v204, v240\n"
    "v_sub_f32 v205, v205, v240\n"
    "v_sub_f32 v206, v206, v240\n"
    "v_sub_f32 v207, v207, v240\n"
    "v_sub_f32 v140, v140, v240\n"
    "v_sub_f32 v141, v141, v240\n"
    "v_sub_f32 v142, v142, v240\n"
    "v_sub_f32 v143, v143, v240\n"
    "v_sub_f32 v156, v156, v240\n"
    "v_sub_f32 v157, v157, v240\n"
    "v_sub_f32 v158, v158, v240\n"
    "v_sub_f32 v159, v159, v240\n"
    "v_exp_f32 v241, -v240\n"
    "s_nop 0\n"
    "v_mul_f32 v251, v251, v241\n"
    "v_accvgpr_read_b32 v242, a132\n"
    "v_mul_f32 v242, v242, v241\n"
    "v_accvgpr_write_b32 a132, v242\n"
    "v_accvgpr_read_b32 v243, a133\n"
    "v_mul_f32 v243, v243, v241\n"
    "v_accvgpr_write_b32 a133, v243\n"
    "v_accvgpr_read_b32 v242, a134\n"
    "v_mul_f32 v242, v242, v241\n"
    "v_accvgpr_write_b32 a134, v242\n"
    "v_accvgpr_read_b32 v243, a135\n"
    "v_mul_f32 v243, v243, v241\n"
    "v_accvgpr_write_b32 a135, v243\n"
    "v_accvgpr_read_b32 v242, a140\n"
    "v_mul_f32 v242, v242, v241\n"
    "v_accvgpr_write_b32 a140, v242\n"
    "v_accvgpr_read_b32 v243, a141\n"
    "v_mul_f32 v243, v243, v241\n"
    "v_accvgpr_write_b32 a141, v243\n"
    "v_accvgpr_read_b32 v242, a142\n"
    "v_mul_f32 v242, v242, v241\n"
    "v_accvgpr_write_b32 a142, v242\n"
    "v_accvgpr_read_b32 v243, a143\n"
    "v_mul_f32 v243, v243, v241\n"
    "v_accvgpr_write_b32 a143, v243\n"
    "v_accvgpr_read_b32 v242, a148\n"
    "v_mul_f32 v242, v242, v241\n"
    "v_accvgpr_write_b32 a148, v242\n"
    "v_accvgpr_read_b32 v243, a149\n"
    "v_mul_f32 v243, v243, v241\n"
    "v_accvgpr_write_b32 a149, v243\n"
    "v_accvgpr_read_b32 v242, a150\n"
    "v_mul_f32 v242, v242, v241\n"
    "v_accvgpr_write_b32 a150, v242\n"
    "v_accvgpr_read_b32 v243, a151\n"
    "v_mul_f32 v243, v243, v241\n"
    "v_accvgpr_write_b32 a151, v243\n"
    "v_accvgpr_read_b32 v242, a156\n"
    "v_mul_f32 v242, v242, v241\n"
    "v_accvgpr_write_b32 a156, v242\n"
    "v_accvgpr_read_b32 v243, a157\n"
    "v_mul_f32 v243, v243, v241\n"
    "v_accvgpr_write_b32 a157, v243\n"
    "v_accvgpr_read_b32 v242, a158\n"
    "v_mul_f32 v242, v242, v241\n"
    "v_accvgpr_write_b32 a158, v242\n"
    "v_accvgpr_read_b32 v243, a159\n"
    "v_mul_f32 v243, v243, v241\n"
    "v_accvgpr_write_b32 a159, v243\n"
    "v_accvgpr_read_b32 v242, a164\n"
    "v_mul_f32 v242, v242, v241\n"
    "v_accvgpr_write_b32 a164, v242\n"
    "v_accvgpr_read_b32 v243, a165\n"
    "v_mul_f32 v243, v243, v241\n"
    "v_accvgpr_write_b32 a165, v243\n"
    "v_accvgpr_read_b32 v242, a166\n"
    "v_mul_f32 v242, v242, v241\n"
    "v_accvgpr_write_b32 a166, v242\n"
    "v_accvgpr_read_b32 v243, a167\n"
    "v_mul_f32 v243, v243, v241\n"
    "v_accvgpr_write_b32 a167, v243\n"
    "v_accvgpr_read_b32 v242, a172\n"
    "v_mul_f32 v242, v242, v241\n"
    "v_accvgpr_write_b32 a172, v242\n"
    "v_accvgpr_read_b32 v243, a173\n"
    "v_mul_f32 v243, v243, v241\n"
    "v_accvgpr_write_b32 a173, v243\n"
    "v_accvgpr_read_b32 v242, a174\n"
    "v_mul_f32 v242, v242, v241\n"
    "v_accvgpr_write_b32 a174, v242\n"
    "v_accvgpr_read_b32 v243, a175\n"
    "v_mul_f32 v243, v243, v241\n"
    "v_accvgpr_write_b32 a175, v243\n"
    "v_accvgpr_read_b32 v242, a180\n"
    "v_mul_f32 v242, v242, v241\n"
    "v_accvgpr_write_b32 a180, v242\n"
    "v_accvgpr_read_b32 v243, a181\n"
    "v_mul_f32 v243, v243, v241\n"
    "v_accvgpr_write_b32 a181, v243\n"
    "v_accvgpr_read_b32 v242, a182\n"
    "v_mul_f32 v242, v242, v241\n"
    "v_accvgpr_write_b32 a182, v242\n"
    "v_accvgpr_read_b32 v243, a183\n"
    "v_mul_f32 v243, v243, v241\n"
    "v_accvgpr_write_b32 a183, v243\n"
    "v_accvgpr_read_b32 v242, a188\n"
    "v_mul_f32 v242, v242, v241\n"
    "v_accvgpr_write_b32 a188, v242\n"
    "v_accvgpr_read_b32 v243, a189\n"
    "v_mul_f32 v243, v243, v241\n"
    "v_accvgpr_write_b32 a189, v243\n"
    "v_accvgpr_read_b32 v242, a190\n"
    "v_mul_f32 v242, v242, v241\n"
    "v_accvgpr_write_b32 a190, v242\n"
    "v_accvgpr_read_b32 v243, a191\n"
    "v_mul_f32 v243, v243, v241\n"
    "v_accvgpr_write_b32 a191, v243\n"
    "v_accvgpr_read_b32 v242, a196\n"
    "v_mul_f32 v242, v242, v241\n"
    "v_accvgpr_write_b32 a196, v242\n"
    "v_accvgpr_read_b32 v243, a197\n"
    "v_mul_f32 v243, v243, v241\n"
    "v_accvgpr_write_b32 a197, v243\n"
    "v_accvgpr_read_b32 v242, a198\n"
    "v_mul_f32 v242, v242, v241\n"
    "v_accvgpr_write_b32 a198, v242\n"
    "v_accvgpr_read_b32 v243, a199\n"
    "v_mul_f32 v243, v243, v241\n"
    "v_accvgpr_write_b32 a199, v243\n"
    "v_accvgpr_read_b32 v242, a204\n"
    "v_mul_f32 v242, v242, v241\n"
    "v_accvgpr_write_b32 a204, v242\n"
    "v_accvgpr_read_b32 v243, a205\n"
    "v_mul_f32 v243, v243, v241\n"
    "v_accvgpr_write_b32 a205, v243\n"
    "v_accvgpr_read_b32 v242, a206\n"
    "v_mul_f32 v242, v242, v241\n"
    "v_accvgpr_write_b32 a206, v242\n"
    "v_accvgpr_read_b32 v243, a207\n"
    "v_mul_f32 v243, v243, v241\n"
    "v_accvgpr_write_b32 a207, v243\n"
    "v_accvgpr_read_b32 v242, a212\n"
    "v_mul_f32 v242, v242, v241\n"
    "v_accvgpr_write_b32 a212, v242\n"
    "v_accvgpr_read_b32 v243, a213\n"
    "v_mul_f32 v243, v243, v241\n"
    "v_accvgpr_write_b32 a213, v243\n"
    "v_accvgpr_read_b32 v242, a214\n"
    "v_mul_f32 v242, v242, v241\n"
    "v_accvgpr_write_b32 a214, v242\n"
    "v_accvgpr_read_b32 v243, a215\n"
    "v_mul_f32 v243, v243, v241\n"
    "v_accvgpr_write_b32 a215, v243\n"
    "v_accvgpr_read_b32 v242, a220\n"
    "v_mul_f32 v242, v242, v241\n"
    "v_accvgpr_write_b32 a220, v242\n"
    "v_accvgpr_read_b32 v243, a221\n"
    "v_mul_f32 v243, v243, v241\n"
    "v_accvgpr_write_b32 a221, v243\n"
    "v_accvgpr_read_b32 v242, a222\n"
    "v_mul_f32 v242, v242, v241\n"
    "v_accvgpr_write_b32 a222, v242\n"
    "v_accvgpr_read_b32 v243, a223\n"
    "v_mul_f32 v243, v243, v241\n"
    "v_accvgpr_write_b32 a223, v243\n"
    "v_accvgpr_read_b32 v242, a228\n"
    "v_mul_f32 v242, v242, v241\n"
    "v_accvgpr_write_b32 a228, v242\n"
    "v_accvgpr_read_b32 v243, a229\n"
    "v_mul_f32 v243, v243, v241\n"
    "v_accvgpr_write_b32 a229, v243\n"
    "v_accvgpr_read_b32 v242, a230\n"
    "v_mul_f32 v242, v242, v241\n"
    "v_accvgpr_write_b32 a230, v242\n"
    "v_accvgpr_read_b32 v243, a231\n"
    "v_mul_f32 v243, v243, v241\n"
    "v_accvgpr_write_b32 a231, v243\n"
    "v_accvgpr_read_b32 v242, a236\n"
    "v_mul_f32 v242, v242, v241\n"
    "v_accvgpr_write_b32 a236, v242\n"
    "v_accvgpr_read_b32 v243, a237\n"
    "v_mul_f32 v243, v243, v241\n"
    "v_accvgpr_write_b32 a237, v243\n"
    "v_accvgpr_read_b32 v242, a238\n"
    "v_mul_f32 v242, v242, v241\n"
    "v_accvgpr_write_b32 a238, v242\n"
    "v_accvgpr_read_b32 v243, a239\n"
    "v_mul_f32 v243, v243, v241\n"
    "v_accvgpr_write_b32 a239, v243\n"
    "v_accvgpr_read_b32 v242, a244\n"
    "v_mul_f32 v242, v242, v241\n"
    "v_accvgpr_write_b32 a244, v242\n"
    "v_accvgpr_read_b32 v243, a245\n"
    "v_mul_f32 v243, v243, v241\n"
    "v_accvgpr_write_b32 a245, v243\n"
    "v_accvgpr_read_b32 v242, a246\n"
    "v_mul_f32 v242, v242, v241\n"
    "v_accvgpr_write_b32 a246, v242\n"
    "v_accvgpr_read_b32 v243, a247\n"
    "v_mul_f32 v243, v243, v241\n"
    "v_accvgpr_write_b32 a247, v243\n"
    "v_accvgpr_read_b32 v242, a252\n"
    "v_mul_f32 v242, v242, v241\n"
    "v_accvgpr_write_b32 a252, v242\n"
    "v_accvgpr_read_b32 v243, a253\n"
    "v_mul_f32 v243, v243, v241\n"
    "v_accvgpr_write_b32 a253, v243\n"
    "v_accvgpr_read_b32 v242, a254\n"
    "v_mul_f32 v242, v242, v241\n"
    "v_accvgpr_write_b32 a254, v242\n"
    "v_accvgpr_read_b32 v243, a255\n"
    "v_mul_f32 v243, v243, v241\n"
    "v_accvgpr_write_b32 a255, v243\n"
    "v_exp_f32 v240, v140\n"
    "v_exp_f32 v241, v141\n"
    "v_exp_f32 v242, v142\n"
    "v_exp_f32 v243, v143\n"
    "v_add_f32 v247, v240, v241\n"
    "v_cvt_pk_bf16_f32 v172, v240, v241\n"
    "v_exp_f32 v240, v136\n"
    "v_exp_f32 v241, v137\n"
    "v_add_f32 v247, v247, v242\n"
    "v_add_f32 v247, v247, v243\n"
    "v_cvt_pk_bf16_f32 v173, v242, v243\n"
    "v_exp_f32 v242, v138\n"
    "v_exp_f32 v243, v139\n"
    "v_add_f32 v246, v240, v241\n"
    "v_cvt_pk_bf16_f32 v168, v240, v241\n"
    "v_exp_f32 v240, v132\n"
    "v_exp_f32 v241, v133\n"
    "v_add_f32 v246, v246, v242\n"
    "v_add_f32 v246, v246, v243\n"
    "v_cvt_pk_bf16_f32 v169, v242, v243\n"
    "v_exp_f32 v242, v134\n"
    "v_exp_f32 v243, v135\n"
    "v_add_f32 v245, v240, v241\n"
    "v_cvt_pk_bf16_f32 v164, v240, v241\n"
    "v_exp_f32 v240, v128\n"
    "v_exp_f32 v241, v129\n"
    "v_add_f32 v245, v245, v242\n"
    "v_add_f32 v245, v245, v243\n"
    "v_cvt_pk_bf16_f32 v165, v242, v243\n"
    "v_exp_f32 v242, v130\n"
    "v_exp_f32 v243, v131\n"
    "v_add_f32 v244, v240, v241\n"
    "v_cvt_pk_bf16_f32 v160, v240, v241\n"
    "v_exp_f32 v240, v156\n"
    "v_exp_f32 v241, v157\n"
    "v_add_f32 v244, v244, v242\n"
    "v_add_f32 v244, v244, v243\n"
    "v_cvt_pk_bf16_f32 v161, v242, v243\n"
    "v_exp_f32 v242, v158\n"
    "v_exp_f32 v243, v159\n"
    "v_add_f32 v247, v247, v240\n"
    "v_add_f32 v247, v247, v241\n"
    "v_cvt_pk_bf16_f32 v174, v240, v241\n"
    "v_exp_f32 v240, v152\n"
    "v_exp_f32 v241, v153\n"
    "v_add_f32 v247, v247, v242\n"
    "v_add_f32 v247, v247, v243\n"
    "v_cvt_pk_bf16_f32 v175, v242, v243\n"
    "v_exp_f32 v242, v154\n"
    "v_exp_f32 v243, v155\n"
    "v_add_f32 v246, v246, v240\n"
    "v_add_f32 v246, v246, v241\n"
    "v_cvt_pk_bf16_f32 v170, v240, v241\n"
    "v_exp_f32 v240, v148\n"
    "v_exp_f32 v241, v149\n"
    "v_add_f32 v246, v246, v242\n"
    "v_add_f32 v246, v246, v243\n"
    "v_cvt_pk_bf16_f32 v171, v242, v243\n"
    "v_exp_f32 v242, v150\n"
    "v_exp_f32 v243, v151\n"
    "v_add_f32 v245, v245, v240\n"
    "v_add_f32 v245, v245, v241\n"
    "v_cvt_pk_bf16_f32 v166, v240, v241\n"
    "v_exp_f32 v240, v144\n"
    "v_exp_f32 v241, v145\n"
    "v_add_f32 v245, v245, v242\n"
    "v_add_f32 v245, v245, v243\n"
    "v_cvt_pk_bf16_f32 v167, v242, v243\n"
    "v_exp_f32 v242, v146\n"
    "v_exp_f32 v243, v147\n"
    "v_add_f32 v244, v244, v240\n"
    "v_add_f32 v244, v244, v241\n"
    "v_cvt_pk_bf16_f32 v162, v240, v241\n"
    "v_add_f32 v244, v244, v242\n"
    "v_add_f32 v244, v244, v243\n"
    "v_cvt_pk_bf16_f32 v163, v242, v243\n"
    "s_nop 1\n"
    "s_branch RET0_%=\n"
    "RARE1_%=:\n"
    "s_nop 7\n"
    "s_nop 7\n"
    "v_max3_f32 v240, v128, v129, v130\n"
    "v_max3_f32 v240, v240, v131, v144\n"
    "v_max3_f32 v240, v240, v145, v146\n"
    "v_max_f32 v240, v240, v147\n"
    "v_mov_b32 v241, v240\n"
    "s_nop 1\n"
    "v_permlane16_swap_b32 v240, v241\n"
    "s_nop 1\n"
    "v_max_f32 v240, v240, v241\n"
    "v_mov_b32 v241, v240\n"
    "s_nop 1\n"
    "v_permlane32_swap_b32 v240, v241\n"
    "s_nop 1\n"
    "v_max_f32 v240, v240, v241\n"
    "v_add_f32 v240, 102.0, v240\n"
    "v_max_f32 v240, 0, v240\n"
    "v_sub_f32 v192, v192, v240\n"
    "v_sub_f32 v193, v193, v240\n"
    "v_sub_f32 v194, v194, v240\n"
    "v_sub_f32 v195, v195, v240\n"
    "v_sub_f32 v128, v128, v240\n"
    "v_sub_f32 v129, v129, v240\n"
    "v_sub_f32 v130, v130, v240\n"
    "v_sub_f32 v131, v131, v240\n"
    "v_sub_f32 v144, v144, v240\n"
    "v_sub_f32 v145, v145, v240\n"
    "v_sub_f32 v146, v146, v240\n"
    "v_sub_f32 v147, v147, v240\n"
    "v_exp_f32 v241, -v240\n"
    "s_nop 0\n"
    "v_mul_f32 v248, v248, v241\n"
    "v_mul_f32 v0, v0, v241\n"
    "v_mul_f32 v1, v1, v241\n"
    "v_mul_f32 v2, v2, v241\n"
    "v_mul_f32 v3, v3, v241\n"
    "v_mul_f32 v8, v8, v241\n"
    "v_mul_f32 v9, v9, v241\n"
    "v_mul_f32 v10, v10, v241\n"
    "v_mul_f32 v11, v11, v241\n"
    "v_mul_f32 v16, v16, v241\n"
    "v_mul_f32 v17, v17, v241\n"
    "v_mul_f32 v18, v18, v241\n"
    "v_mul_f32 v19, v19, v241\n"
    "v_mul_f32 v24, v24, v241\n"
    "v_mul_f32 v25, v25, v241\n"
    "v_mul_f32 v26, v26, v241\n"
    "v_mul_f32 v27, v27, v241\n"
    "v_mul_f32 v32, v32, v241\n"
    "v_mul_f32 v33, v33, v241\n"
    "v_mul_f32 v34, v34, v241\n"
    "v_mul_f32 v35, v35, v241\n"
    "v_mul_f32 v40, v40, v241\n"
    "v_mul_f32 v41, v41, v241\n"
    "v_mul_f32 v42, v42, v241\n"
    "v_mul_f32 v43, v43, v241\n"
    "v_mul_f32 v48, v48, v241\n"
    "v_mul_f32 v49, v49, v241\n"
    "v_mul_f32 v50, v50, v241\n"
    "v_mul_f32 v51, v51, v241\n"
    "v_mul_f32 v56, v56, v241\n"
    "v_mul_f32 v57, v57, v241\n"
    "v_mul_f32 v58, v58, v241\n"
    "v_mul_f32 v59, v59, v241\n"
    "v_mul_f32 v64, v64, v241\n"
    "v_mul_f32 v65, v65, v241\n"
    "v_mul_f32 v66, v66, v241\n"
    "v_mul_f32 v67, v67, v241\n"
    "v_mul_f32 v72, v72, v241\n"
    "v_mul_f32 v73, v73, v241\n"
    "v_mul_f32 v74, v74, v241\n"
    "v_mul_f32 v75, v75, v241\n"
    "v_mul_f32 v80, v80, v241\n"
    "v_mul_f32 v81, v81, v241\n"
    "v_mul_f32 v82, v82, v241\n"
    "v_mul_f32 v83, v83, v241\n"
    "v_mul_f32 v88, v88, v241\n"
    "v_mul_f32 v89, v89, v241\n"
    "v_mul_f32 v90, v90, v241\n"
    "v_mul_f32 v91, v91, v241\n"
    "v_mul_f32 v96, v96, v241\n"
    "v_mul_f32 v97, v97, v241\n"
    "v_mul_f32 v98, v98, v241\n"
    "v_mul_f32 v99, v99, v241\n"
    "v_mul_f32 v104, v104, v241\n"
    "v_mul_f32 v105, v105, v241\n"
    "v_mul_f32 v106, v106, v241\n"
    "v_mul_f32 v107, v107, v241\n"
    "v_mul_f32 v112, v112, v241\n"
    "v_mul_f32 v113, v113, v241\n"
    "v_mul_f32 v114, v114, v241\n"
    "v_mul_f32 v115, v115, v241\n"
    "v_mul_f32 v120, v120, v241\n"
    "v_mul_f32 v121, v121, v241\n"
    "v_mul_f32 v122, v122, v241\n"
    "v_mul_f32 v123, v123, v241\n"
    "v_max3_f32 v240, v132, v133, v134\n"
    "v_max3_f32 v240, v240, v135, v148\n"
    "v_max3_f32 v240, v240, v149, v150\n"
    "v_max_f32 v240, v240, v151\n"
    "v_mov_b32 v241, v240\n"
    "s_nop 1\n"
    "v_permlane16_swap_b32 v240, v241\n"
    "s_nop 1\n"
    "v_max_f32 v240, v240, v241\n"
    "v_mov_b32 v241, v240\n"
    "s_nop 1\n"
    "v_permlane32_swap_b32 v240, v241\n"
    "s_nop 1\n"
    "v_max_f32 v240, v240, v241\n"
    "v_add_f32 v240, 102.0, v240\n"
    "v_max_f32 v240, 0, v240\n"
    "v_sub_f32 v196, v196, v240\n"
    "v_sub_f32 v197, v197, v240\n"
    "v_sub_f32 v198, v198, v240\n"
    "v_sub_f32 v199, v199, v240\n"
    "v_sub_f32 v132, v132, v240\n"
    "v_sub_f32 v133, v133, v240\n"
    "v_sub_f32 v134, v134, v240\n"
    "v_sub_f32 v135, v135, v240\n"
    "v_sub_f32 v148, v148, v240\n"
    "v_sub_f32 v149, v149, v240\n"
    "v_sub_f32 v150, v150, v240\n"
    "v_sub_f32 v151, v151, v240\n"
    "v_exp_f32 v241, -v240\n"
    "s_nop 0\n"
    "v_mul_f32 v249, v249, v241\n"
    "v_mul_f32 v4, v4, v241\n"
    "v_mul_f32 v5, v5, v241\n"
    "v_mul_f32 v6, v6, v241\n"
    "v_mul_f32 v7, v7, v241\n"
    "v_mul_f32 v12, v12, v241\n"
    "v_mul_f32 v13, v13, v241\n"
    "v_mul_f32 v14, v14, v241\n"
    "v_mul_f32 v15, v15, v241\n"
    "v_mul_f32 v20, v20, v241\n"
    "v_mul_f32 v21, v21, v241\n"
    "v_mul_f32 v22, v22, v241\n"
    "v_mul_f32 v23, v23, v241\n"
    "v_mul_f32 v28, v28, v241\n"
    "v_mul_f32 v29, v29, v241\n"
    "v_mul_f32 v30, v30, v241\n"
    "v_mul_f32 v31, v31, v241\n"
    "v_mul_f32 v36, v36, v241\n"
    "v_mul_f32 v37, v37, v241\n"
    "v_mul_f32 v38, v38, v241\n"
    "v_mul_f32 v39, v39, v241\n"
    "v_mul_f32 v44, v44, v241\n"
    "v_mul_f32 v45, v45, v241\n"
    "v_mul_f32 v46, v46, v241\n"
    "v_mul_f32 v47, v47, v241\n"
    "v_mul_f32 v52, v52, v241\n"
    "v_mul_f32 v53, v53, v241\n"
    "v_mul_f32 v54, v54, v241\n"
    "v_mul_f32 v55, v55, v241\n"
    "v_mul_f32 v60, v60, v241\n"
    "v_mul_f32 v61, v61, v241\n"
    "v_mul_f32 v62, v62, v241\n"
    "v_mul_f32 v63, v63, v241\n"
    "v_mul_f32 v68, v68, v241\n"
    "v_mul_f32 v69, v69, v241\n"
    "v_mul_f32 v70, v70, v241\n"
    "v_mul_f32 v71, v71, v241\n"
    "v_mul_f32 v76, v76, v241\n"
    "v_mul_f32 v77, v77, v241\n"
    "v_mul_f32 v78, v78, v241\n"
    "v_mul_f32 v79, v79, v241\n"
    "v_mul_f32 v84, v84, v241\n"
    "v_mul_f32 v85, v85, v241\n"
    "v_mul_f32 v86, v86, v241\n"
    "v_mul_f32 v87, v87, v241\n"
    "v_mul_f32 v92, v92, v241\n"
    "v_mul_f32 v93, v93, v241\n"
    "v_mul_f32 v94, v94, v241\n"
    "v_mul_f32 v95, v95, v241\n"
    "v_mul_f32 v100, v100, v241\n"
    "v_mul_f32 v101, v101, v241\n"
    "v_mul_f32 v102, v102, v241\n"
    "v_mul_f32 v103, v103, v241\n"
    "v_mul_f32 v108, v108, v241\n"
    "v_mul_f32 v109, v109, v241\n"
    "v_mul_f32 v110, v110, v241\n"
    "v_mul_f32 v111, v111, v241\n"
    "v_mul_f32 v116, v116, v241\n"
    "v_mul_f32 v117, v117, v241\n"
    "v_mul_f32 v118, v118, v241\n"
    "v_mul_f32 v119, v119, v241\n"
    "v_mul_f32 v124, v124, v241\n"
    "v_mul_f32 v125, v125, v241\n"
    "v_mul_f32 v126, v126, v241\n"
    "v_mul_f32 v127, v127, v241\n"
    "v_max3_f32 v240, v136, v137, v138\n"
    "v_max3_f32 v240, v240, v139, v152\n"
    "v_max3_f32 v240, v240, v153, v154\n"
    "v_max_f32 v240, v240, v155\n"
    "v_mov_b32 v241, v240\n"
    "s_nop 1\n"
    "v_permlane16_swap_b32 v240, v241\n"
    "s_nop 1\n"
    "v_max_f32 v240, v240, v241\n"
    "v_mov_b32 v241, v240\n"
    "s_nop 1\n"
    "v_permlane32_swap_b32 v240, v241\n"
    "s_nop 1\n"
    "v_max_f32 v240, v240, v241\n"
    "v_add_f32 v240, 102.0, v240\n"
    "v_max_f32 v240, 0, v240\n"
    "v_sub_f32 v200, v200, v240\n"
    "v_sub_f32 v201, v201, v240\n"
    "v_sub_f32 v202, v202, v240\n"
    "v_sub_f32 v203, v203, v240\n"
    "v_sub_f32 v136, v136, v240\n"
    "v_sub_f32 v137, v137, v240\n"
    "v_sub_f32 v138, v138, v240\n"
    "v_sub_f32 v139, v139, v240\n"
    "v_sub_f32 v152, v152, v240\n"
    "v_sub_f32 v153, v153, v240\n"
    "v_sub_f32 v154, v154, v240\n"
    "v_sub_f32 v155, v155, v240\n"
    "v_exp_f32 v241, -v240\n"
    "s_nop 0\n"
    "v_mul_f32 v250, v250, v241\n"
    "v_accvgpr_read_b32 v242, a128\n"
    "v_mul_f32 v242, v242, v241\n"
    "v_accvgpr_write_b32 a128, v242\n"
    "v_accvgpr_read_b32 v243, a129\n"
    "v_mul_f32 v243, v243, v241\n"
    "v_accvgpr_write_b32 a129, v243\n"
    "v_accvgpr_read_b32 v242, a130\n"
    "v_mul_f32 v242, v242, v241\n"
    "v_accvgpr_write_b32 a130, v242\n"
    "v_accvgpr_read_b32 v243, a131\n"
    "v_mul_f32 v243, v243, v241\n"
    "v_accvgpr_write_b32 a131, v243\n"
    "v_accvgpr_read_b32 v242, a136\n"
    "v_mul_f32 v242, v242, v241\n"
    "v_accvgpr_write_b32 a136, v242\n"
    "v_accvgpr_read_b32 v243, a137\n"
    "v_mul_f32 v243, v243, v241\n"
    "v_accvgpr_write_b32 a137, v243\n"
    "v_accvgpr_read_b32 v242, a138\n"
    "v_mul_f32 v242, v242, v241\n"
    "v_accvgpr_write_b32 a138, v242\n"
    "v_accvgpr_read_b32 v243, a139\n"
    "v_mul_f32 v243, v243, v241\n"
    "v_accvgpr_write_b32 a139, v243\n"
    "v_accvgpr_read_b32 v242, a144\n"
    "v_mul_f32 v242, v242, v241\n"
    "v_accvgpr_write_b32 a144, v242\n"
    "v_accvgpr_read_b32 v243, a145\n"
    "v_mul_f32 v243, v243, v241\n"
    "v_accvgpr_write_b32 a145, v243\n"
    "v_accvgpr_read_b32 v242, a146\n"
    "v_mul_f32 v242, v242, v241\n"
    "v_accvgpr_write_b32 a146, v242\n"
    "v_accvgpr_read_b32 v243, a147\n"
    "v_mul_f32 v243, v243, v241\n"
    "v_accvgpr_write_b32 a147, v243\n"
    "v_accvgpr_read_b32 v242, a152\n"
    "v_mul_f32 v242, v242, v241\n"
    "v_accvgpr_write_b32 a152, v242\n"
    "v_accvgpr_read_b32 v243, a153\n"
    "v_mul_f32 v243, v243, v241\n"
    "v_accvgpr_write_b32 a153, v243\n"
    "v_accvgpr_read_b32 v242, a154\n"
    "v_mul_f32 v242, v242, v241\n"
    "v_accvgpr_write_b32 a154, v242\n"
    "v_accvgpr_read_b32 v243, a155\n"
    "v_mul_f32 v243, v243, v241\n"
    "v_accvgpr_write_b32 a155, v243\n"
    "v_accvgpr_read_b32 v242, a160\n"
    "v_mul_f32 v242, v242, v241\n"
    "v_accvgpr_write_b32 a160, v242\n"
    "v_accvgpr_read_b32 v243, a161\n"
    "v_mul_f32 v243, v243, v241\n"
    "v_accvgpr_write_b32 a161, v243\n"
    "v_accvgpr_read_b32 v242, a162\n"
    "v_mul_f32 v242, v242, v241\n"
    "v_accvgpr_write_b32 a162, v242\n"
    "v_accvgpr_read_b32 v243, a163\n"
    "v_mul_f32 v243, v243, v241\n"
    "v_accvgpr_write_b32 a163, v243\n"
    "v_accvgpr_read_b32 v242, a168\n"
    "v_mul_f32 v242, v242, v241\n"
    "v_accvgpr_write_b32 a168, v242\n"
    "v_accvgpr_read_b32 v243, a169\n"
    "v_mul_f32 v243, v243, v241\n"
    "v_accvgpr_write_b32 a169, v243\n"
    "v_accvgpr_read_b32 v242, a170\n"
    "v_mul_f32 v242, v242, v241\n"
    "v_accvgpr_write_b32 a170, v242\n"
    "v_accvgpr_read_b32 v243, a171\n"
    "v_mul_f32 v243, v243, v241\n"
    "v_accvgpr_write_b32 a171, v243\n"
    "v_accvgpr_read_b32 v242, a176\n"
    "v_mul_f32 v242, v242, v241\n"
    "v_accvgpr_write_b32 a176, v242\n"
    "v_accvgpr_read_b32 v243, a177\n"
    "v_mul_f32 v243, v243, v241\n"
    "v_accvgpr_write_b32 a177, v243\n"
    "v_accvgpr_read_b32 v242, a178\n"
    "v_mul_f32 v242, v242, v241\n"
    "v_accvgpr_write_b32 a178, v242\n"
    "v_accvgpr_read_b32 v243, a179\n"
    "v_mul_f32 v243, v243, v241\n"
    "v_accvgpr_write_b32 a179, v243\n"
    "v_accvgpr_read_b32 v242, a184\n"
    "v_mul_f32 v242, v242, v241\n"
    "v_accvgpr_write_b32 a184, v242\n"
    "v_accvgpr_read_b32 v243, a185\n"
    "v_mul_f32 v243, v243, v241\n"
    "v_accvgpr_write_b32 a185, v243\n"
    "v_accvgpr_read_b32 v242, a186\n"
    "v_mul_f32 v242, v242, v241\n"
    "v_accvgpr_write_b32 a186, v242\n"
    "v_accvgpr_read_b32 v243, a187\n"
    "v_mul_f32 v243, v243, v241\n"
    "v_accvgpr_write_b32 a187, v243\n"
    "v_accvgpr_read_b32 v242, a192\n"
    "v_mul_f32 v242, v242, v241\n"
    "v_accvgpr_write_b32 a192, v242\n"
    "v_accvgpr_read_b32 v243, a193\n"
    "v_mul_f32 v243, v243, v241\n"
    "v_accvgpr_write_b32 a193, v243\n"
    "v_accvgpr_read_b32 v242, a194\n"
    "v_mul_f32 v242, v242, v241\n"
    "v_accvgpr_write_b32 a194, v242\n"
    "v_accvgpr_read_b32 v243, a195\n"
    "v_mul_f32 v243, v243, v241\n"
    "v_accvgpr_write_b32 a195, v243\n"
    "v_accvgpr_read_b32 v242, a200\n"
    "v_mul_f32 v242, v242, v241\n"
    "v_accvgpr_write_b32 a200, v242\n"
    "v_accvgpr_read_b32 v243, a201\n"
    "v_mul_f32 v243, v243, v241\n"
    "v_accvgpr_write_b32 a201, v243\n"
    "v_accvgpr_read_b32 v242, a202\n"
    "v_mul_f32 v242, v242, v241\n"
    "v_accvgpr_write_b32 a202, v242\n"
    "v_accvgpr_read_b32 v243, a203\n"
    "v_mul_f32 v243, v243, v241\n"
    "v_accvgpr_write_b32 a203, v243\n"
    "v_accvgpr_read_b32 v242, a208\n"
    "v_mul_f32 v242, v242, v241\n"
    "v_accvgpr_write_b32 a208, v242\n"
    "v_accvgpr_read_b32 v243, a209\n"
    "v_mul_f32 v243, v243, v241\n"
    "v_accvgpr_write_b32 a209, v243\n"
    "v_accvgpr_read_b32 v242, a210\n"
    "v_mul_f32 v242, v242, v241\n"
    "v_accvgpr_write_b32 a210, v242\n"
    "v_accvgpr_read_b32 v243, a211\n"
    "v_mul_f32 v243, v243, v241\n"
    "v_accvgpr_write_b32 a211, v243\n"
    "v_accvgpr_read_b32 v242, a216\n"
    "v_mul_f32 v242, v242, v241\n"
    "v_accvgpr_write_b32 a216, v242\n"
    "v_accvgpr_read_b32 v243, a217\n"
    "v_mul_f32 v243, v243, v241\n"
    "v_accvgpr_write_b32 a217, v243\n"
    "v_accvgpr_read_b32 v242, a218\n"
    "v_mul_f32 v242, v242, v241\n"
    "v_accvgpr_write_b32 a218, v242\n"
    "v_accvgpr_read_b32 v243, a219\n"
    "v_mul_f32 v243, v243, v241\n"
    "v_accvgpr_write_b32 a219, v243\n"
    "v_accvgpr_read_b32 v242, a224\n"
    "v_mul_f32 v242, v242, v241\n"
    "v_accvgpr_write_b32 a224, v242\n"
    "v_accvgpr_read_b32 v243, a225\n"
    "v_mul_f32 v243, v243, v241\n"
    "v_accvgpr_write_b32 a225, v243\n"
    "v_accvgpr_read_b32 v242, a226\n"
    "v_mul_f32 v242, v242, v241\n"
    "v_accvgpr_write_b32 a226, v242\n"
    "v_accvgpr_read_b32 v243, a227\n"
    "v_mul_f32 v243, v243, v241\n"
    "v_accvgpr_write_b32 a227, v243\n"
    "v_accvgpr_read_b32 v242, a232\n"
    "v_mul_f32 v242, v242, v241\n"
    "v_accvgpr_write_b32 a232, v242\n"
    "v_accvgpr_read_b32 v243, a233\n"
    "v_mul_f32 v243, v243, v241\n"
    "v_accvgpr_write_b32 a233, v243\n"
    "v_accvgpr_read_b32 v242, a234\n"
    "v_mul_f32 v242, v242, v241\n"
    "v_accvgpr_write_b32 a234, v242\n"
    "v_accvgpr_read_b32 v243, a235\n"
    "v_mul_f32 v243, v243, v241\n"
    "v_accvgpr_write_b32 a235, v243\n"
    "v_accvgpr_read_b32 v242, a240\n"
    "v_mul_f32 v242, v242, v241\n"
    "v_accvgpr_write_b32 a240, v242\n"
    "v_accvgpr_read_b32 v243, a241\n"
    "v_mul_f32 v243, v243, v241\n"
    "v_accvgpr_write_b32 a241, v243\n"
    "v_accvgpr_read_b32 v242, a242\n"
    "v_mul_f32 v242, v242, v241\n"
    "v_accvgpr_write_b32 a242, v242\n"
    "v_accvgpr_read_b32 v243, a243\n"
    "v_mul_f32 v243, v243, v241\n"
    "v_accvgpr_write_b32 a243, v243\n"
    "v_accvgpr_read_b32 v242, a248\n"
    "v_mul_f32 v242, v242, v241\n"
    "v_accvgpr_write_b32 a248, v242\n"
    "v_accvgpr_read_b32 v243, a249\n"
    "v_mul_f32 v243, v243, v241\n"
    "v_accvgpr_write_b32 a249, v243\n"
    "v_accvgpr_read_b32 v242, a250\n"
    "v_mul_f32 v242, v242, v241\n"
    "v_accvgpr_write_b32 a250, v242\n"
    "v_accvgpr_read_b32 v243, a251\n"
    "v_mul_f32 v243, v243, v241\n"
    "v_accvgpr_write_b32 a251, v243\n"
    "v_max3_f32 v240, v140, v141, v142\n"
    "v_max3_f32 v240, v240, v143, v156\n"
    "v_max3_f32 v240, v240, v157, v158\n"
    "v_max_f32 v240, v240, v159\n"
    "v_mov_b32 v241, v240\n"
    "s_nop 1\n"
    "v_permlane16_swap_b32 v240, v241\n"
    "s_nop 1\n"
    "v_max_f32 v240, v240, v241\n"
    "v_mov_b32 v241, v240\n"
    "s_nop 1\n"
    "v_permlane32_swap_b32 v240, v241\n"
    "s_nop 1\n"
    "v_max_f32 v240, v240, v241\n"
    "v_add_f32 v240, 102.0, v240\n"
    "v_max_f32 v240, 0, v240\n"
    "v_sub_f32 v204, v204, v240\n"
    "v_sub_f32 v205, v205, v240\n"
    "v_sub_f32 v206, v206, v240\n"
    "v_sub_f32 v207, v207, v240\n"
    "v_sub_f32 v140, v140, v240\n"
    "v_sub_f32 v141, v141, v240\n"
    "v_sub_f32 v142, v142, v240\n"
    "v_sub_f32 v143, v143, v240\n"
    "v_sub_f32 v156, v156, v240\n"
    "v_sub_f32 v157, v157, v240\n"
    "v_sub_f32 v158, v158, v240\n"
    "v_sub_f32 v159, v159, v240\n"
    "v_exp_f32 v241, -v240\n"
    "s_nop 0\n"
    "v_mul_f32 v251, v251, v241\n"
    "v_accvgpr_read_b32 v242, a132\n"
    "v_mul_f32 v242, v242, v241\n"
    "v_accvgpr_write_b32 a132, v242\n"
    "v_accvgpr_read_b32 v243, a133\n"
    "v_mul_f32 v243, v243, v241\n"
    "v_accvgpr_write_b32 a133, v243\n"
    "v_accvgpr_read_b32 v242, a134\n"
    "v_mul_f32 v242, v242, v241\n"
    "v_accvgpr_write_b32 a134, v242\n"
    "v_accvgpr_read_b32 v243, a135\n"
    "v_mul_f32 v243, v243, v241\n"
    "v_accvgpr_write_b32 a135, v243\n"
    "v_accvgpr_read_b32 v242, a140\n"
    "v_mul_f32 v242, v242, v241\n"
    "v_accvgpr_write_b32 a140, v242\n"
    "v_accvgpr_read_b32 v243, a141\n"
    "v_mul_f32 v243, v243, v241\n"
    "v_accvgpr_write_b32 a141, v243\n"
    "v_accvgpr_read_b32 v242, a142\n"
    "v_mul_f32 v242, v242, v241\n"
    "v_accvgpr_write_b32 a142, v242\n"
    "v_accvgpr_read_b32 v243, a143\n"
    "v_mul_f32 v243, v243, v241\n"
    "v_accvgpr_write_b32 a143, v243\n"
    "v_accvgpr_read_b32 v242, a148\n"
    "v_mul_f32 v242, v242, v241\n"
    "v_accvgpr_write_b32 a148, v242\n"
    "v_accvgpr_read_b32 v243, a149\n"
    "v_mul_f32 v243, v243, v241\n"
    "v_accvgpr_write_b32 a149, v243\n"
    "v_accvgpr_read_b32 v242, a150\n"
    "v_mul_f32 v242, v242, v241\n"
    "v_accvgpr_write_b32 a150, v242\n"
    "v_accvgpr_read_b32 v243, a151\n"
    "v_mul_f32 v243, v243, v241\n"
    "v_accvgpr_write_b32 a151, v243\n"
    "v_accvgpr_read_b32 v242, a156\n"
    "v_mul_f32 v242, v242, v241\n"
    "v_accvgpr_write_b32 a156, v242\n"
    "v_accvgpr_read_b32 v243, a157\n"
    "v_mul_f32 v243, v243, v241\n"
    "v_accvgpr_write_b32 a157, v243\n"
    "v_accvgpr_read_b32 v242, a158\n"
    "v_mul_f32 v242, v242, v241\n"
    "v_accvgpr_write_b32 a158, v242\n"
    "v_accvgpr_read_b32 v243, a159\n"
    "v_mul_f32 v243, v243, v241\n"
    "v_accvgpr_write_b32 a159, v243\n"
    "v_accvgpr_read_b32 v242, a164\n"
    "v_mul_f32 v242, v242, v241\n"
    "v_accvgpr_write_b32 a164, v242\n"
    "v_accvgpr_read_b32 v243, a165\n"
    "v_mul_f32 v243, v243, v241\n"
    "v_accvgpr_write_b32 a165, v243\n"
    "v_accvgpr_read_b32 v242, a166\n"
    "v_mul_f32 v242, v242, v241\n"
    "v_accvgpr_write_b32 a166, v242\n"
    "v_accvgpr_read_b32 v243, a167\n"
    "v_mul_f32 v243, v243, v241\n"
    "v_accvgpr_write_b32 a167, v243\n"
    "v_accvgpr_read_b32 v242, a172\n"
    "v_mul_f32 v242, v242, v241\n"
    "v_accvgpr_write_b32 a172, v242\n"
    "v_accvgpr_read_b32 v243, a173\n"
    "v_mul_f32 v243, v243, v241\n"
    "v_accvgpr_write_b32 a173, v243\n"
    "v_accvgpr_read_b32 v242, a174\n"
    "v_mul_f32 v242, v242, v241\n"
    "v_accvgpr_write_b32 a174, v242\n"
    "v_accvgpr_read_b32 v243, a175\n"
    "v_mul_f32 v243, v243, v241\n"
    "v_accvgpr_write_b32 a175, v243\n"
    "v_accvgpr_read_b32 v242, a180\n"
    "v_mul_f32 v242, v242, v241\n"
    "v_accvgpr_write_b32 a180, v242\n"
    "v_accvgpr_read_b32 v243, a181\n"
    "v_mul_f32 v243, v243, v241\n"
    "v_accvgpr_write_b32 a181, v243\n"
    "v_accvgpr_read_b32 v242, a182\n"
    "v_mul_f32 v242, v242, v241\n"
    "v_accvgpr_write_b32 a182, v242\n"
    "v_accvgpr_read_b32 v243, a183\n"
    "v_mul_f32 v243, v243, v241\n"
    "v_accvgpr_write_b32 a183, v243\n"
    "v_accvgpr_read_b32 v242, a188\n"
    "v_mul_f32 v242, v242, v241\n"
    "v_accvgpr_write_b32 a188, v242\n"
    "v_accvgpr_read_b32 v243, a189\n"
    "v_mul_f32 v243, v243, v241\n"
    "v_accvgpr_write_b32 a189, v243\n"
    "v_accvgpr_read_b32 v242, a190\n"
    "v_mul_f32 v242, v242, v241\n"
    "v_accvgpr_write_b32 a190, v242\n"
    "v_accvgpr_read_b32 v243, a191\n"
    "v_mul_f32 v243, v243, v241\n"
    "v_accvgpr_write_b32 a191, v243\n"
    "v_accvgpr_read_b32 v242, a196\n"
    "v_mul_f32 v242, v242, v241\n"
    "v_accvgpr_write_b32 a196, v242\n"
    "v_accvgpr_read_b32 v243, a197\n"
    "v_mul_f32 v243, v243, v241\n"
    "v_accvgpr_write_b32 a197, v243\n"
    "v_accvgpr_read_b32 v242, a198\n"
    "v_mul_f32 v242, v242, v241\n"
    "v_accvgpr_write_b32 a198, v242\n"
    "v_accvgpr_read_b32 v243, a199\n"
    "v_mul_f32 v243, v243, v241\n"
    "v_accvgpr_write_b32 a199, v243\n"
    "v_accvgpr_read_b32 v242, a204\n"
    "v_mul_f32 v242, v242, v241\n"
    "v_accvgpr_write_b32 a204, v242\n"
    "v_accvgpr_read_b32 v243, a205\n"
    "v_mul_f32 v243, v243, v241\n"
    "v_accvgpr_write_b32 a205, v243\n"
    "v_accvgpr_read_b32 v242, a206\n"
    "v_mul_f32 v242, v242, v241\n"
    "v_accvgpr_write_b32 a206, v242\n"
    "v_accvgpr_read_b32 v243, a207\n"
    "v_mul_f32 v243, v243, v241\n"
    "v_accvgpr_write_b32 a207, v243\n"
    "v_accvgpr_read_b32 v242, a212\n"
    "v_mul_f32 v242, v242, v241\n"
    "v_accvgpr_write_b32 a212, v242\n"
    "v_accvgpr_read_b32 v243, a213\n"
    "v_mul_f32 v243, v243, v241\n"
    "v_accvgpr_write_b32 a213, v243\n"
    "v_accvgpr_read_b32 v242, a214\n"
    "v_mul_f32 v242, v242, v241\n"
    "v_accvgpr_write_b32 a214, v242\n"
    "v_accvgpr_read_b32 v243, a215\n"
    "v_mul_f32 v243, v243, v241\n"
    "v_accvgpr_write_b32 a215, v243\n"
    "v_accvgpr_read_b32 v242, a220\n"
    "v_mul_f32 v242, v242, v241\n"
    "v_accvgpr_write_b32 a220, v242\n"
    "v_accvgpr_read_b32 v243, a221\n"
    "v_mul_f32 v243, v243, v241\n"
    "v_accvgpr_write_b32 a221, v243\n"
    "v_accvgpr_read_b32 v242, a222\n"
    "v_mul_f32 v242, v242, v241\n"
    "v_accvgpr_write_b32 a222, v242\n"
    "v_accvgpr_read_b32 v243, a223\n"
    "v_mul_f32 v243, v243, v241\n"
    "v_accvgpr_write_b32 a223, v243\n"
    "v_accvgpr_read_b32 v242, a228\n"
    "v_mul_f32 v242, v242, v241\n"
    "v_accvgpr_write_b32 a228, v242\n"
    "v_accvgpr_read_b32 v243, a229\n"
    "v_mul_f32 v243, v243, v241\n"
    "v_accvgpr_write_b32 a229, v243\n"
    "v_accvgpr_read_b32 v242, a230\n"
    "v_mul_f32 v242, v242, v241\n"
    "v_accvgpr_write_b32 a230, v242\n"
    "v_accvgpr_read_b32 v243, a231\n"
    "v_mul_f32 v243, v243, v241\n"
    "v_accvgpr_write_b32 a231, v243\n"
    "v_accvgpr_read_b32 v242, a236\n"
    "v_mul_f32 v242, v242, v241\n"
    "v_accvgpr_write_b32 a236, v242\n"
    "v_accvgpr_read_b32 v243, a237\n"
    "v_mul_f32 v243, v243, v241\n"
    "v_accvgpr_write_b32 a237, v243\n"
    "v_accvgpr_read_b32 v242, a238\n"
    "v_mul_f32 v242, v242, v241\n"
    "v_accvgpr_write_b32 a238, v242\n"
    "v_accvgpr_read_b32 v243, a239\n"
    "v_mul_f32 v243, v243, v241\n"
    "v_accvgpr_write_b32 a239, v243\n"
    "v_accvgpr_read_b32 v242, a244\n"
    "v_mul_f32 v242, v242, v241\n"
    "v_accvgpr_write_b32 a244, v242\n"
    "v_accvgpr_read_b32 v243, a245\n"
    "v_mul_f32 v243, v243, v241\n"
    "v_accvgpr_write_b32 a245, v243\n"
    "v_accvgpr_read_b32 v242, a246\n"
    "v_mul_f32 v242, v242, v241\n"
    "v_accvgpr_write_b32 a246, v242\n"
    "v_accvgpr_read_b32 v243, a247\n"
    "v_mul_f32 v243, v243, v241\n"
    "v_accvgpr_write_b32 a247, v243\n"
    "v_accvgpr_read_b32 v242, a252\n"
    "v_mul_f32 v242, v242, v241\n"
    "v_accvgpr_write_b32 a252, v242\n"
    "v_accvgpr_read_b32 v243, a253\n"
    "v_mul_f32 v243, v243, v241\n"
    "v_accvgpr_write_b32 a253, v243\n"
    "v_accvgpr_read_b32 v242, a254\n"
    "v_mul_f32 v242, v242, v241\n"
    "v_accvgpr_write_b32 a254, v242\n"
    "v_accvgpr_read_b32 v243, a255\n"
    "v_mul_f32 v243, v243, v241\n"
    "v_accvgpr_write_b32 a255, v243\n"
    "v_exp_f32 v240, v140\n"
    "v_exp_f32 v241, v141\n"
    "v_exp_f32 v242, v142\n"
    "v_exp_f32 v243, v143\n"
    "v_add_f32 v247, v240, v241\n"
    "v_cvt_pk_bf16_f32 v188, v240, v241\n"
    "v_exp_f32 v240, v136\n"
    "v_exp_f32 v241, v137\n"
    "v_add_f32 v247, v247, v242\n"
    "v_add_f32 v247, v247, v243\n"
    "v_cvt_pk_bf16_f32 v189, v242, v243\n"
    "v_exp_f32 v242, v138\n"
    "v_exp_f32 v243, v139\n"
    "v_add_f32 v246, v240, v241\n"
    "v_cvt_pk_bf16_f32 v184, v240, v241\n"
    "v_exp_f32 v240, v132\n"
    "v_exp_f32 v241, v133\n"
    "v_add_f32 v246, v246, v242\n"
    "v_add_f32 v246, v246, v243\n"
    "v_cvt_pk_bf16_f32 v185, v242, v243\n"
    "v_exp_f32 v242, v134\n"
    "v_exp_f32 v243, v135\n"
    "v_add_f32 v245, v240, v241\n"
    "v_cvt_pk_bf16_f32 v180, v240, v241\n"
    "v_exp_f32 v240, v128\n"
    "v_exp_f32 v241, v129\n"
    "v_add_f32 v245, v245, v242\n"
    "v_add_f32 v245, v245, v243\n"
    "v_cvt_pk_bf16_f32 v181, v242, v243\n"
    "v_exp_f32 v242, v130\n"
    "v_exp_f32 v243, v131\n"
    "v_add_f32 v244, v240, v241\n"
    "v_cvt_pk_bf16_f32 v176, v240, v241\n"
    "v_exp_f32 v240, v156\n"
    "v_exp_f32 v241, v157\n"
    "v_add_f32 v244, v244, v242\n"
    "v_add_f32 v244, v244, v243\n"
    "v_cvt_pk_bf16_f32 v177, v242, v243\n"
    "v_exp_f32 v242, v158\n"
    "v_exp_f32 v243, v159\n"
    "v_add_f32 v247, v247, v240\n"
    "v_add_f32 v247, v247, v241\n"
    "v_cvt_pk_bf16_f32 v190, v240, v241\n"
    "v_exp_f32 v240, v152\n"
    "v_exp_f32 v241, v153\n"
    "v_add_f32 v247, v247, v242\n"
    "v_add_f32 v247, v247, v243\n"
    "v_cvt_pk_bf16_f32 v191, v242, v243\n"
    "v_exp_f32 v242, v154\n"
    "v_exp_f32 v243, v155\n"
    "v_add_f32 v246, v246, v240\n"
    "v_add_f32 v246, v246, v241\n"
    "v_cvt_pk_bf16_f32 v186, v240, v241\n"
    "v_exp_f32 v240, v148\n"
    "v_exp_f32 v241, v149\n"
    "v_add_f32 v246, v246, v242\n"
    "v_add_f32 v246, v246, v243\n"
    "v_cvt_pk_bf16_f32 v187, v242, v243\n"
    "v_exp_f32 v242, v150\n"
    "v_exp_f32 v243, v151\n"
    "v_add_f32 v245, v245, v240\n"
    "v_add_f32 v245, v245, v241\n"
    "v_cvt_pk_bf16_f32 v182, v240, v241\n"
    "v_exp_f32 v240, v144\n"
    "v_exp_f32 v241, v145\n"
    "v_add_f32 v245, v245, v242\n"
    "v_add_f32 v245, v245, v243\n"
    "v_cvt_pk_bf16_f32 v183, v242, v243\n"
    "v_exp_f32 v242, v146\n"
    "v_exp_f32 v243, v147\n"
    "v_add_f32 v244, v244, v240\n"
    "v_add_f32 v244, v244, v241\n"
    "v_cvt_pk_bf16_f32 v178, v240, v241\n"
    "v_add_f32 v244, v244, v242\n"
    "v_add_f32 v244, v244, v243\n"
    "v_cvt_pk_bf16_f32 v179, v242, v243\n"
    "s_nop 1\n"
    "s_cmp_lg_u32 s55, 0\n"
    "s_cbranch_scc1 LOOP_%=\n"
    "s_branch RET1_%=\n"
    :: "s"(__builtin_amdgcn_kernarg_segment_ptr()), "s"(blockIdx.x), "s"(w)
    : "v0","v1","v2","v3","v4","v5","v6","v7","v8","v9","v10","v11","v12","v13","v14","v15","v16","v17","v18","v19","v20","v21","v22","v23",
    "v24","v25","v26","v27","v28","v29","v30","v31","v32","v33","v34","v35","v36","v37","v38","v39","v40","v41","v42","v43","v44","v45","v46","v47",
    "v48","v49","v50","v51","v52","v53","v54","v55","v56","v57","v58","v59","v60","v61","v62","v63","v64","v65","v66","v67","v68","v69","v70","v71",
    "v72","v73","v74","v75","v76","v77","v78","v79","v80","v81","v82","v83","v84","v85","v86","v87","v88","v89","v90","v91","v92","v93","v94","v95",
    "v96","v97","v98","v99","v100","v101","v102","v103","v104","v105","v106","v107","v108","v109","v110","v111","v112","v113","v114","v115","v116","v117","v118","v119",
    "v120","v121","v122","v123","v124","v125","v126","v127","v128","v129","v130","v131","v132","v133","v134","v135","v136","v137","v138","v139","v140","v141","v142","v143",
    "v144","v145","v146","v147","v148","v149","v150","v151","v152","v153","v154","v155","v156","v157","v158","v159","v160","v161","v162","v163","v164","v165","v166","v167",
    "v168","v169","v170","v171","v172","v173","v174","v175","v176","v177","v178","v179","v180","v181","v182","v183","v184","v185","v186","v187","v188","v189","v190","v191",
    "v192","v193","v194","v195","v196","v197","v198","v199","v200","v201","v202","v203","v204","v205","v206","v207","v208","v209","v210","v211","v212","v213","v214","v215",
    "v216","v217","v218","v219","v220","v221","v222","v223","v224","v225","v226","v227","v228","v229","v230","v231","v232","v233","v234","v235","v236","v237","v238","v239",
    "v240","v241","v242","v243","v244","v245","v246","v247","v248","v249","v250","v251","v252","v253","v254","v255","a0","a1","a2","a3","a4","a5","a6","a7",
    "a8","a9","a10","a11","a12","a13","a14","a15","a16","a17","a18","a19","a20","a21","a22","a23","a24","a25","a26","a27","a28","a29","a30","a31",
    "a32","a33","a34","a35","a36","a37","a38","a39","a40","a41","a42","a43","a44","a45","a46","a47","a48","a49","a50","a51","a52","a53","a54","a55",
    "a56","a57","a58","a59","a60","a61","a62","a63","a64","a65","a66","a67","a68","a69","a70","a71","a72","a73","a74","a75","a76","a77","a78","a79",
    "a80","a81","a82","a83","a84","a85","a86","a87","a88","a89","a90","a91","a92","a93","a94","a95","a96","a97","a98","a99","a100","a101","a102","a103",
    "a104","a105","a106","a107","a108","a109","a110","a111","a112","a113","a114","a115","a116","a117","a118","a119","a120","a121","a122","a123","a124","a125","a126","a127",
    "a128","a129","a130","a131","a132","a133","a134","a135","a136","a137","a138","a139","a140","a141","a142","a143","a144","a145","a146","a147","a148","a149","a150","a151",
    "a152","a153","a154","a155","a156","a157","a158","a159","a160","a161","a162","a163","a164","a165","a166","a167","a168","a169","a170","a171","a172","a173","a174","a175",
    "a176","a177","a178","a179","a180","a181","a182","a183","a184","a185","a186","a187","a188","a189","a190","a191","a192","a193","a194","a195","a196","a197","a198","a199",
    "a200","a201","a202","a203","a204","a205","a206","a207","a208","a209","a210","a211","a212","a213","a214","a215","a216","a217","a218","a219","a220","a221","a222","a223",
    "a224","a225","a226","a227","a228","a229","a230","a231","a232","a233","a234","a235","a236","a237","a238","a239","a240","a241","a242","a243","a244","a245","a246","a247",
    "a248","a249","a250","a251","a252","a253","a254","a255","s4","s5","s6","s7","s8","s9","s10","s11","s12","s13","s14","s15","s16","s17","s18","s19",
    "s20","s21","s22","s23","s24","s25","s26","s27","s28","s29","s30","s40","s41","s42","s43","s44","s45","s46","s47","s48","s49","s50","s51","s52",
    "s53","s54","s55","s56","s57","s58","s59","s60","s61","s62","s63","s64","s65","s66","s67","s68","s69","s70","s71","s72","s73","s74","s75","s76",
    "s77","s78","s79","s80","s81","s82","s83","s84","s85","s86","s87","s88","s89","s90","s91","s92","s93","vcc","scc","memory");
}

extern "C" void kernel_launch(void* const* d_in, const int* in_sizes, int n_in, void* d_out, int out_size, void* d_ws, size_t ws_size, hipStream_t stream) {
  (void)hipFuncSetAttribute((const void*)k_block, hipFuncAttributeMaxDynamicSharedMemorySize, 163840);
  k_block<<<256, 256, 163840, stream>>>((const char*)d_in[0], (const char*)d_in[1], (const char*)d_in[2], (const char*)d_in[3], (const char*)d_in[4], (const char*)d_in[6],
                                        (const char*)d_in[7], (const char*)d_in[8], (const char*)d_in[9], (const char*)d_in[10], (char*)d_out, (char*)d_ws);
}
```
